```python
import torch

def _softmax(x, axis=-1):
    return torch.softmax(x, dim=axis)
def _gelu(x):
    return torch.nn.functional.gelu(x, approximate="tanh")
def _silu(x):
    return torch.nn.functional.silu(x)
def _sigmoid(x):
    return torch.sigmoid(x)
def _layernorm(x, axis=-1, eps=1e-5):
    m = x.mean(dim=axis, keepdim=True)
    v = x.var(dim=axis, keepdim=True, unbiased=False)
    return (x - m) / torch.sqrt(v + eps)
def _scatter_add(tgt, idx, src, axis=0):
    return tgt.clone().index_add_(axis, idx, src)
def _segment_sum(src, seg, n):
    out = torch.zeros((n,) + src.shape[1:], dtype=src.dtype)
    return out.index_add_(0, seg, src)
def _pad(x, pw):
    flat = tuple(v for pair in reversed(pw) for v in pair)
    return torch.nn.functional.pad(x, flat)

kernel_name = 'sequential_preLN__grouped_kv_mqa__moe_arch_mod__prompt'

def setup_inputs():

    _w = (lambda a: (lambda t: t.to(torch.bfloat16) if t.is_floating_point() else t)(torch.as_tensor(a)))
    def _A_setup():
        B, S, D, H = 1, 2048, 640, 32
        Dh = D // H
        return {
            "x": _w((lambda *s: torch.randn(*s, dtype=torch.bfloat16) * 0.05)(B, S, D)),
            "wq": _w((lambda *s: torch.randn(*s, dtype=torch.bfloat16) * 0.05)(D, H * Dh)),
            "wk": _w((lambda *s: torch.randn(*s, dtype=torch.bfloat16) * 0.05)(D, Dh)),
            "wv": _w((lambda *s: torch.randn(*s, dtype=torch.bfloat16) * 0.05)(D, Dh)),
            "wo": _w((lambda *s: torch.randn(*s, dtype=torch.bfloat16) * 0.05)(H * Dh, D)),
        }
    _A_in = _A_setup()

    def _B_setup():
        B, S, D, F, C = 1, 2048, 640, 1728, 1
        return {
            "x": _w((lambda *s: torch.randn(*s, dtype=torch.bfloat16) * 0.05)(B, S, D)),
            "w_r": _w((lambda *s: torch.randn(*s, dtype=torch.bfloat16) * 0.05)(D)),
            "w_gate": _w((lambda *s: torch.randn(*s, dtype=torch.bfloat16) * 0.05)(D, F)),
            "w_up": _w((lambda *s: torch.randn(*s, dtype=torch.bfloat16) * 0.05)(D, F)),
            "w_down": _w((lambda *s: torch.randn(*s, dtype=torch.bfloat16) * 0.05)(F, D)),
        }
    _B_in = _B_setup()

    return {
        "A_x": _A_in["x"],
        "A_wq": _A_in["wq"],
        "A_wk": _A_in["wk"],
        "A_wv": _A_in["wv"],
        "A_wo": _A_in["wo"],
        "B_x": _B_in["x"],
        "B_w_r": _B_in["w_r"],
        "B_w_gate": _B_in["w_gate"],
        "B_w_up": _B_in["w_up"],
        "B_w_down": _B_in["w_down"],
    }

def reference(A_x, A_wq, A_wk, A_wv, A_wo, B_x, B_w_r, B_w_gate, B_w_up, B_w_down):
    def _A_ref(x, wq, wk, wv, wo):
        B, S, D = x.shape
        H = 32
        Dh = D // H
        q = (lambda x, *d: x.permute(*d))((x @ wq).reshape(B, S, H, Dh), 0, 2, 1, 3)
        k = x @ wk
        v = x @ wv
        scores = torch.einsum("bhqd,bkd->bhqk", q, k) * (Dh ** -0.5)
        p = _softmax(scores, axis=-1)
        o = torch.einsum("bhqk,bkd->bhqd", p, v)
        return (lambda x, *d: x.permute(*d))(o, 0, 2, 1, 3).reshape(B, S, H * Dh) @ wo

    def _B_ref(x, w_r, w_gate, w_up, w_down):
        B, S, D = x.shape
        C = 1
        r = x @ w_r
        idx = torch.argsort(-r, axis=-1)[:, :C]
        idx_e = (lambda x, n, axis: torch.repeat_interleave(x, n, dim=axis))(idx.reshape(B, C, 1), D, axis=2)
        x_sel = (lambda x, i, axis: torch.take_along_dim(x, i, dim=axis))(x, idx_e, axis=1)
        g = _silu(x_sel @ w_gate)
        u = x_sel @ w_up
        y = (g * u) @ w_down
        r_sel = _sigmoid((lambda x, i, axis: torch.take_along_dim(x, i, dim=axis))(r, idx, axis=1))
        y = y * r_sel.reshape(B, C, 1)
        b_off = (torch.arange(B) * S).reshape(B, 1)
        flat_idx = (idx + b_off).reshape(B * C)
        zero = (x * 0.0).reshape(B * S, D)
        upd = _scatter_add(zero, flat_idx, y.reshape(B * C, D), axis=0)
        return x + upd.reshape(B, S, D)

    x = A_x
    h = x + _A_ref(_layernorm(x, axis=-1), A_wq, A_wk, A_wv, A_wo)
    h2 = h + _B_ref(_layernorm(h, axis=-1), B_w_r, B_w_gate, B_w_up, B_w_down)
    return h2
```

```cpp
#include <hip/hip_runtime.h>
#include <hip/hip_bf16.h>
#include <stdint.h>

typedef short bf16x8 __attribute__((ext_vector_type(8)));
typedef short bf16x4 __attribute__((ext_vector_type(4)));
typedef float f32x2 __attribute__((ext_vector_type(2)));
typedef float f32x4 __attribute__((ext_vector_type(4)));
typedef float f32x16 __attribute__((ext_vector_type(16)));
typedef unsigned u32x4 __attribute__((ext_vector_type(4)));
typedef unsigned u32x2 __attribute__((ext_vector_type(2)));
typedef unsigned short ushort_t;
#define LAS __attribute__((address_space(3)))
#define GAS __attribute__((address_space(1)))
#define DI __device__ __forceinline__

constexpr int NTOK = 2048, DM = 640, NHEAD = 32, DH = 20, FF = 1728;

constexpr size_t OFF_QF1  = 0;
constexpr size_t OFF_QF2  = OFF_QF1 + 2097152;
constexpr size_t OFF_KF1  = OFF_QF2 + 524288;
constexpr size_t OFF_KF2  = OFF_KF1 + 65536;
constexpr size_t OFF_VF   = OFF_KF2 + 16384;
constexpr size_t OFF_WOF  = OFF_VF + 131072;
constexpr size_t OFF_KEYG = OFF_WOF + 819200;
constexpr size_t OFF_ACTG = OFF_KEYG + 2048;
constexpr size_t OFF_CROW = OFF_KEYG + 9216;
constexpr size_t OFF_REPL = OFF_CROW + 1310720;
constexpr size_t OFF_CONST = OFF_REPL + 2048;
constexpr unsigned GTAG = 0x600DF00Du;
constexpr size_t OFF_DIAG = 64u << 20;

#define STAMP(kid, idx) do {} while (0)
#define STAMP_INIT() do {} while (0)
#define RSTAMP(idx) do {} while (0)
#define STAMP_FLUSH(kid) do {} while (0)
#define DIAG_BYTES 0
DI float bf2f(ushort_t b) { return __uint_as_float(((unsigned)b) << 16); }
typedef __bf16 bf16x2_t __attribute__((ext_vector_type(2)));
DI unsigned cvtpk(float lo, float hi) { return __builtin_bit_cast(unsigned, __builtin_convertvector((f32x2){lo, hi}, bf16x2_t)); }
DI unsigned cvtpk2(f32x2 v) { return __builtin_bit_cast(unsigned, __builtin_convertvector(v, bf16x2_t)); }
DI f32x2 unpk(unsigned p) { return (f32x2){__uint_as_float(p << 16), __uint_as_float(p & 0xffff0000u)}; }
DI float rbf(float x) { return __uint_as_float(cvtpk(x, x) << 16); }
DI ushort_t f2bf(float x) { return (ushort_t)(cvtpk(x, x) & 0xffffu); }
DI float dot2acc(unsigned a, unsigned b, float c) { asm("v_dot2c_f32_bf16 %0, %1, %2" : "+v"(c) : "v"(a), "v"(b)); return c; }
DI void glds16(const void* g, unsigned lds_base) {
    unsigned sv;
    asm volatile("s_mov_b32 %0, m0\n\ts_mov_b32 m0, %2\n\ts_nop 0\n\tglobal_load_lds_dwordx4 %1, off\n\ts_mov_b32 m0, %0"
                 : "=&s"(sv) : "v"(g), "s"(lds_base) : "memory");
}
DI void glds16s(const char* sbase, unsigned voff, unsigned lds_base) {
    unsigned sv;
    asm volatile("s_mov_b32 %0, m0\n\ts_mov_b32 m0, %3\n\ts_nop 0\n\tglobal_load_lds_dwordx4 %1, %2\n\ts_mov_b32 m0, %0"
                 : "=&s"(sv) : "v"(voff), "s"(sbase), "s"(lds_base) : "memory");
}
#define SBAR() __builtin_amdgcn_sched_barrier(0)
#define BAR_LDS() asm volatile("s_waitcnt lgkmcnt(0)\n\ts_barrier" ::: "memory")
#define MFMA32(a, b, c) __builtin_amdgcn_mfma_f32_32x32x16_bf16((a), (b), (c), 0, 0, 0)
#define MFMA32K8(a, b, c) __builtin_amdgcn_mfma_f32_32x32x8bf16_1k((a), (b), (c), 0, 0, 0)
#define MFMA16(a, b, c) __builtin_amdgcn_mfma_f32_16x16x32_bf16((a), (b), (c), 0, 0, 0)
typedef short v4i16_t __attribute__((ext_vector_type(4)));
DI bf16x4 trread(unsigned lds_addr) {
    return __builtin_amdgcn_ds_read_tr16_b64_v4i16((LAS v4i16_t*)(uintptr_t)lds_addr);
}
#define DPP_ADD(v, ctrl, rmask, bc) v += __builtin_bit_cast(float, __builtin_amdgcn_update_dpp(0, __builtin_bit_cast(int, v), ctrl, rmask, 0xf, bc))
DI float wave_sum(float v) {
    DPP_ADD(v, 0x111, 0xf, true); DPP_ADD(v, 0x112, 0xf, true); DPP_ADD(v, 0x114, 0xf, true); DPP_ADD(v, 0x118, 0xf, true);
    DPP_ADD(v, 0x142, 0xa, false); DPP_ADD(v, 0x143, 0xc, false);
    return __builtin_bit_cast(float, __builtin_amdgcn_readlane(__builtin_bit_cast(int, v), 63));
}
DI void wave_sum2(float& a, float& b) {
    DPP_ADD(a, 0x111, 0xf, true); DPP_ADD(b, 0x111, 0xf, true); DPP_ADD(a, 0x112, 0xf, true); DPP_ADD(b, 0x112, 0xf, true);
    DPP_ADD(a, 0x114, 0xf, true); DPP_ADD(b, 0x114, 0xf, true); DPP_ADD(a, 0x118, 0xf, true); DPP_ADD(b, 0x118, 0xf, true);
    DPP_ADD(a, 0x142, 0xa, false); DPP_ADD(b, 0x142, 0xa, false); DPP_ADD(a, 0x143, 0xc, false); DPP_ADD(b, 0x143, 0xc, false);
    a = __builtin_bit_cast(float, __builtin_amdgcn_readlane(__builtin_bit_cast(int, a), 63));
    b = __builtin_bit_cast(float, __builtin_amdgcn_readlane(__builtin_bit_cast(int, b), 63));
}
DI unsigned wave_umax(unsigned v) {
#define DPP_MAX(ctrl, rmask) v = max(v, (unsigned)__builtin_amdgcn_update_dpp(0, (int)v, ctrl, rmask, 0xf, true))
    DPP_MAX(0x111, 0xf); DPP_MAX(0x112, 0xf); DPP_MAX(0x114, 0xf); DPP_MAX(0x118, 0xf); DPP_MAX(0x142, 0xa); DPP_MAX(0x143, 0xc);
    return (unsigned)__builtin_amdgcn_readlane((int)v, 63);
}
DI float xor16(float v) { auto r = __builtin_amdgcn_permlane16_swap(__float_as_uint(v), __float_as_uint(v), false, false);
                          return __uint_as_float(r[0]) + __uint_as_float(r[1]); }
DI float xor32(float v) { auto r = __builtin_amdgcn_permlane32_swap(__float_as_uint(v), __float_as_uint(v), false, false);
                          return __uint_as_float(r[0]) + __uint_as_float(r[1]); }
DI float max32(float v) { auto r = __builtin_amdgcn_permlane32_swap(__float_as_uint(v), __float_as_uint(v), false, false);
                          return __builtin_fmaxf(__uint_as_float(r[0]), __uint_as_float(r[1])); }

constexpr int K1_XB = 0;
constexpr int K1_W = 40960;
constexpr int K1_KV = 143360;
constexpr int K1_WOT = 153664;
constexpr int K1_ST = 158784;
constexpr int K1_CS = K1_ST + 1024;
constexpr int K1_JUNK = K1_CS + 768;
constexpr int K1_END = K1_JUNK + 256;
constexpr int K1_LDS_BYTES = K1_END + DIAG_BYTES;
constexpr int K1_OQ = K1_XB;
constexpr int K1_OKV = K1_XB + 10240;

DI int swap23(int r) { return (r & ~12) | ((r & 4) << 1) | ((r & 8) >> 1); }
DI void glds5(const char* sbase, const unsigned (&v)[5], unsigned lds_base) {
    unsigned sv;
    asm volatile("s_mov_b32 %0, m0\n\ts_mov_b32 m0, %7\n\ts_nop 0\n\tglobal_load_lds_dwordx4 %1, %6\n\t"
                 "s_add_u32 m0, m0, 0x400\n\ts_nop 0\n\tglobal_load_lds_dwordx4 %2, %6\n\t"
                 "s_add_u32 m0, m0, 0x400\n\ts_nop 0\n\tglobal_load_lds_dwordx4 %3, %6\n\t"
                 "s_add_u32 m0, m0, 0x400\n\ts_nop 0\n\tglobal_load_lds_dwordx4 %4, %6\n\t"
                 "s_add_u32 m0, m0, 0x400\n\ts_nop 0\n\tglobal_load_lds_dwordx4 %5, %6\n\t"
                 "s_mov_b32 m0, %0"
                 : "=&s"(sv) : "v"(v[0]), "v"(v[1]), "v"(v[2]), "v"(v[3]), "v"(v[4]), "s"(sbase), "s"(lds_base) : "memory", "scc");
}
#define VMBAR(n) asm volatile("s_waitcnt vmcnt(" #n ") lgkmcnt(0)\n\ts_barrier" ::: "memory")

#define RAWBAR() asm volatile("s_barrier" ::: "memory")
#define K1_SYM "_Z6k1_qkvPKtS0_S0_S0_S0_S0_S0_S0_Pc"
#define K2_SYM "_Z7k2_attnPKtS0_S0_S0_S0_PcPt"
#define CODE_ADDR(dst, sym) asm volatile("s_getpc_b64 s[40:41]\n\ts_add_u32 s40, s40, " sym "@rel32@lo+4\n\ts_addc_u32 s41, s41, " sym "@rel32@hi+12\n\t" \
                                         "s_mov_b64 %0, s[40:41]" : "=s"(dst) :: "scc", "s40", "s41")
DI void k1_loader(char* smem, unsigned lds0, const ushort_t* x, const ushort_t* wq, const ushort_t* wk, const ushort_t* wv,
                  const ushort_t* wo, const ushort_t* w_gate, const ushort_t* w_up, const ushort_t* w_down,
                  char* ws, int d, int lane, int b, int panel, int j) {
#define DIAG_LDS K1_END
    const int rot = (5 * (panel & 3) + 2 * j + (b & 7)) % 20;
    unsigned xo[5], xo2[5], wo5[5];
#pragma unroll
    for (int k = 0; k < 5; ++k) {
        const int p = k * 64 + lane;
        const int r = p / 40, c = p - r * 40;
        const int cb = (((c >> 2) ^ ((r >> 1) & 1)) * 64) + (c & 3) * 16 + rot * 64;
        xo[k] = r * 1280 + (cb >= 1280 ? cb - 1280 : cb);
        const int cb2 = cb + 640;
        xo2[k] = r * 1280 + (cb2 >= 1280 ? cb2 - 1280 : cb2);
    }
    const char* xb = (const char*)(x + (size_t)(panel * 64 + d * 16) * DM);
    glds5(xb, xo, lds0 + K1_XB + d * 10240);
    glds5(xb + 10240, xo, lds0 + K1_XB + d * 10240 + 5120);
#pragma unroll
    for (int k = 0; k < 5; ++k) {
        const int p = k * 64 + lane;
        const int rw = p / 10, c8 = p - rw * 10;
        wo5[k] = swap23(rw) * 1280 + c8 * 16;
    }
    const char* wqb = (const char*)(wq + j * 80);
    const unsigned wl = lds0 + K1_W + d * 5120;
#define K1_WSLOT(s4) glds5(wqb + (size_t)((d + (s4) + rot) % 20) * 40960, wo5, wl + (s4) * 5120)
    K1_WSLOT(0);
    VMBAR(5);
    K1_WSLOT(4);
    VMBAR(5);
    glds5(xb, xo2, lds0 + K1_XB + d * 10240);
    glds5(xb + 10240, xo2, lds0 + K1_XB + d * 10240 + 5120);
    K1_WSLOT(8);
    VMBAR(15);
    K1_WSLOT(12);
    K1_WSLOT(16);
    VMBAR(10);
    {
        const int bo = min(b, 159);
        const char* wob = (const char*)(wo + (size_t)(bo >> 3) * 32 * DM + (bo & 7) * 80);
        const unsigned oa = (d == 0) ? wo5[0] : (d == 1) ? wo5[1] : (d == 2) ? wo5[2] : wo5[3];
        glds16s(wob, oa, lds0 + K1_WOT + d * 1024);
        glds16s(wob, wo5[4], lds0 + K1_WOT + 4096);
    }
    {
        const unsigned sl = (b >> 3) * 4 + d;
#pragma unroll
        for (int i = 0; i < 2; ++i) {
            const unsigned l = min(i * 64u + lane, 76u);
            const char* p;
            if (l < 50u) { const unsigned n = sl * 50 + l;
                p = (const char*)((n >= 3200u) ? w_up : w_gate) + (size_t)((n % 3200u) / 5u) * 3456 + min(((b & 7) * 432u & ~127u) + (n % 5u) * 128u, 3328u); }
            else { const unsigned n = sl * 27 + (l - 50u);
                p = (const char*)w_down + (size_t)(n >> 1) * 1280 + min(((b & 7) * 160u & ~127u) + (n & 1u) * 128u, 1152u); }
            unsigned sv;
            asm volatile("s_mov_b32 %0, m0\n\ts_mov_b32 m0, %2\n\ts_nop 0\n\tglobal_load_lds_dword %1, off\n\ts_mov_b32 m0, %0"
                         : "=&s"(sv) : "v"(p), "s"(lds0 + K1_JUNK) : "memory");
        }
        if (d == 1) {
            unsigned long long c0, c1;
            CODE_ADDR(c0, K2_SYM); CODE_ADDR(c1, "k2_code_end_mark");
            const unsigned off = min(((b >> 3) * 12u + min((unsigned)lane, 11u)) * 128u, (unsigned)(c1 - c0) & ~3u);
            unsigned sv;
            asm volatile("s_mov_b32 %0, m0\n\ts_mov_b32 m0, %3\n\ts_nop 0\n\tglobal_load_lds_dword %1, %2\n\ts_mov_b32 m0, %0"
                         : "=&s"(sv) : "v"(off), "s"(c0), "s"(lds0 + K1_JUNK) : "memory");
        }
        const u32x4 z = {0, 0, 0, 0};
        char* zp = ws + OFF_CROW + (size_t)b * 5120 + d * 1024 + lane * 16;
        asm volatile("global_store_dwordx4 %0, %1, off\n\ts_nop 1" :: "v"(zp), "v"(z) : "memory");
        if (d == 0) zp += 4096; else if (b < 9) zp = ws + OFF_KEYG + b * 1024 + lane * 16; else if (b < 11) zp = ws + OFF_REPL + (b - 9) * 1024 + lane * 16;
        asm volatile("global_store_dwordx4 %0, %1, off\n\ts_nop 1" :: "v"(zp), "v"(z) : "memory");
    }
    VMBAR(6);
    if (d == 2 && b == 0 && lane < 3) {
        const unsigned o2 = 0x3F803F80u;
        *(u32x4*)(ws + OFF_CONST + lane * 16) = (lane == 0) ? (u32x4){o2, o2, o2, o2} : (lane == 1) ? (u32x4){0, 0, 0, 0} : (u32x4){o2, 0x3F80u, 0, 0};
    }
    VMBAR(4);
    if (d == 3 && b < 160) {
        const int l15 = lane & 15, lg = lane >> 4;
        const unsigned a = lds0 + K1_WOT + (16 * (lg >> 1) + 4 * (lg & 1) + (l15 >> 2)) * 160 + (l15 & 3) * 8;
        char* dst = ws + OFF_WOF + (size_t)((b & 7) * 100 + (b >> 3) * 5) * 1024 + lane * 16;
#pragma unroll
        for (int t = 0; t < 5; ++t) {
            const bf16x4 lo = trread(a + t * 32), hi = trread(a + t * 32 + 1280);
            *(bf16x8*)(dst + t * 1024) = __builtin_shufflevector(lo, hi, 0, 1, 2, 3, 4, 5, 6, 7);
        }
    }
    if (d == 1 && j < 6) {
        const u32x4 kv = *(const u32x4*)(smem + K1_OKV + lane * 16);
        const int tile0 = panel * 2;
        if (j < 2) {
            *(u32x4*)(ws + OFF_KF1 + (size_t)((tile0 + (lane >> 5)) * 64 + 32 * j + (lane & 31)) * 16) = kv;
        } else if (j == 2) {
            if (lane < 32) *(u32x4*)(ws + OFF_KF2 + (size_t)tile0 * 256 + lane * 16) = kv;
        } else {
            const int dd = (j == 5) ? 12 + (lane & 7) : 8 * (j - 3) + (lane & 7);
            if (j < 5 || (lane & 7) >= 4)
                *(u32x4*)(ws + OFF_VF + (size_t)((tile0 * 2 + (lane >> 4)) * 64 + 32 * ((lane >> 3) & 1) + dd) * 16) = kv;
        }
    }
    asm volatile("s_waitcnt vmcnt(0)\n\t.globl k1_code_end_mark\n\t.protected k1_code_end_mark\nk1_code_end_mark:" ::: "memory");
#undef DIAG_LDS
}

template <int NH, int LOW>
DI void k1_body(char* smem, unsigned lds0, char* ws, int u, int lane, int j) {
#define DIAG_LDS K1_END
    const int l15 = lane & 15, lg = lane >> 4;
    const int m = u >> 1, rho = (m ^ NH) & 1;
    bf16x8 xf[10];
    const char* xr0 = smem + K1_XB + (m * 16 + l15) * 640 + lg * 16;
    const int e = rho ^ ((l15 >> 1) & 1);
    const char* xe = xr0 + e * 64;
    const char* xod = xr0 - e * 64;
    float s1 = 0.f, s2 = 0.f;
    const unsigned ones2 = 0x3F803F80u;
    f32x4 acc[3], cs[3];
#pragma unroll
    for (int t = 0; t < 3; ++t) { acc[t] = (f32x4){0, 0, 0, 0}; cs[t] = (f32x4){0, 0, 0, 0}; }
    bf16x8 onesf;
#pragma unroll
    for (int i = 0; i < 8; ++i) onesf[i] = (short)0x3F80;
    asm volatile("" : "+v"(onesf));
    const int rrow = 16 * (lg >> 1) + 4 * (lg & 1) + (l15 >> 2);
    const unsigned aW = lds0 + K1_W + rrow * 160 + (l15 & 3) * 8 + NH * 96;
    const unsigned aW4[4] = {aW + rho * 5120, aW - rho * 5120, aW + rho * 5120 + 51200, aW - rho * 5120 + 51200};
    const unsigned aK = lds0 + K1_KV + (8 * lg + (l15 >> 2)) * 16 + (l15 & 3) * 8;
    const unsigned aKe = aK + rho * 512, aKo = aK - rho * 512;
    bf16x8 wf[4][3];
#define K1_STATS() do { _Pragma("unroll") for (int i_ = 0; i_ < 10; i_ += 2) { const u32x4 v_ = __builtin_bit_cast(u32x4, xf[i_]); \
        _Pragma("unroll") for (int q_ = 0; q_ < 4; ++q_) { s1 = dot2acc(v_[q_], ones2, s1); s2 = dot2acc(v_[q_], v_[q_], s2); } } SBAR(); } while (0)
#define K1_READ(i) do { \
        const unsigned ab_ = aW4[((i) >= 10 ? 2 : 0) + ((i) & 1)] + ((i) % 10) * 5120; \
        _Pragma("unroll") for (int t_ = 0; t_ < (NH ? 2 : 3); ++t_) { \
            const bf16x4 lo_ = trread(ab_ + t_ * 32), hi_ = trread(ab_ + t_ * 32 + 1280); \
            wf[(i) & 3][t_] = __builtin_shufflevector(lo_, hi_, 0, 1, 2, 3, 4, 5, 6, 7); } \
        if (NH) { const unsigned ak_ = (((i) & 1) ? aKo : aKe) + (i) * 512; \
            const bf16x4 lo_ = trread(ak_), hi_ = trread(ak_ + 64); \
            wf[(i) & 3][2] = __builtin_shufflevector(lo_, hi_, 0, 1, 2, 3, 4, 5, 6, 7); } \
    } while (0)
#define K1_MMA(i, XF) do { \
        _Pragma("unroll") for (int t_ = 0; t_ < 3; ++t_) { \
            acc[t_] = MFMA16(wf[(i) & 3][t_], XF[(i) % 10], acc[t_]); \
            if ((i) >= 12 || (LOW && ((i) & 1) == 0)) cs[t_] = MFMA16(wf[(i) & 3][t_], onesf, cs[t_]); } \
    } while (0)
#define K1_CHUNK(i0, XA, XB) do { K1_READ(i0); K1_READ((i0) + 1); K1_READ((i0) + 2); K1_READ((i0) + 3); SBAR(); \
        K1_MMA(i0, XA); K1_MMA((i0) + 1, XA); K1_MMA((i0) + 2, XB); K1_MMA((i0) + 3, XB); SBAR(); } while (0)
    RAWBAR();
    STAMP(0, 11);
#pragma unroll
    for (int i = 0; i < 10; ++i) xf[i] = *(const bf16x8*)(((i & 1) ? xod : xe) + i * 64);
    VMBAR(0);
    K1_CHUNK(0, xf, xf);
    K1_STATS();
    STAMP(0, 1);
    RAWBAR();
    K1_CHUNK(4, xf, xf);
    RAWBAR();
    bf16x8 xg[10];
#pragma unroll
    for (int i = 0; i < 10; ++i) xg[i] = *(const bf16x8*)(((i & 1) ? xod : xe) + i * 64);
    K1_CHUNK(8, xf, xg);
#pragma unroll
    for (int i = 0; i < 10; ++i) xf[i] = xg[i];
    K1_STATS();
    s1 = xor32(xor16(s1));
    s2 = xor32(xor16(s2));
    if (lg == 0) *(f32x2*)(smem + K1_ST + (NH * 64 + m * 16 + l15) * 8) = (f32x2){s1, s2};
    if (LOW && l15 == 0) {
#pragma unroll
        for (int t = 0; t < 3; ++t) *(f32x4*)(smem + K1_CS + (m * 96 + NH * 48 + t * 16 + lg * 4) * 4) = cs[t];
    }
    STAMP(0, 2);
    BAR_LDS();
    const int tl = m * 16 + l15;
    K1_READ(12); K1_READ(13);
    const f32x2 sa = *(const f32x2*)(smem + K1_ST + tl * 8);
    const f32x2 sb = *(const f32x2*)(smem + K1_ST + (64 + tl) * 8);
#pragma unroll
    for (int t = 0; t < 3; ++t)
        cs[t] = *(const f32x4*)(smem + K1_CS + (NH * 48 + t * 16 + lg * 4) * 4) +
                *(const f32x4*)(smem + K1_CS + (96 + NH * 48 + t * 16 + lg * 4) * 4);
    SBAR();
#pragma unroll
    for (int i = 12; i < 20; ++i) { if (i + 2 < 20) K1_READ(i + 2); SBAR(); K1_MMA(i, xf); SBAR(); }
    const float mean = (sa[0] + sb[0]) * (1.0f / DM);
    const float var = fmaxf((sa[1] + sb[1]) * (1.0f / DM) - mean * mean, 0.f);
    const float mb = rbf(mean);
    const float sig = rbf(__builtin_amdgcn_sqrtf(rbf(rbf(var) + 1e-5f)));
    const float rs = __builtin_amdgcn_rcpf(sig);
    STAMP(0, 4);
    const float QSCALE = 0.22360679774997896f * 1.4426950408889634f;
    const int tok = tl & 7;
#pragma unroll
    for (int t = 0; t < 3; ++t) {
        const f32x4 csum = cs[t];
        float v[4];
#pragma unroll
        for (int r = 0; r < 4; ++r) v[r] = (acc[t][r] - mb * csum[r]) * rs;
        if (t == 2 && NH == 1) {
            if (lg < 2) {
                const int kk = tl & 31;
                if (j < 3) {
                    u32x2 pk; pk[0] = cvtpk(v[0], v[1]); pk[1] = cvtpk(v[2], v[3]);
                    if (j < 2) *(u32x2*)(smem + K1_OKV + tl * 16 + lg * 8) = pk;
                    else if (lg == 1) *(u32x2*)(smem + K1_OKV + tl * 8) = pk;
                } else {
                    const int s = kk >> 4, h = (kk >> 2) & 1, jj = ((kk >> 3) & 1) * 4 + (kk & 3);
                    char* d = smem + K1_OKV + (((((tl >> 5) * 2 + s) * 2 + h) * 8 + lg * 4) * 16) + jj * 2;
#pragma unroll
                    for (int r = 0; r < 4; ++r) *(ushort_t*)(d + r * 16) = f2bf(v[r]);
                }
            }
        } else {
            const int nl = (NH * 3 + t) * 16 + lg * 4;
            const int hl = nl / 20, dq = (nl - hl * 20) >> 2;
            u32x2 pk;
            pk[0] = cvtpk(v[0] * QSCALE, v[1] * QSCALE);
            pk[1] = cvtpk(v[2] * QSCALE, v[3] * QSCALE);
            const int off = (dq < 4) ? ((hl * 8 + tok + 32 * (dq >> 1)) * 16 + (dq & 1) * 8) : (1024 + (hl * 8 + tok) * 8);
            *(u32x2*)(smem + K1_OQ + (tl >> 3) * 1280 + off) = pk;
        }
    }
    BAR_LDS();
    STAMP(0, 5);
#undef DIAG_LDS
}

__global__ __launch_bounds__(768, 3) void k1_qkv(const ushort_t* __restrict__ x, const ushort_t* __restrict__ wq,
                                                 const ushort_t* __restrict__ wk, const ushort_t* __restrict__ wv,
                                                 const ushort_t* __restrict__ wo, const ushort_t* __restrict__ w_gate,
                                                 const ushort_t* __restrict__ w_up, const ushort_t* __restrict__ w_down,
                                                 char* __restrict__ ws) {
    extern __shared__ __attribute__((aligned(16))) char smem[];
    const unsigned lds0 = (unsigned)(uintptr_t)smem;
    const int lane = threadIdx.x & 63;
    const int u = __builtin_amdgcn_readfirstlane(threadIdx.x >> 6);
    const int b = blockIdx.x;
    const int panel = (b & 7) * 4 + ((b >> 3) >> 3), j = (b >> 3) & 7;
    if (u >= 8) { k1_loader(smem, lds0, x, wq, wk, wv, wo, w_gate, w_up, w_down, ws, u - 8, lane, b, panel, j); return; }
#define DIAG_LDS K1_END
    STAMP_INIT();
    STAMP(0, 0); RSTAMP(30);
    if (u >= 2 && u < 5) {
        unsigned long long c0, c1;
        CODE_ADDR(c0, K1_SYM); CODE_ADDR(c1, "k1_code_end_mark");
        const unsigned off = min(((u - 2) * 64u + lane) * 128u, (unsigned)(c1 - c0) & ~3u);
        unsigned sv;
        asm volatile("s_mov_b32 %0, m0\n\ts_mov_b32 m0, %3\n\ts_nop 0\n\tglobal_load_lds_dword %1, %2\n\ts_mov_b32 m0, %0"
                     : "=&s"(sv) : "v"(off), "s"(c0), "s"(lds0 + K1_JUNK) : "memory");
    }
    {
        const int rot = (5 * (panel & 3) + 2 * j + (b & 7)) % 20;
        const int jq = (j >= 3) ? ((j >= 6) ? 0 : j - 3) : j;
        const char* kvb = (const char*)((j >= 3 && j < 6) ? wv : wk) + (jq == 2 ? 24 : jq * 16);
        int row = u * 64 + lane + rot * 32;
        glds16s(kvb, (unsigned)((row >= 640 ? row - 640 : row) * 40), lds0 + K1_KV + u * 1024);
        if (u < 2) {
            row += 512;
            glds16s(kvb, (unsigned)((row >= 640 ? row - 640 : row) * 40), lds0 + K1_KV + (8 + u) * 1024);
        }
    }
    if (u < 4) {
        if (u & 1) k1_body<1, 1>(smem, lds0, ws, u, lane, j);
        else k1_body<0, 1>(smem, lds0, ws, u, lane, j);
    } else {
        if (u & 1) k1_body<1, 0>(smem, lds0, ws, u, lane, j);
        else k1_body<0, 0>(smem, lds0, ws, u, lane, j);
    }
    {
        const int wg2 = panel * 8 + u;
        const u32x4 v = *(const u32x4*)(smem + K1_OQ + u * 1280 + lane * 16);
        *(u32x4*)(ws + OFF_QF1 + (size_t)(wg2 * 8 + j) * 1024 + lane * 16) = v;
        if (lane < 16) {
            const u32x4 v2 = *(const u32x4*)(smem + K1_OQ + u * 1280 + 1024 + lane * 16);
            *(u32x4*)(ws + OFF_QF2 + (size_t)(wg2 * 8 + j) * 256 + lane * 16) = v2;
        }
    }
    STAMP(0, 6); RSTAMP(31);
    STAMP_FLUSH(0);
#undef DIAG_LDS
}

constexpr int K2_PBUF = 0;
constexpr int K2_QL = 49152;
constexpr int K2_OT = 61440;
constexpr int K2_MISC = 71680;
constexpr int K2_TAIL = K2_MISC + 1152;
constexpr int K2_GU = K2_TAIL;
constexpr int K2_ACT = K2_TAIL + 57344;
constexpr int K2_STG = K2_PBUF;
constexpr int K2_JUNK = K2_TAIL + 81920;
constexpr int K2_END = K2_JUNK + 1024;
constexpr int K2_LDS_BYTES = K2_END + DIAG_BYTES;

DI void split3(float v, ushort_t& a, ushort_t& b, ushort_t& c) {
    a = f2bf(v); const float r1 = v - bf2f(a);
    b = f2bf(r1); const float r2 = r1 - bf2f(b);
    c = f2bf(r2);
}
#define MX3(a, b, c) __builtin_fmaxf(__builtin_fmaxf((a), (b)), (c))
DI float max16(const f32x16& s) {
    float a = MX3(s[0], s[1], s[2]), b = MX3(s[3], s[4], s[5]);
    a = MX3(a, s[6], s[7]); b = MX3(b, s[8], s[9]);
    a = MX3(a, s[10], s[11]); b = MX3(b, s[12], s[13]);
    a = MX3(a, s[14], s[15]);
    return __builtin_fmaxf(a, b);
}

__global__ __launch_bounds__(512, 2) void k2_attn(const ushort_t* __restrict__ x, const ushort_t* __restrict__ w_r,
                                                   const ushort_t* __restrict__ w_gate, const ushort_t* __restrict__ w_up,
                                                   const ushort_t* __restrict__ w_down,
                                                   char* __restrict__ ws, ushort_t* __restrict__ out) {
    extern __shared__ __attribute__((aligned(16))) char smem[];
    const unsigned lds0 = (unsigned)(uintptr_t)smem;
    const int w_0 = __builtin_amdgcn_readfirstlane(threadIdx.x >> 6);
    const int wg_0 = (blockIdx.x & 7) * 32 + (blockIdx.x >> 3);
    const unsigned lane16 = (threadIdx.x & 63) * 16;
#define FRESH_IDS int w = w_0, wg = wg_0; asm volatile("" : "+s"(w), "+s"(wg));
#define DIAG_LDS K2_END
    STAMP_INIT();
    STAMP(1, 0); RSTAMP(30);
    bf16x8 q1w = *(const bf16x8*)(ws + OFF_QF1 + (size_t)(wg_0 * 8 + w_0) * 1024 + lane16);
    bf16x4 q2w = *(const bf16x4*)(ws + (lane16 < 512u ? (unsigned)OFF_QF2 + (unsigned)(wg_0 * 8 + w_0) * 256u + (lane16 >> 1) : (unsigned)OFF_CONST + 16u));
    bf16x8 k1w = *(const bf16x8*)(ws + OFF_KF1 + (size_t)w_0 * 8192 + lane16);
    bf16x4 k2w = *(const bf16x4*)(ws + (lane16 < 512u ? (unsigned)OFF_KF2 + (unsigned)w_0 * 2048u + (lane16 >> 1) : (unsigned)OFF_CONST + 32u));
    SBAR();
    const __amdgpu_buffer_rsrc_t wrs = __builtin_amdgcn_make_buffer_rsrc((void*)(ws + OFF_WOF + (size_t)w_0 * 102400), 0, 102400 + 5120, 0x00020000);
    const __amdgpu_buffer_rsrc_t wsr = __builtin_amdgcn_make_buffer_rsrc((void*)ws, 0, 8 << 20, 0x00020000);
    unsigned lo4[4] = {lane16, lane16 + 1024, lane16 + 2048, lane16 + 3072};
    asm volatile("" : "+v"(lo4[1]), "+v"(lo4[2]), "+v"(lo4[3]));
#define WOLOAD(off) __builtin_bit_cast(bf16x8, __builtin_amdgcn_raw_buffer_load_b128(wrs, lo4[((off) >> 10) & 3], (off) & ~4095, 0))
#define OPQ16() ({ unsigned l_ = lane16; asm volatile("" : "+v"(l_)); l_; })

    const int bi = blockIdx.x;
    const int gcb = (bi & 7) * 27 + min(bi >> 3, 26);
    const int dcb = (bi & 7) * 5 + max((bi >> 3) - 27, 0);
    u32x4 xa, wa4; unsigned xb1, wb1;
    f32x4 oacc[5];
    const f32x16 zero16 = {};

    for (int attempt = 0; attempt < 2; ++attempt) {
        FRESH_IDS
        bf16x8 kf1[8]; bf16x4 kf2[8]; bf16x8 vf[8][2];
        unsigned k2o, k2s, vo, vs;
        STAMP(1, 14);
        {
            const unsigned l16 = OPQ16();
            const unsigned lane = l16 >> 4, r32 = lane & 31, hi = lane >> 5;
            k2o = hi == 0 ? (unsigned)OFF_KF2 + (unsigned)(w * 256 + r32) * 8u : (unsigned)OFF_CONST + 32u;
            k2s = hi == 0 ? 256u : 0u;
            vo = r32 < DH ? (unsigned)OFF_VF + (unsigned)(w * 1024) * 16u + l16 : (unsigned)OFF_CONST + (r32 == DH ? 0u : 16u);
            vs = r32 < DH ? 1024u : 0u;
#define KLOAD(t) do { kf1[t] = __builtin_bit_cast(bf16x8, __builtin_amdgcn_raw_buffer_load_b128(wsr, lane16, (int)OFF_KF1 + w * 8192 + (t) * 1024, 0)); \
            kf2[t] = __builtin_bit_cast(bf16x4, __builtin_amdgcn_raw_buffer_load_b64(wsr, k2o + (t) * k2s, 0, 0)); } while (0)
#define VLOAD(t) do { vf[t][0] = __builtin_bit_cast(bf16x8, __builtin_amdgcn_raw_buffer_load_b128(wsr, vo + (2 * (t)) * vs, 0, 0)); \
            vf[t][1] = __builtin_bit_cast(bf16x8, __builtin_amdgcn_raw_buffer_load_b128(wsr, vo + (2 * (t) + 1) * vs, 0, 0)); } while (0)
            KLOAD(0); SBAR(); KLOAD(1); SBAR(); VLOAD(0); SBAR(); KLOAD(2); SBAR(); VLOAD(1); SBAR();
            if (attempt == 0) {
                f32x16 s = MFMA32(k1w, q1w, zero16);
                s = MFMA32K8(k2w, q2w, s);
                const float m = max32(max16(s));
                if (hi == 1) { ushort_t a, b2, c; split3(-m, a, b2, c); q2w = (bf16x4){(short)a, (short)b2, (short)c, 0}; }
                *(bf16x8*)(smem + K2_QL + w * 1536 + l16) = q1w;
                *(bf16x4*)(smem + K2_QL + w * 1536 + 1024 + (l16 >> 1)) = q2w;
            }
            if (w == 0) *(unsigned*)(smem + K2_MISC) = 0u;
        }
#pragma unroll
        for (int t = 0; t < 5; ++t) oacc[t] = (f32x4){0, 0, 0, 0};
        BAR_LDS();
        STAMP(1, 1); RSTAMP(28);

        unsigned long long badm = 0ull;
        f32x16 sc, o;
        u32x4 p0, p1;
        bf16x8 wa[5];
        bf16x8 q1 = *(const bf16x8*)(smem + K2_QL + OPQ16());
        bf16x4 q2 = *(const bf16x4*)(smem + K2_QL + 1024 + (OPQ16() >> 1));
        const char* otp; { const unsigned l_ = OPQ16(); otp = smem + K2_OT + ((l_ >> 1) & 0x180) + (l_ & 0x70); }
        char* pbv = smem + K2_PBUF + w * 3072 + OPQ16();
        const char* q2v = smem + K2_QL + 1024 + (OPQ16() >> 1);
        const char *s2pa, *s2pl; char* s2dst;
        {
            const unsigned l_ = OPQ16() >> 4;
            const unsigned it_ = w * 20 + min(l_ >> 1, 19u);
            s2pa = smem + K2_PBUF + (l_ & 1) * 12288 + it_ * 16;
            s2pl = smem + K2_PBUF + (l_ & 1) * 12288 + (160 + (it_ & 31)) * 16;
            const unsigned kr_ = 20 * ((it_ & 31) >> 3) + 4 * (it_ >> 5);
            s2dst = smem + K2_OT + ((kr_ >> 3) * 8 + (it_ & 7)) * 16 + (kr_ & 7) * 2;
        }
#define PHASE_LOCALS bf16x8 q1n; bf16x4 q2n; bf16x8 ob; f32x4 sa0, sa1, sa2, sa3; float sl0, sl1, sl2, sl3;
        sc = MFMA32(kf1[0], q1, zero16);
        sc = MFMA32K8(kf2[0], q2, sc);
#define PRIO(n) __builtin_amdgcn_s_setprio(n)
#define LSTAMP(a, b) STAMP(a, b)

#define EXP4(S, i) do { S[i] = __builtin_amdgcn_exp2f(S[i]); S[i + 1] = __builtin_amdgcn_exp2f(S[i + 1]); \
                        S[i + 2] = __builtin_amdgcn_exp2f(S[i + 2]); S[i + 3] = __builtin_amdgcn_exp2f(S[i + 3]); } while (0)
#define PACK4(P, S, i) do { P[0] = cvtpk(S[i], S[i + 1]); P[1] = cvtpk(S[i + 2], S[i + 3]); \
                            P[2] = cvtpk(S[i + 4], S[i + 5]); P[3] = cvtpk(S[i + 6], S[i + 7]); } while (0)
#define BF8(P) __builtin_bit_cast(bf16x8, P)
#define WP_ADDR(PAR) char* pb_ = pbv + (PAR) * 24576
#define WP_ST(i) *(f32x4*)(pb_ + (i) * 1024) = (f32x4){o[4 * (i)], o[4 * (i) + 1], o[4 * (i) + 2], o[4 * (i) + 3]}
#define WRITE_PARTIAL(PAR) do { WP_ADDR(PAR); asm volatile("" : "+v"(o)); WP_ST(0); WP_ST(1); WP_ST(2); } while (0)
#define STEP2A(PAR) do { const char* a_ = s2pa + (PAR) * 24576; const char* l_ = s2pl + (PAR) * 24576; \
            sa0 = *(const f32x4*)(a_); sa1 = *(const f32x4*)(a_ + 3072); sa2 = *(const f32x4*)(a_ + 6144); sa3 = *(const f32x4*)(a_ + 9216); \
            sl0 = *(const float*)(l_); sl1 = *(const float*)(l_ + 3072); sl2 = *(const float*)(l_ + 6144); sl3 = *(const float*)(l_ + 9216); \
        } while (0)
#define STEP2B() do { \
            sa0 = (sa0 + sa1) + (sa2 + sa3); sl0 += sl1; asm volatile("" : "+v"(sl0)); sl2 += sl3; asm volatile("" : "+v"(sl2)); sl0 += sl2; \
            DPP_ADD(sl0, 0xB1, 0xf, true); \
            badm |= __ballot(!(sl0 < 1.8446744e19f)); \
            sl0 = __builtin_amdgcn_rcpf(sl0); \
            asm volatile("" : "+v"(sa0), "+v"(sl0)); \
        } while (0)
#define STEP2C(G) do { \
            float a0_ = sa0[0], a1_ = sa0[1], a2_ = sa0[2], a3_ = sa0[3]; \
            DPP_ADD(a0_, 0xB1, 0xf, true); DPP_ADD(a1_, 0xB1, 0xf, true); DPP_ADD(a2_, 0xB1, 0xf, true); DPP_ADD(a3_, 0xB1, 0xf, true); \
            u32x2 pk_; pk_[0] = cvtpk(a0_ * sl0, a1_ * sl0); pk_[1] = cvtpk(a2_ * sl0, a3_ * sl0); \
            *(u32x2*)(s2dst + (G) * 1280) = pk_; \
        } while (0)
#define TAILDMA(i) glds16s(ws + OFF_WOF + (size_t)w * 102400 + 92160 + (i) * 1024, lane16, lds0 + K2_TAIL + w * 10240 + (i) * 1024)
#define WARMDMA(i) glds16s(ws + OFF_WOF + (size_t)((blockIdx.x >> 3) * 25 + min(w + 8 * (i), 24)) * 1024, lane16, lds0 + K2_JUNK)
#define GUDMA(i) do { const unsigned l_ = OPQ16() >> 4; const int in_ = min(w + 8 * (i), 19); \
            glds16((const char*)((l_ & 1) ? w_up : w_gate) + (size_t)(in_ * 32 + (l_ >> 1)) * 3456 + gcb * 16, lds0 + K2_GU + in_ * 1024); } while (0)
#define WARMXW() do { const unsigned l_ = OPQ16() >> 4; \
            const char* p_ = (l_ & 32) ? (const char*)w_r + min(l_ & 31u, 9u) * 128 : (const char*)(x + (size_t)(wg * 8 + w) * DM) + min(l_, 9u) * 128; \
            unsigned sv_; asm volatile("s_mov_b32 %0, m0\n\ts_mov_b32 m0, %2\n\ts_nop 0\n\tglobal_load_lds_dword %1, off\n\ts_mov_b32 m0, %0" \
                                       : "=&s"(sv_) : "v"(p_), "s"(lds0 + K2_JUNK) : "memory"); } while (0)
#define XWLOAD() do { const unsigned l_ = OPQ16(); const char* xrow_ = (const char*)(x + (size_t)(wg * 8 + w) * DM); \
            xa = *(const u32x4*)(xrow_ + l_); xb1 = *(const unsigned*)(xrow_ + 1024 + (l_ >> 2)); \
            wa4 = *(const u32x4*)((const char*)w_r + l_); wb1 = *(const unsigned*)((const char*)w_r + 1024 + (l_ >> 2)); } while (0)
#define OPJ(...) __VA_ARGS__
#define LOOPBAR() BAR_LDS()
#define S2(...) __VA_ARGS__

        {
            const int gn = 1;
            PHASE_LOCALS
            FRESH_IDS
            PRIO(1); EXP4(sc, 0); SBAR(); PRIO(2);
            EXP4(sc, 4); PACK4(p0, sc, 0); SBAR(); EXP4(sc, 8); EXP4(sc, 12); PACK4(p1, sc, 8); SBAR(); PRIO(3);
            sc = MFMA32(kf1[1], q1, zero16); SBAR();
            sc = MFMA32K8(kf2[1], q2, sc); SBAR();
            KLOAD(3);
            SBAR();
            o = MFMA32(vf[0][0], BF8(p0), zero16); SBAR();
            VLOAD(2);
            SBAR();
            o = MFMA32(vf[0][1], BF8(p1), o); SBAR();
            PRIO(1); EXP4(sc, 0); SBAR(); PRIO(2);
            EXP4(sc, 4); PACK4(p0, sc, 0); SBAR(); EXP4(sc, 8); EXP4(sc, 12); PACK4(p1, sc, 8); SBAR(); PRIO(3);
            sc = MFMA32(kf1[2], q1, zero16); SBAR();
            sc = MFMA32K8(kf2[2], q2, sc); SBAR();
            KLOAD(4);
            SBAR();
            o = MFMA32(vf[1][0], BF8(p0), o); SBAR();
            VLOAD(3);
            SBAR();
            o = MFMA32(vf[1][1], BF8(p1), o); SBAR();
            PRIO(1); EXP4(sc, 0); SBAR(); PRIO(2);
            EXP4(sc, 4); PACK4(p0, sc, 0); SBAR(); EXP4(sc, 8); EXP4(sc, 12); PACK4(p1, sc, 8); SBAR(); PRIO(3);
            sc = MFMA32(kf1[3], q1, zero16); SBAR();
            sc = MFMA32K8(kf2[3], q2, sc); SBAR();
            KLOAD(5);
            SBAR();
            o = MFMA32(vf[2][0], BF8(p0), o); SBAR();
            VLOAD(4);
            SBAR();
            o = MFMA32(vf[2][1], BF8(p1), o); SBAR();
            PRIO(1); EXP4(sc, 0); SBAR(); PRIO(2);
            EXP4(sc, 4); PACK4(p0, sc, 0); SBAR(); EXP4(sc, 8); EXP4(sc, 12); PACK4(p1, sc, 8); SBAR(); PRIO(3);
            sc = MFMA32(kf1[4], q1, zero16); SBAR();
            sc = MFMA32K8(kf2[4], q2, sc); SBAR();
            KLOAD(6);
            SBAR();
            o = MFMA32(vf[3][0], BF8(p0), o); SBAR();
            VLOAD(5);
            SBAR();
            o = MFMA32(vf[3][1], BF8(p1), o); SBAR();
            PRIO(1); EXP4(sc, 0); SBAR(); PRIO(2);
            EXP4(sc, 4); PACK4(p0, sc, 0); SBAR(); EXP4(sc, 8); EXP4(sc, 12); PACK4(p1, sc, 8); SBAR(); PRIO(3);
            sc = MFMA32(kf1[5], q1, zero16); SBAR();
            sc = MFMA32K8(kf2[5], q2, sc); SBAR();
            KLOAD(7);
            SBAR();
            o = MFMA32(vf[4][0], BF8(p0), o); SBAR();
            VLOAD(6);
            SBAR();
            o = MFMA32(vf[4][1], BF8(p1), o); SBAR();
            PRIO(1); EXP4(sc, 0); SBAR(); PRIO(2);
            EXP4(sc, 4); PACK4(p0, sc, 0); SBAR(); EXP4(sc, 8); EXP4(sc, 12); PACK4(p1, sc, 8); SBAR(); PRIO(3);
            sc = MFMA32(kf1[6], q1, zero16); SBAR();
            sc = MFMA32K8(kf2[6], q2, sc); SBAR();
            o = MFMA32(vf[5][0], BF8(p0), o); SBAR();
            VLOAD(7);
            SBAR();
            o = MFMA32(vf[5][1], BF8(p1), o); SBAR();
            PRIO(1); EXP4(sc, 0); SBAR(); PRIO(2);
            EXP4(sc, 4); PACK4(p0, sc, 0); SBAR(); EXP4(sc, 8); EXP4(sc, 12); PACK4(p1, sc, 8); SBAR(); PRIO(3);
            sc = MFMA32(kf1[7], q1, zero16); SBAR();
            sc = MFMA32K8(kf2[7], q2, sc); SBAR();
            o = MFMA32(vf[6][0], BF8(p0), o); SBAR();
            q1n = *(const bf16x8*)(smem + K2_QL + gn * 1536 + lane16); q2n = *(const bf16x4*)(q2v + gn * 1536);
            SBAR();
            o = MFMA32(vf[6][1], BF8(p1), o); SBAR();
            PRIO(1); EXP4(sc, 0); SBAR(); PRIO(2);
            EXP4(sc, 4); PACK4(p0, sc, 0); SBAR(); EXP4(sc, 8); EXP4(sc, 12); PACK4(p1, sc, 8); SBAR(); PRIO(3);
            sc = MFMA32(kf1[0], q1n, zero16); SBAR();
            sc = MFMA32K8(kf2[0], q2n, sc); SBAR();
            o = MFMA32(vf[7][0], BF8(p0), o); SBAR();
            o = MFMA32(vf[7][1], BF8(p1), o); SBAR();
            q1 = q1n; q2 = q2n;
            LSTAMP(1, 2);
        }
        {
            const int gn = 2;
            PHASE_LOCALS
            FRESH_IDS
            PRIO(1); EXP4(sc, 0); SBAR(); PRIO(2); WP_ADDR(0); WP_ST(0); SBAR();
            EXP4(sc, 4); SBAR(); WP_ST(1); SBAR(); PACK4(p0, sc, 0); EXP4(sc, 8); SBAR(); WP_ST(2); SBAR(); EXP4(sc, 12); PACK4(p1, sc, 8); SBAR(); PRIO(3);
            asm volatile("" : "+v"(p0), "+v"(p1)); if (w >= 4) LOOPBAR();
            sc = MFMA32(kf1[1], q1, zero16); SBAR();
            WARMXW();
            SBAR();
            sc = MFMA32K8(kf2[1], q2, sc); SBAR();
            OPJ(WARMDMA(0);)
            SBAR();
            o = MFMA32(vf[0][0], BF8(p0), zero16); SBAR();
            OPJ(WARMDMA(1);)
            SBAR();
            o = MFMA32(vf[0][1], BF8(p1), o); SBAR();
            asm volatile("" : "+v"(o), "+v"(sc)); if (w < 4) LOOPBAR();
            PRIO(1); EXP4(sc, 0); SBAR(); PRIO(2);
            EXP4(sc, 4); PACK4(p0, sc, 0); SBAR(); EXP4(sc, 8); EXP4(sc, 12); PACK4(p1, sc, 8); SBAR(); PRIO(3);
            sc = MFMA32(kf1[2], q1, zero16); SBAR();
            S2(STEP2A(0);)
            SBAR();
            sc = MFMA32K8(kf2[2], q2, sc); SBAR();
            OPJ(WARMDMA(2);)
            OPJ(WARMDMA(3);)
            SBAR();
            o = MFMA32(vf[1][0], BF8(p0), o); SBAR();
            OPJ(TAILDMA(0);)
            SBAR();
            o = MFMA32(vf[1][1], BF8(p1), o); SBAR();
            PRIO(1); EXP4(sc, 0); SBAR(); PRIO(2);
            EXP4(sc, 4); PACK4(p0, sc, 0); SBAR(); EXP4(sc, 8); EXP4(sc, 12); PACK4(p1, sc, 8); SBAR(); PRIO(3);
            sc = MFMA32(kf1[3], q1, zero16); SBAR();
            S2(STEP2B();)
            SBAR();
            sc = MFMA32K8(kf2[3], q2, sc); SBAR();
            OPJ(TAILDMA(1);)
            SBAR();
            o = MFMA32(vf[2][0], BF8(p0), o); SBAR();
            OPJ(TAILDMA(2);)
            SBAR();
            o = MFMA32(vf[2][1], BF8(p1), o); SBAR();
            PRIO(1); EXP4(sc, 0); SBAR(); PRIO(2);
            EXP4(sc, 4); PACK4(p0, sc, 0); SBAR(); EXP4(sc, 8); EXP4(sc, 12); PACK4(p1, sc, 8); SBAR(); PRIO(3);
            sc = MFMA32(kf1[4], q1, zero16); SBAR();
            S2(STEP2C(gn - 2);)
            SBAR();
            sc = MFMA32K8(kf2[4], q2, sc); SBAR();
            OPJ(TAILDMA(3);)
            SBAR();
            o = MFMA32(vf[3][0], BF8(p0), o); SBAR();
            OPJ(TAILDMA(4);)
            SBAR();
            o = MFMA32(vf[3][1], BF8(p1), o); SBAR();
            PRIO(1); EXP4(sc, 0); SBAR(); PRIO(2);
            EXP4(sc, 4); PACK4(p0, sc, 0); SBAR(); EXP4(sc, 8); EXP4(sc, 12); PACK4(p1, sc, 8); SBAR(); PRIO(3);
            sc = MFMA32(kf1[5], q1, zero16); SBAR();
            OPJ(TAILDMA(5);)
            SBAR();
            sc = MFMA32K8(kf2[5], q2, sc); SBAR();
            OPJ(TAILDMA(6);)
            SBAR();
            o = MFMA32(vf[4][0], BF8(p0), o); SBAR();
            OPJ(TAILDMA(7);)
            SBAR();
            o = MFMA32(vf[4][1], BF8(p1), o); SBAR();
            PRIO(1); EXP4(sc, 0); SBAR(); PRIO(2);
            EXP4(sc, 4); PACK4(p0, sc, 0); SBAR(); EXP4(sc, 8); EXP4(sc, 12); PACK4(p1, sc, 8); SBAR(); PRIO(3);
            sc = MFMA32(kf1[6], q1, zero16); SBAR();
            OPJ(TAILDMA(8);)
            SBAR();
            sc = MFMA32K8(kf2[6], q2, sc); SBAR();
            OPJ(TAILDMA(9);)
            SBAR();
            o = MFMA32(vf[5][0], BF8(p0), o); SBAR();
            OPJ(wa[0] = WOLOAD(0);)
            SBAR();
            o = MFMA32(vf[5][1], BF8(p1), o); SBAR();
            PRIO(1); EXP4(sc, 0); SBAR(); PRIO(2);
            EXP4(sc, 4); PACK4(p0, sc, 0); SBAR(); EXP4(sc, 8); EXP4(sc, 12); PACK4(p1, sc, 8); SBAR(); PRIO(3);
            sc = MFMA32(kf1[7], q1, zero16); SBAR();
            OPJ(wa[1] = WOLOAD(1024);)
            SBAR();
            sc = MFMA32K8(kf2[7], q2, sc); SBAR();
            OPJ(wa[2] = WOLOAD(2048);)
            SBAR();
            o = MFMA32(vf[6][0], BF8(p0), o); SBAR();
            q1n = *(const bf16x8*)(smem + K2_QL + gn * 1536 + lane16); q2n = *(const bf16x4*)(q2v + gn * 1536);
            SBAR();
            o = MFMA32(vf[6][1], BF8(p1), o); SBAR();
            PRIO(1); EXP4(sc, 0); SBAR(); PRIO(2);
            EXP4(sc, 4); PACK4(p0, sc, 0); SBAR(); EXP4(sc, 8); EXP4(sc, 12); PACK4(p1, sc, 8); SBAR(); PRIO(3);
            sc = MFMA32(kf1[0], q1n, zero16); SBAR();
            OPJ(wa[3] = WOLOAD(3072);)
            SBAR();
            sc = MFMA32K8(kf2[0], q2n, sc); SBAR();
            OPJ(wa[4] = WOLOAD(4096);)
            SBAR();
            o = MFMA32(vf[7][0], BF8(p0), o); SBAR();
            o = MFMA32(vf[7][1], BF8(p1), o); SBAR();
            q1 = q1n; q2 = q2n;
            LSTAMP(1, 3);
        }
        {
            const int gn = 3;
            PHASE_LOCALS
            FRESH_IDS
            PRIO(1); EXP4(sc, 0); SBAR(); PRIO(2); WP_ADDR(1); WP_ST(0); SBAR();
            EXP4(sc, 4); SBAR(); WP_ST(1); SBAR(); PACK4(p0, sc, 0); EXP4(sc, 8); SBAR(); WP_ST(2); SBAR(); EXP4(sc, 12); PACK4(p1, sc, 8); SBAR(); PRIO(3);
            asm volatile("" : "+v"(p0), "+v"(p1)); if (w >= 4) LOOPBAR();
            sc = MFMA32(kf1[1], q1, zero16); SBAR();
            sc = MFMA32K8(kf2[1], q2, sc); SBAR();
            o = MFMA32(vf[0][0], BF8(p0), zero16); SBAR();
            o = MFMA32(vf[0][1], BF8(p1), o); SBAR();
            asm volatile("" : "+v"(o), "+v"(sc)); if (w < 4) LOOPBAR();
            PRIO(1); EXP4(sc, 0); SBAR(); PRIO(2);
            EXP4(sc, 4); PACK4(p0, sc, 0); SBAR(); EXP4(sc, 8); EXP4(sc, 12); PACK4(p1, sc, 8); SBAR(); PRIO(3);
            sc = MFMA32(kf1[2], q1, zero16); SBAR();
            OPJ(ob = *(const bf16x8*)(otp + 0);)
            S2(STEP2A(1);)
            SBAR();
            sc = MFMA32K8(kf2[2], q2, sc); SBAR();
            o = MFMA32(vf[1][0], BF8(p0), o); SBAR();
            OPJ(oacc[0] = MFMA16(wa[0], ob, oacc[0]);)
            OPJ(wa[0] = WOLOAD(5120);)
            SBAR();
            o = MFMA32(vf[1][1], BF8(p1), o); SBAR();
            PRIO(1); EXP4(sc, 0); SBAR(); PRIO(2);
            EXP4(sc, 4); PACK4(p0, sc, 0); SBAR(); EXP4(sc, 8); EXP4(sc, 12); PACK4(p1, sc, 8); SBAR(); PRIO(3);
            sc = MFMA32(kf1[3], q1, zero16); SBAR();
            S2(STEP2B();)
            SBAR();
            sc = MFMA32K8(kf2[3], q2, sc); SBAR();
            o = MFMA32(vf[2][0], BF8(p0), o); SBAR();
            OPJ(oacc[1] = MFMA16(wa[1], ob, oacc[1]);)
            OPJ(wa[1] = WOLOAD(6144);)
            SBAR();
            o = MFMA32(vf[2][1], BF8(p1), o); SBAR();
            PRIO(1); EXP4(sc, 0); SBAR(); PRIO(2);
            EXP4(sc, 4); PACK4(p0, sc, 0); SBAR(); EXP4(sc, 8); EXP4(sc, 12); PACK4(p1, sc, 8); SBAR(); PRIO(3);
            sc = MFMA32(kf1[4], q1, zero16); SBAR();
            S2(STEP2C(gn - 2);)
            SBAR();
            sc = MFMA32K8(kf2[4], q2, sc); SBAR();
            o = MFMA32(vf[3][0], BF8(p0), o); SBAR();
            OPJ(oacc[2] = MFMA16(wa[2], ob, oacc[2]);)
            OPJ(wa[2] = WOLOAD(7168);)
            SBAR();
            o = MFMA32(vf[3][1], BF8(p1), o); SBAR();
            PRIO(1); EXP4(sc, 0); SBAR(); PRIO(2);
            EXP4(sc, 4); PACK4(p0, sc, 0); SBAR(); EXP4(sc, 8); EXP4(sc, 12); PACK4(p1, sc, 8); SBAR(); PRIO(3);
            sc = MFMA32(kf1[5], q1, zero16); SBAR();
            sc = MFMA32K8(kf2[5], q2, sc); SBAR();
            OPJ(oacc[3] = MFMA16(wa[3], ob, oacc[3]);)
            OPJ(wa[3] = WOLOAD(8192);)
            SBAR();
            o = MFMA32(vf[4][0], BF8(p0), o); SBAR();
            OPJ(oacc[4] = MFMA16(wa[4], ob, oacc[4]);)
            OPJ(wa[4] = WOLOAD(9216);)
            OPJ(ob = *(const bf16x8*)(otp + 512);)
            SBAR();
            o = MFMA32(vf[4][1], BF8(p1), o); SBAR();
            PRIO(1); EXP4(sc, 0); SBAR(); PRIO(2);
            EXP4(sc, 4); PACK4(p0, sc, 0); SBAR(); EXP4(sc, 8); EXP4(sc, 12); PACK4(p1, sc, 8); SBAR(); PRIO(3);
            sc = MFMA32(kf1[6], q1, zero16); SBAR();
            sc = MFMA32K8(kf2[6], q2, sc); SBAR();
            OPJ(oacc[0] = MFMA16(wa[0], ob, oacc[0]);)
            OPJ(wa[0] = WOLOAD(10240);)
            SBAR();
            o = MFMA32(vf[5][0], BF8(p0), o); SBAR();
            OPJ(oacc[1] = MFMA16(wa[1], ob, oacc[1]);)
            OPJ(wa[1] = WOLOAD(11264);)
            SBAR();
            o = MFMA32(vf[5][1], BF8(p1), o); SBAR();
            PRIO(1); EXP4(sc, 0); SBAR(); PRIO(2);
            EXP4(sc, 4); PACK4(p0, sc, 0); SBAR(); EXP4(sc, 8); EXP4(sc, 12); PACK4(p1, sc, 8); SBAR(); PRIO(3);
            sc = MFMA32(kf1[7], q1, zero16); SBAR();
            sc = MFMA32K8(kf2[7], q2, sc); SBAR();
            OPJ(oacc[2] = MFMA16(wa[2], ob, oacc[2]);)
            OPJ(wa[2] = WOLOAD(12288);)
            SBAR();
            o = MFMA32(vf[6][0], BF8(p0), o); SBAR();
            q1n = *(const bf16x8*)(smem + K2_QL + gn * 1536 + lane16); q2n = *(const bf16x4*)(q2v + gn * 1536);
            SBAR();
            o = MFMA32(vf[6][1], BF8(p1), o); SBAR();
            PRIO(1); EXP4(sc, 0); SBAR(); PRIO(2);
            EXP4(sc, 4); PACK4(p0, sc, 0); SBAR(); EXP4(sc, 8); EXP4(sc, 12); PACK4(p1, sc, 8); SBAR(); PRIO(3);
            sc = MFMA32(kf1[0], q1n, zero16); SBAR();
            sc = MFMA32K8(kf2[0], q2n, sc); SBAR();
            OPJ(oacc[3] = MFMA16(wa[3], ob, oacc[3]);)
            OPJ(wa[3] = WOLOAD(13312);)
            SBAR();
            o = MFMA32(vf[7][0], BF8(p0), o); SBAR();
            OPJ(oacc[4] = MFMA16(wa[4], ob, oacc[4]);)
            OPJ(wa[4] = WOLOAD(14336);)
            SBAR();
            o = MFMA32(vf[7][1], BF8(p1), o); SBAR();
            q1 = q1n; q2 = q2n;
            LSTAMP(1, 4);
        }
        {
            const int gn = 4;
            PHASE_LOCALS
            FRESH_IDS
            PRIO(1); EXP4(sc, 0); SBAR(); PRIO(2); WP_ADDR(0); WP_ST(0); SBAR();
            EXP4(sc, 4); SBAR(); WP_ST(1); SBAR(); PACK4(p0, sc, 0); EXP4(sc, 8); SBAR(); WP_ST(2); SBAR(); EXP4(sc, 12); PACK4(p1, sc, 8); SBAR(); PRIO(3);
            asm volatile("" : "+v"(p0), "+v"(p1)); if (w >= 4) LOOPBAR();
            sc = MFMA32(kf1[1], q1, zero16); SBAR();
            sc = MFMA32K8(kf2[1], q2, sc); SBAR();
            o = MFMA32(vf[0][0], BF8(p0), zero16); SBAR();
            o = MFMA32(vf[0][1], BF8(p1), o); SBAR();
            asm volatile("" : "+v"(o), "+v"(sc)); if (w < 4) LOOPBAR();
            PRIO(1); EXP4(sc, 0); SBAR(); PRIO(2);
            EXP4(sc, 4); PACK4(p0, sc, 0); SBAR(); EXP4(sc, 8); EXP4(sc, 12); PACK4(p1, sc, 8); SBAR(); PRIO(3);
            sc = MFMA32(kf1[2], q1, zero16); SBAR();
            OPJ(ob = *(const bf16x8*)(otp + 1024);)
            S2(STEP2A(0);)
            SBAR();
            sc = MFMA32K8(kf2[2], q2, sc); SBAR();
            o = MFMA32(vf[1][0], BF8(p0), o); SBAR();
            OPJ(oacc[0] = MFMA16(wa[0], ob, oacc[0]);)
            OPJ(wa[0] = WOLOAD(15360);)
            SBAR();
            o = MFMA32(vf[1][1], BF8(p1), o); SBAR();
            PRIO(1); EXP4(sc, 0); SBAR(); PRIO(2);
            EXP4(sc, 4); PACK4(p0, sc, 0); SBAR(); EXP4(sc, 8); EXP4(sc, 12); PACK4(p1, sc, 8); SBAR(); PRIO(3);
            sc = MFMA32(kf1[3], q1, zero16); SBAR();
            S2(STEP2B();)
            SBAR();
            sc = MFMA32K8(kf2[3], q2, sc); SBAR();
            OPJ(oacc[1] = MFMA16(wa[1], ob, oacc[1]);)
            OPJ(wa[1] = WOLOAD(16384);)
            SBAR();
            o = MFMA32(vf[2][0], BF8(p0), o); SBAR();
            OPJ(oacc[2] = MFMA16(wa[2], ob, oacc[2]);)
            OPJ(wa[2] = WOLOAD(17408);)
            SBAR();
            o = MFMA32(vf[2][1], BF8(p1), o); SBAR();
            PRIO(1); EXP4(sc, 0); SBAR(); PRIO(2);
            EXP4(sc, 4); PACK4(p0, sc, 0); SBAR(); EXP4(sc, 8); EXP4(sc, 12); PACK4(p1, sc, 8); SBAR(); PRIO(3);
            sc = MFMA32(kf1[4], q1, zero16); SBAR();
            S2(STEP2C(gn - 2);)
            SBAR();
            sc = MFMA32K8(kf2[4], q2, sc); SBAR();
            OPJ(oacc[3] = MFMA16(wa[3], ob, oacc[3]);)
            OPJ(wa[3] = WOLOAD(18432);)
            SBAR();
            o = MFMA32(vf[3][0], BF8(p0), o); SBAR();
            OPJ(oacc[4] = MFMA16(wa[4], ob, oacc[4]);)
            OPJ(wa[4] = WOLOAD(19456);)
            OPJ(ob = *(const bf16x8*)(otp + 1536);)
            SBAR();
            o = MFMA32(vf[3][1], BF8(p1), o); SBAR();
            PRIO(1); EXP4(sc, 0); SBAR(); PRIO(2);
            EXP4(sc, 4); PACK4(p0, sc, 0); SBAR(); EXP4(sc, 8); EXP4(sc, 12); PACK4(p1, sc, 8); SBAR(); PRIO(3);
            sc = MFMA32(kf1[5], q1, zero16); SBAR();
            OPJ(oacc[0] = MFMA16(wa[0], ob, oacc[0]);)
            OPJ(wa[0] = WOLOAD(20480);)
            SBAR();
            sc = MFMA32K8(kf2[5], q2, sc); SBAR();
            OPJ(oacc[1] = MFMA16(wa[1], ob, oacc[1]);)
            OPJ(wa[1] = WOLOAD(21504);)
            SBAR();
            o = MFMA32(vf[4][0], BF8(p0), o); SBAR();
            o = MFMA32(vf[4][1], BF8(p1), o); SBAR();
            PRIO(1); EXP4(sc, 0); SBAR(); PRIO(2);
            EXP4(sc, 4); PACK4(p0, sc, 0); SBAR(); EXP4(sc, 8); EXP4(sc, 12); PACK4(p1, sc, 8); SBAR(); PRIO(3);
            sc = MFMA32(kf1[6], q1, zero16); SBAR();
            OPJ(oacc[2] = MFMA16(wa[2], ob, oacc[2]);)
            OPJ(wa[2] = WOLOAD(22528);)
            SBAR();
            sc = MFMA32K8(kf2[6], q2, sc); SBAR();
            OPJ(oacc[3] = MFMA16(wa[3], ob, oacc[3]);)
            OPJ(wa[3] = WOLOAD(23552);)
            SBAR();
            o = MFMA32(vf[5][0], BF8(p0), o); SBAR();
            OPJ(oacc[4] = MFMA16(wa[4], ob, oacc[4]);)
            OPJ(wa[4] = WOLOAD(24576);)
            OPJ(ob = *(const bf16x8*)(otp + 2048);)
            SBAR();
            o = MFMA32(vf[5][1], BF8(p1), o); SBAR();
            PRIO(1); EXP4(sc, 0); SBAR(); PRIO(2);
            EXP4(sc, 4); PACK4(p0, sc, 0); SBAR(); EXP4(sc, 8); EXP4(sc, 12); PACK4(p1, sc, 8); SBAR(); PRIO(3);
            sc = MFMA32(kf1[7], q1, zero16); SBAR();
            OPJ(oacc[0] = MFMA16(wa[0], ob, oacc[0]);)
            OPJ(wa[0] = WOLOAD(25600);)
            SBAR();
            sc = MFMA32K8(kf2[7], q2, sc); SBAR();
            OPJ(oacc[1] = MFMA16(wa[1], ob, oacc[1]);)
            OPJ(wa[1] = WOLOAD(26624);)
            SBAR();
            o = MFMA32(vf[6][0], BF8(p0), o); SBAR();
            q1n = *(const bf16x8*)(smem + K2_QL + gn * 1536 + lane16); q2n = *(const bf16x4*)(q2v + gn * 1536);
            SBAR();
            o = MFMA32(vf[6][1], BF8(p1), o); SBAR();
            PRIO(1); EXP4(sc, 0); SBAR(); PRIO(2);
            EXP4(sc, 4); PACK4(p0, sc, 0); SBAR(); EXP4(sc, 8); EXP4(sc, 12); PACK4(p1, sc, 8); SBAR(); PRIO(3);
            sc = MFMA32(kf1[0], q1n, zero16); SBAR();
            OPJ(oacc[2] = MFMA16(wa[2], ob, oacc[2]);)
            OPJ(wa[2] = WOLOAD(27648);)
            SBAR();
            sc = MFMA32K8(kf2[0], q2n, sc); SBAR();
            OPJ(oacc[3] = MFMA16(wa[3], ob, oacc[3]);)
            OPJ(wa[3] = WOLOAD(28672);)
            SBAR();
            o = MFMA32(vf[7][0], BF8(p0), o); SBAR();
            OPJ(oacc[4] = MFMA16(wa[4], ob, oacc[4]);)
            OPJ(wa[4] = WOLOAD(29696);)
            SBAR();
            o = MFMA32(vf[7][1], BF8(p1), o); SBAR();
            q1 = q1n; q2 = q2n;
            LSTAMP(1, 5);
        }
        {
            const int gn = 5;
            PHASE_LOCALS
            FRESH_IDS
            PRIO(1); EXP4(sc, 0); SBAR(); PRIO(2); WP_ADDR(1); WP_ST(0); SBAR();
            EXP4(sc, 4); SBAR(); WP_ST(1); SBAR(); PACK4(p0, sc, 0); EXP4(sc, 8); SBAR(); WP_ST(2); SBAR(); EXP4(sc, 12); PACK4(p1, sc, 8); SBAR(); PRIO(3);
            asm volatile("" : "+v"(p0), "+v"(p1)); if (w >= 4) LOOPBAR();
            sc = MFMA32(kf1[1], q1, zero16); SBAR();
            sc = MFMA32K8(kf2[1], q2, sc); SBAR();
            o = MFMA32(vf[0][0], BF8(p0), zero16); SBAR();
            o = MFMA32(vf[0][1], BF8(p1), o); SBAR();
            asm volatile("" : "+v"(o), "+v"(sc)); if (w < 4) LOOPBAR();
            PRIO(1); EXP4(sc, 0); SBAR(); PRIO(2);
            EXP4(sc, 4); PACK4(p0, sc, 0); SBAR(); EXP4(sc, 8); EXP4(sc, 12); PACK4(p1, sc, 8); SBAR(); PRIO(3);
            sc = MFMA32(kf1[2], q1, zero16); SBAR();
            OPJ(ob = *(const bf16x8*)(otp + 2560);)
            S2(STEP2A(1);)
            SBAR();
            sc = MFMA32K8(kf2[2], q2, sc); SBAR();
            o = MFMA32(vf[1][0], BF8(p0), o); SBAR();
            OPJ(oacc[0] = MFMA16(wa[0], ob, oacc[0]);)
            OPJ(wa[0] = WOLOAD(30720);)
            SBAR();
            o = MFMA32(vf[1][1], BF8(p1), o); SBAR();
            PRIO(1); EXP4(sc, 0); SBAR(); PRIO(2);
            EXP4(sc, 4); PACK4(p0, sc, 0); SBAR(); EXP4(sc, 8); EXP4(sc, 12); PACK4(p1, sc, 8); SBAR(); PRIO(3);
            sc = MFMA32(kf1[3], q1, zero16); SBAR();
            S2(STEP2B();)
            SBAR();
            sc = MFMA32K8(kf2[3], q2, sc); SBAR();
            o = MFMA32(vf[2][0], BF8(p0), o); SBAR();
            OPJ(oacc[1] = MFMA16(wa[1], ob, oacc[1]);)
            OPJ(wa[1] = WOLOAD(31744);)
            SBAR();
            o = MFMA32(vf[2][1], BF8(p1), o); SBAR();
            PRIO(1); EXP4(sc, 0); SBAR(); PRIO(2);
            EXP4(sc, 4); PACK4(p0, sc, 0); SBAR(); EXP4(sc, 8); EXP4(sc, 12); PACK4(p1, sc, 8); SBAR(); PRIO(3);
            sc = MFMA32(kf1[4], q1, zero16); SBAR();
            S2(STEP2C(gn - 2);)
            SBAR();
            sc = MFMA32K8(kf2[4], q2, sc); SBAR();
            o = MFMA32(vf[3][0], BF8(p0), o); SBAR();
            OPJ(oacc[2] = MFMA16(wa[2], ob, oacc[2]);)
            OPJ(wa[2] = WOLOAD(32768);)
            SBAR();
            o = MFMA32(vf[3][1], BF8(p1), o); SBAR();
            PRIO(1); EXP4(sc, 0); SBAR(); PRIO(2);
            EXP4(sc, 4); PACK4(p0, sc, 0); SBAR(); EXP4(sc, 8); EXP4(sc, 12); PACK4(p1, sc, 8); SBAR(); PRIO(3);
            sc = MFMA32(kf1[5], q1, zero16); SBAR();
            sc = MFMA32K8(kf2[5], q2, sc); SBAR();
            OPJ(oacc[3] = MFMA16(wa[3], ob, oacc[3]);)
            OPJ(wa[3] = WOLOAD(33792);)
            SBAR();
            o = MFMA32(vf[4][0], BF8(p0), o); SBAR();
            OPJ(oacc[4] = MFMA16(wa[4], ob, oacc[4]);)
            OPJ(wa[4] = WOLOAD(34816);)
            OPJ(ob = *(const bf16x8*)(otp + 3072);)
            SBAR();
            o = MFMA32(vf[4][1], BF8(p1), o); SBAR();
            PRIO(1); EXP4(sc, 0); SBAR(); PRIO(2);
            EXP4(sc, 4); PACK4(p0, sc, 0); SBAR(); EXP4(sc, 8); EXP4(sc, 12); PACK4(p1, sc, 8); SBAR(); PRIO(3);
            sc = MFMA32(kf1[6], q1, zero16); SBAR();
            sc = MFMA32K8(kf2[6], q2, sc); SBAR();
            OPJ(oacc[0] = MFMA16(wa[0], ob, oacc[0]);)
            OPJ(wa[0] = WOLOAD(35840);)
            SBAR();
            o = MFMA32(vf[5][0], BF8(p0), o); SBAR();
            OPJ(oacc[1] = MFMA16(wa[1], ob, oacc[1]);)
            OPJ(wa[1] = WOLOAD(36864);)
            SBAR();
            o = MFMA32(vf[5][1], BF8(p1), o); SBAR();
            PRIO(1); EXP4(sc, 0); SBAR(); PRIO(2);
            EXP4(sc, 4); PACK4(p0, sc, 0); SBAR(); EXP4(sc, 8); EXP4(sc, 12); PACK4(p1, sc, 8); SBAR(); PRIO(3);
            sc = MFMA32(kf1[7], q1, zero16); SBAR();
            sc = MFMA32K8(kf2[7], q2, sc); SBAR();
            OPJ(oacc[2] = MFMA16(wa[2], ob, oacc[2]);)
            OPJ(wa[2] = WOLOAD(37888);)
            SBAR();
            o = MFMA32(vf[6][0], BF8(p0), o); SBAR();
            q1n = *(const bf16x8*)(smem + K2_QL + gn * 1536 + lane16); q2n = *(const bf16x4*)(q2v + gn * 1536);
            SBAR();
            o = MFMA32(vf[6][1], BF8(p1), o); SBAR();
            PRIO(1); EXP4(sc, 0); SBAR(); PRIO(2);
            EXP4(sc, 4); PACK4(p0, sc, 0); SBAR(); EXP4(sc, 8); EXP4(sc, 12); PACK4(p1, sc, 8); SBAR(); PRIO(3);
            sc = MFMA32(kf1[0], q1n, zero16); SBAR();
            sc = MFMA32K8(kf2[0], q2n, sc); SBAR();
            OPJ(oacc[3] = MFMA16(wa[3], ob, oacc[3]);)
            OPJ(wa[3] = WOLOAD(38912);)
            SBAR();
            o = MFMA32(vf[7][0], BF8(p0), o); SBAR();
            OPJ(oacc[4] = MFMA16(wa[4], ob, oacc[4]);)
            OPJ(wa[4] = WOLOAD(39936);)
            SBAR();
            o = MFMA32(vf[7][1], BF8(p1), o); SBAR();
            q1 = q1n; q2 = q2n;
            LSTAMP(1, 6);
        }
        {
            const int gn = 6;
            PHASE_LOCALS
            FRESH_IDS
            PRIO(1); EXP4(sc, 0); SBAR(); PRIO(2); WP_ADDR(0); WP_ST(0); SBAR();
            EXP4(sc, 4); SBAR(); WP_ST(1); SBAR(); PACK4(p0, sc, 0); EXP4(sc, 8); SBAR(); WP_ST(2); SBAR(); EXP4(sc, 12); PACK4(p1, sc, 8); SBAR(); PRIO(3);
            asm volatile("" : "+v"(p0), "+v"(p1)); if (w >= 4) LOOPBAR();
            sc = MFMA32(kf1[1], q1, zero16); SBAR();
            sc = MFMA32K8(kf2[1], q2, sc); SBAR();
            o = MFMA32(vf[0][0], BF8(p0), zero16); SBAR();
            o = MFMA32(vf[0][1], BF8(p1), o); SBAR();
            asm volatile("" : "+v"(o), "+v"(sc)); if (w < 4) LOOPBAR();
            PRIO(1); EXP4(sc, 0); SBAR(); PRIO(2);
            EXP4(sc, 4); PACK4(p0, sc, 0); SBAR(); EXP4(sc, 8); EXP4(sc, 12); PACK4(p1, sc, 8); SBAR(); PRIO(3);
            sc = MFMA32(kf1[2], q1, zero16); SBAR();
            OPJ(ob = *(const bf16x8*)(otp + 3584);)
            S2(STEP2A(0);)
            SBAR();
            sc = MFMA32K8(kf2[2], q2, sc); SBAR();
            o = MFMA32(vf[1][0], BF8(p0), o); SBAR();
            OPJ(oacc[0] = MFMA16(wa[0], ob, oacc[0]);)
            OPJ(wa[0] = WOLOAD(40960);)
            SBAR();
            o = MFMA32(vf[1][1], BF8(p1), o); SBAR();
            PRIO(1); EXP4(sc, 0); SBAR(); PRIO(2);
            EXP4(sc, 4); PACK4(p0, sc, 0); SBAR(); EXP4(sc, 8); EXP4(sc, 12); PACK4(p1, sc, 8); SBAR(); PRIO(3);
            sc = MFMA32(kf1[3], q1, zero16); SBAR();
            S2(STEP2B();)
            SBAR();
            sc = MFMA32K8(kf2[3], q2, sc); SBAR();
            OPJ(oacc[1] = MFMA16(wa[1], ob, oacc[1]);)
            OPJ(wa[1] = WOLOAD(41984);)
            SBAR();
            o = MFMA32(vf[2][0], BF8(p0), o); SBAR();
            OPJ(oacc[2] = MFMA16(wa[2], ob, oacc[2]);)
            OPJ(wa[2] = WOLOAD(43008);)
            SBAR();
            o = MFMA32(vf[2][1], BF8(p1), o); SBAR();
            PRIO(1); EXP4(sc, 0); SBAR(); PRIO(2);
            EXP4(sc, 4); PACK4(p0, sc, 0); SBAR(); EXP4(sc, 8); EXP4(sc, 12); PACK4(p1, sc, 8); SBAR(); PRIO(3);
            sc = MFMA32(kf1[4], q1, zero16); SBAR();
            S2(STEP2C(gn - 2);)
            SBAR();
            sc = MFMA32K8(kf2[4], q2, sc); SBAR();
            OPJ(oacc[3] = MFMA16(wa[3], ob, oacc[3]);)
            OPJ(wa[3] = WOLOAD(44032);)
            SBAR();
            o = MFMA32(vf[3][0], BF8(p0), o); SBAR();
            OPJ(oacc[4] = MFMA16(wa[4], ob, oacc[4]);)
            OPJ(wa[4] = WOLOAD(45056);)
            OPJ(ob = *(const bf16x8*)(otp + 4096);)
            SBAR();
            o = MFMA32(vf[3][1], BF8(p1), o); SBAR();
            PRIO(1); EXP4(sc, 0); SBAR(); PRIO(2);
            EXP4(sc, 4); PACK4(p0, sc, 0); SBAR(); EXP4(sc, 8); EXP4(sc, 12); PACK4(p1, sc, 8); SBAR(); PRIO(3);
            sc = MFMA32(kf1[5], q1, zero16); SBAR();
            OPJ(oacc[0] = MFMA16(wa[0], ob, oacc[0]);)
            OPJ(wa[0] = WOLOAD(46080);)
            SBAR();
            sc = MFMA32K8(kf2[5], q2, sc); SBAR();
            OPJ(oacc[1] = MFMA16(wa[1], ob, oacc[1]);)
            OPJ(wa[1] = WOLOAD(47104);)
            SBAR();
            o = MFMA32(vf[4][0], BF8(p0), o); SBAR();
            o = MFMA32(vf[4][1], BF8(p1), o); SBAR();
            PRIO(1); EXP4(sc, 0); SBAR(); PRIO(2);
            EXP4(sc, 4); PACK4(p0, sc, 0); SBAR(); EXP4(sc, 8); EXP4(sc, 12); PACK4(p1, sc, 8); SBAR(); PRIO(3);
            sc = MFMA32(kf1[6], q1, zero16); SBAR();
            OPJ(oacc[2] = MFMA16(wa[2], ob, oacc[2]);)
            OPJ(wa[2] = WOLOAD(48128);)
            SBAR();
            sc = MFMA32K8(kf2[6], q2, sc); SBAR();
            OPJ(oacc[3] = MFMA16(wa[3], ob, oacc[3]);)
            OPJ(wa[3] = WOLOAD(49152);)
            SBAR();
            o = MFMA32(vf[5][0], BF8(p0), o); SBAR();
            OPJ(oacc[4] = MFMA16(wa[4], ob, oacc[4]);)
            OPJ(wa[4] = WOLOAD(50176);)
            OPJ(ob = *(const bf16x8*)(otp + 4608);)
            SBAR();
            o = MFMA32(vf[5][1], BF8(p1), o); SBAR();
            PRIO(1); EXP4(sc, 0); SBAR(); PRIO(2);
            EXP4(sc, 4); PACK4(p0, sc, 0); SBAR(); EXP4(sc, 8); EXP4(sc, 12); PACK4(p1, sc, 8); SBAR(); PRIO(3);
            sc = MFMA32(kf1[7], q1, zero16); SBAR();
            OPJ(oacc[0] = MFMA16(wa[0], ob, oacc[0]);)
            OPJ(wa[0] = WOLOAD(51200);)
            SBAR();
            sc = MFMA32K8(kf2[7], q2, sc); SBAR();
            OPJ(oacc[1] = MFMA16(wa[1], ob, oacc[1]);)
            OPJ(wa[1] = WOLOAD(52224);)
            SBAR();
            o = MFMA32(vf[6][0], BF8(p0), o); SBAR();
            q1n = *(const bf16x8*)(smem + K2_QL + gn * 1536 + lane16); q2n = *(const bf16x4*)(q2v + gn * 1536);
            SBAR();
            o = MFMA32(vf[6][1], BF8(p1), o); SBAR();
            PRIO(1); EXP4(sc, 0); SBAR(); PRIO(2);
            EXP4(sc, 4); PACK4(p0, sc, 0); SBAR(); EXP4(sc, 8); EXP4(sc, 12); PACK4(p1, sc, 8); SBAR(); PRIO(3);
            sc = MFMA32(kf1[0], q1n, zero16); SBAR();
            OPJ(oacc[2] = MFMA16(wa[2], ob, oacc[2]);)
            OPJ(wa[2] = WOLOAD(53248);)
            SBAR();
            sc = MFMA32K8(kf2[0], q2n, sc); SBAR();
            OPJ(oacc[3] = MFMA16(wa[3], ob, oacc[3]);)
            OPJ(wa[3] = WOLOAD(54272);)
            SBAR();
            o = MFMA32(vf[7][0], BF8(p0), o); SBAR();
            OPJ(oacc[4] = MFMA16(wa[4], ob, oacc[4]);)
            OPJ(wa[4] = WOLOAD(55296);)
            SBAR();
            o = MFMA32(vf[7][1], BF8(p1), o); SBAR();
            q1 = q1n; q2 = q2n;
            LSTAMP(1, 7);
        }
        {
            const int gn = 7;
            PHASE_LOCALS
            FRESH_IDS
            PRIO(1); EXP4(sc, 0); SBAR(); PRIO(2); WP_ADDR(1); WP_ST(0); SBAR();
            EXP4(sc, 4); SBAR(); WP_ST(1); SBAR(); PACK4(p0, sc, 0); EXP4(sc, 8); SBAR(); WP_ST(2); SBAR(); EXP4(sc, 12); PACK4(p1, sc, 8); SBAR(); PRIO(3);
            asm volatile("" : "+v"(p0), "+v"(p1)); if (w >= 4) LOOPBAR();
            sc = MFMA32(kf1[1], q1, zero16); SBAR();
            sc = MFMA32K8(kf2[1], q2, sc); SBAR();
            o = MFMA32(vf[0][0], BF8(p0), zero16); SBAR();
            o = MFMA32(vf[0][1], BF8(p1), o); SBAR();
            asm volatile("" : "+v"(o), "+v"(sc)); if (w < 4) LOOPBAR();
            PRIO(1); EXP4(sc, 0); SBAR(); PRIO(2);
            EXP4(sc, 4); PACK4(p0, sc, 0); SBAR(); EXP4(sc, 8); EXP4(sc, 12); PACK4(p1, sc, 8); SBAR(); PRIO(3);
            sc = MFMA32(kf1[2], q1, zero16); SBAR();
            OPJ(ob = *(const bf16x8*)(otp + 5120);)
            S2(STEP2A(1);)
            SBAR();
            sc = MFMA32K8(kf2[2], q2, sc); SBAR();
            o = MFMA32(vf[1][0], BF8(p0), o); SBAR();
            OPJ(oacc[0] = MFMA16(wa[0], ob, oacc[0]);)
            OPJ(wa[0] = WOLOAD(56320);)
            SBAR();
            o = MFMA32(vf[1][1], BF8(p1), o); SBAR();
            PRIO(1); EXP4(sc, 0); SBAR(); PRIO(2);
            EXP4(sc, 4); PACK4(p0, sc, 0); SBAR(); EXP4(sc, 8); EXP4(sc, 12); PACK4(p1, sc, 8); SBAR(); PRIO(3);
            sc = MFMA32(kf1[3], q1, zero16); SBAR();
            S2(STEP2B();)
            SBAR();
            sc = MFMA32K8(kf2[3], q2, sc); SBAR();
            o = MFMA32(vf[2][0], BF8(p0), o); SBAR();
            OPJ(oacc[1] = MFMA16(wa[1], ob, oacc[1]);)
            OPJ(wa[1] = WOLOAD(57344);)
            SBAR();
            o = MFMA32(vf[2][1], BF8(p1), o); SBAR();
            PRIO(1); EXP4(sc, 0); SBAR(); PRIO(2);
            EXP4(sc, 4); PACK4(p0, sc, 0); SBAR(); EXP4(sc, 8); EXP4(sc, 12); PACK4(p1, sc, 8); SBAR(); PRIO(3);
            sc = MFMA32(kf1[4], q1, zero16); SBAR();
            S2(STEP2C(gn - 2);)
            SBAR();
            sc = MFMA32K8(kf2[4], q2, sc); SBAR();
            o = MFMA32(vf[3][0], BF8(p0), o); SBAR();
            OPJ(oacc[2] = MFMA16(wa[2], ob, oacc[2]);)
            OPJ(wa[2] = WOLOAD(58368);)
            SBAR();
            o = MFMA32(vf[3][1], BF8(p1), o); SBAR();
            PRIO(1); EXP4(sc, 0); SBAR(); PRIO(2);
            EXP4(sc, 4); PACK4(p0, sc, 0); SBAR(); EXP4(sc, 8); EXP4(sc, 12); PACK4(p1, sc, 8); SBAR(); PRIO(3);
            sc = MFMA32(kf1[5], q1, zero16); SBAR();
            sc = MFMA32K8(kf2[5], q2, sc); SBAR();
            OPJ(oacc[3] = MFMA16(wa[3], ob, oacc[3]);)
            OPJ(wa[3] = WOLOAD(59392);)
            SBAR();
            o = MFMA32(vf[4][0], BF8(p0), o); SBAR();
            OPJ(oacc[4] = MFMA16(wa[4], ob, oacc[4]);)
            OPJ(wa[4] = WOLOAD(60416);)
            OPJ(ob = *(const bf16x8*)(otp + 5632);)
            SBAR();
            o = MFMA32(vf[4][1], BF8(p1), o); SBAR();
            PRIO(1); EXP4(sc, 0); SBAR(); PRIO(2);
            EXP4(sc, 4); PACK4(p0, sc, 0); SBAR(); EXP4(sc, 8); EXP4(sc, 12); PACK4(p1, sc, 8); SBAR(); PRIO(3);
            sc = MFMA32(kf1[6], q1, zero16); SBAR();
            sc = MFMA32K8(kf2[6], q2, sc); SBAR();
            OPJ(oacc[0] = MFMA16(wa[0], ob, oacc[0]);)
            OPJ(wa[0] = WOLOAD(61440);)
            SBAR();
            o = MFMA32(vf[5][0], BF8(p0), o); SBAR();
            OPJ(oacc[1] = MFMA16(wa[1], ob, oacc[1]);)
            OPJ(wa[1] = WOLOAD(62464);)
            SBAR();
            o = MFMA32(vf[5][1], BF8(p1), o); SBAR();
            PRIO(1); EXP4(sc, 0); SBAR(); PRIO(2);
            EXP4(sc, 4); PACK4(p0, sc, 0); SBAR(); EXP4(sc, 8); EXP4(sc, 12); PACK4(p1, sc, 8); SBAR(); PRIO(3);
            sc = MFMA32(kf1[7], q1, zero16); SBAR();
            sc = MFMA32K8(kf2[7], q2, sc); SBAR();
            OPJ(oacc[2] = MFMA16(wa[2], ob, oacc[2]);)
            OPJ(wa[2] = WOLOAD(63488);)
            SBAR();
            o = MFMA32(vf[6][0], BF8(p0), o); SBAR();
            q1n = *(const bf16x8*)(smem + K2_QL + gn * 1536 + lane16); q2n = *(const bf16x4*)(q2v + gn * 1536);
            SBAR();
            o = MFMA32(vf[6][1], BF8(p1), o); SBAR();
            PRIO(1); EXP4(sc, 0); SBAR(); PRIO(2);
            EXP4(sc, 4); PACK4(p0, sc, 0); SBAR(); EXP4(sc, 8); EXP4(sc, 12); PACK4(p1, sc, 8); SBAR(); PRIO(3);
            sc = MFMA32(kf1[0], q1n, zero16); SBAR();
            sc = MFMA32K8(kf2[0], q2n, sc); SBAR();
            OPJ(oacc[3] = MFMA16(wa[3], ob, oacc[3]);)
            OPJ(wa[3] = WOLOAD(64512);)
            SBAR();
            o = MFMA32(vf[7][0], BF8(p0), o); SBAR();
            OPJ(oacc[4] = MFMA16(wa[4], ob, oacc[4]);)
            OPJ(wa[4] = WOLOAD(65536);)
            SBAR();
            o = MFMA32(vf[7][1], BF8(p1), o); SBAR();
            q1 = q1n; q2 = q2n;
            LSTAMP(1, 8);
        }

        bf16x8 wl[10];
        {
            const int gn = 8;
            PHASE_LOCALS
            PRIO(1); EXP4(sc, 0); SBAR(); PRIO(2); WP_ADDR(0); WP_ST(0); SBAR();
            EXP4(sc, 4); SBAR(); WP_ST(1); SBAR(); PACK4(p0, sc, 0); EXP4(sc, 8); SBAR(); WP_ST(2); SBAR(); EXP4(sc, 12); PACK4(p1, sc, 8); SBAR(); PRIO(3);
            asm volatile("" : "+v"(p0), "+v"(p1)); if (w >= 4) LOOPBAR();
            sc = MFMA32(kf1[1], q1, zero16); SBAR();
            sc = MFMA32K8(kf2[1], q2, sc); SBAR();
            o = MFMA32(vf[0][0], BF8(p0), zero16); SBAR();
            o = MFMA32(vf[0][1], BF8(p1), o); SBAR();
            asm volatile("" : "+v"(o), "+v"(sc)); if (w < 4) LOOPBAR();
            PRIO(1); EXP4(sc, 0); SBAR(); PRIO(2);
            EXP4(sc, 4); PACK4(p0, sc, 0); SBAR(); EXP4(sc, 8); EXP4(sc, 12); PACK4(p1, sc, 8); SBAR(); PRIO(3);
            sc = MFMA32(kf1[2], q1, zero16); SBAR();
            OPJ(ob = *(const bf16x8*)(otp + 6144);)
            S2(STEP2A(0);)
            SBAR();
            sc = MFMA32K8(kf2[2], q2, sc); SBAR();
            o = MFMA32(vf[1][0], BF8(p0), o); SBAR();
            OPJ(oacc[0] = MFMA16(wa[0], ob, oacc[0]);)
            OPJ(wa[0] = WOLOAD(66560);)
            SBAR();
            o = MFMA32(vf[1][1], BF8(p1), o); SBAR();
            PRIO(1); EXP4(sc, 0); SBAR(); PRIO(2);
            EXP4(sc, 4); PACK4(p0, sc, 0); SBAR(); EXP4(sc, 8); EXP4(sc, 12); PACK4(p1, sc, 8); SBAR(); PRIO(3);
            sc = MFMA32(kf1[3], q1, zero16); SBAR();
            S2(STEP2B();)
            wl[0] = WOLOAD(81920);
            SBAR();
            sc = MFMA32K8(kf2[3], q2, sc); SBAR();
            OPJ(oacc[1] = MFMA16(wa[1], ob, oacc[1]);)
            OPJ(wa[1] = WOLOAD(67584);)
            SBAR();
            o = MFMA32(vf[2][0], BF8(p0), o); SBAR();
            OPJ(oacc[2] = MFMA16(wa[2], ob, oacc[2]);)
            OPJ(wa[2] = WOLOAD(68608);)
            SBAR();
            o = MFMA32(vf[2][1], BF8(p1), o); SBAR();
            PRIO(1); EXP4(sc, 0); SBAR(); PRIO(2);
            EXP4(sc, 4); PACK4(p0, sc, 0); SBAR(); EXP4(sc, 8); EXP4(sc, 12); PACK4(p1, sc, 8); SBAR(); PRIO(3);
            sc = MFMA32(kf1[4], q1, zero16); SBAR();
            S2(STEP2C(gn - 2);)
            wl[1] = WOLOAD(82944);
            SBAR();
            sc = MFMA32K8(kf2[4], q2, sc); SBAR();
            OPJ(oacc[3] = MFMA16(wa[3], ob, oacc[3]);)
            OPJ(wa[3] = WOLOAD(69632);)
            SBAR();
            o = MFMA32(vf[3][0], BF8(p0), o); SBAR();
            OPJ(oacc[4] = MFMA16(wa[4], ob, oacc[4]);)
            OPJ(wa[4] = WOLOAD(70656);)
            OPJ(ob = *(const bf16x8*)(otp + 6656);)
            wl[2] = WOLOAD(83968);
            SBAR();
            o = MFMA32(vf[3][1], BF8(p1), o); SBAR();
            PRIO(1); EXP4(sc, 0); SBAR(); PRIO(2);
            EXP4(sc, 4); PACK4(p0, sc, 0); SBAR(); EXP4(sc, 8); EXP4(sc, 12); PACK4(p1, sc, 8); SBAR(); PRIO(3);
            sc = MFMA32(kf1[5], q1, zero16); SBAR();
            OPJ(oacc[0] = MFMA16(wa[0], ob, oacc[0]);)
            OPJ(wa[0] = WOLOAD(71680);)
            wl[3] = WOLOAD(84992);
            SBAR();
            sc = MFMA32K8(kf2[5], q2, sc); SBAR();
            OPJ(oacc[1] = MFMA16(wa[1], ob, oacc[1]);)
            OPJ(wa[1] = WOLOAD(72704);)
            SBAR();
            o = MFMA32(vf[4][0], BF8(p0), o); SBAR();
            wl[4] = WOLOAD(86016);
            SBAR();
            o = MFMA32(vf[4][1], BF8(p1), o); SBAR();
            PRIO(1); EXP4(sc, 0); SBAR(); PRIO(2);
            EXP4(sc, 4); PACK4(p0, sc, 0); SBAR(); EXP4(sc, 8); EXP4(sc, 12); PACK4(p1, sc, 8); SBAR(); PRIO(3);
            sc = MFMA32(kf1[6], q1, zero16); SBAR();
            OPJ(oacc[2] = MFMA16(wa[2], ob, oacc[2]);)
            OPJ(wa[2] = WOLOAD(73728);)
            wl[5] = WOLOAD(87040);
            SBAR();
            sc = MFMA32K8(kf2[6], q2, sc); SBAR();
            OPJ(oacc[3] = MFMA16(wa[3], ob, oacc[3]);)
            OPJ(wa[3] = WOLOAD(74752);)
            SBAR();
            o = MFMA32(vf[5][0], BF8(p0), o); SBAR();
            OPJ(oacc[4] = MFMA16(wa[4], ob, oacc[4]);)
            OPJ(wa[4] = WOLOAD(75776);)
            OPJ(ob = *(const bf16x8*)(otp + 7168);)
            wl[6] = WOLOAD(88064);
            SBAR();
            o = MFMA32(vf[5][1], BF8(p1), o); SBAR();
            PRIO(1); EXP4(sc, 0); SBAR(); PRIO(2);
            EXP4(sc, 4); PACK4(p0, sc, 0); SBAR(); EXP4(sc, 8); EXP4(sc, 12); PACK4(p1, sc, 8); SBAR(); PRIO(3);
            sc = MFMA32(kf1[7], q1, zero16); SBAR();
            OPJ(oacc[0] = MFMA16(wa[0], ob, oacc[0]);)
            OPJ(wa[0] = WOLOAD(76800);)
            wl[7] = WOLOAD(89088);
            SBAR();
            sc = MFMA32K8(kf2[7], q2, sc); SBAR();
            OPJ(oacc[1] = MFMA16(wa[1], ob, oacc[1]);)
            OPJ(wa[1] = WOLOAD(77824);)
            SBAR();
            o = MFMA32(vf[6][0], BF8(p0), o); SBAR();
            wl[8] = WOLOAD(90112);
            SBAR();
            o = MFMA32(vf[6][1], BF8(p1), o); SBAR();
            PRIO(1); EXP4(sc, 0); SBAR(); PRIO(2);
            EXP4(sc, 4); PACK4(p0, sc, 0); SBAR(); EXP4(sc, 8); EXP4(sc, 12); PACK4(p1, sc, 8); SBAR(); PRIO(3);
            OPJ(oacc[2] = MFMA16(wa[2], ob, oacc[2]);)
            OPJ(wa[2] = WOLOAD(78848);)
            wl[9] = WOLOAD(91136);
            SBAR();
            OPJ(oacc[3] = MFMA16(wa[3], ob, oacc[3]);)
            OPJ(wa[3] = WOLOAD(79872);)
            SBAR();
            o = MFMA32(vf[7][0], BF8(p0), o); SBAR();
            OPJ(oacc[4] = MFMA16(wa[4], ob, oacc[4]);)
            OPJ(wa[4] = WOLOAD(80896);)
            SBAR();
            o = MFMA32(vf[7][1], BF8(p1), o); SBAR();

            STAMP(1, 9); RSTAMP(29);
            otp += 7680;
        }
        PRIO(0);
        XWLOAD();
        WRITE_PARTIAL(1);
        bf16x8 wt[10];
        {
            const char* tp = smem + K2_TAIL + w * 10240 + OPQ16();
#pragma unroll
            for (int f = 0; f < 10; ++f) wt[f] = *(const bf16x8*)(tp + f * 1024);
        }
        BAR_LDS();
        STAMP(1, 12);
        {
            PHASE_LOCALS
            STEP2A(1); SBAR();
            const bf16x8 ob0 = *(const bf16x8*)(otp), ob1 = *(const bf16x8*)(otp + 512);
#pragma unroll
            for (int t = 0; t < 5; ++t) oacc[t] = MFMA16(wa[t], ob0, oacc[t]);
            STEP2B(); STEP2C(7);
            if (badm != 0ull) *(unsigned*)(smem + K2_MISC) = 1u;
#pragma unroll
            for (int t = 0; t < 5; ++t) oacc[t] = MFMA16(wl[t], ob1, oacc[t]);
            asm volatile("" : "+v"(oacc[0]), "+v"(oacc[1]), "+v"(oacc[2]), "+v"(oacc[3]), "+v"(oacc[4]));
        }
        BAR_LDS();
        STAMP(1, 13);
        {
            const bf16x8 ob2 = *(const bf16x8*)(otp + 1024), ob3 = *(const bf16x8*)(otp + 1536), ob4 = *(const bf16x8*)(otp + 2048);
#pragma unroll
            for (int t = 0; t < 5; ++t) oacc[t] = MFMA16(wl[5 + t], ob2, oacc[t]);
#pragma unroll
            for (int t = 0; t < 5; ++t) oacc[t] = MFMA16(wt[t], ob3, oacc[t]);
#pragma unroll
            for (int t = 0; t < 5; ++t) oacc[t] = MFMA16(wt[5 + t], ob4, oacc[t]);
        }
        const unsigned bad = *(const unsigned*)(smem + K2_MISC);
        asm volatile("" : "+v"(xa), "+v"(xb1), "+v"(wa4), "+v"(wb1));
        if (__builtin_amdgcn_readfirstlane(bad) == 0u) break;

        {
            FRESH_IDS
            const unsigned l16 = OPQ16();
            const unsigned lane = l16 >> 4, r32 = lane & 31, hi = lane >> 5;
            for (int g = 0; g < 8; ++g) {
                const bf16x8 qa = *(const bf16x8*)(smem + K2_QL + g * 1536 + l16);
                bf16x4 qb = *(const bf16x4*)(smem + K2_QL + g * 1536 + 1024 + (l16 >> 1));
                if (hi == 1) qb = (bf16x4){0, 0, 0, 0};
                float m = -INFINITY;
                for (int t = 0; t < 8; ++t) {
                    const bf16x8 ka = *(const bf16x8*)(ws + OFF_KF1 + (size_t)(w * 8 + t) * 1024 + l16);
                    const bf16x4 kb = *(const bf16x4*)(ws + OFF_KF2 + (size_t)((w * 8 + t) * 32 + r32) * 8);
                    f32x16 s = MFMA32(ka, qa, zero16);
                    s = MFMA32K8(kb, qb, s);
                    m = fmaxf(m, max16(s));
                }
                m = max32(m);
                if (hi == 0) *(float*)(smem + K2_PBUF + ((w * 8 + g) * 32 + r32) * 4) = m;
            }
            BAR_LDS();
            if (hi == 1) {
                float m = -INFINITY;
#pragma unroll
                for (int u = 0; u < 8; ++u) m = fmaxf(m, *(const float*)(smem + K2_PBUF + ((u * 8 + w) * 32 + r32) * 4));
                ushort_t a, b2, c; split3(-m, a, b2, c);
                *(bf16x4*)(smem + K2_QL + w * 1536 + 1024 + (l16 >> 1)) = (bf16x4){(short)a, (short)b2, (short)c, 0};
            }
        }
        asm volatile("" : "=v"(q1w), "=v"(q2w), "=v"(k1w), "=v"(k2w));
    }

    STAMP(1, 10); RSTAMP(24);
    FRESH_IDS
    const int lane = OPQ16() >> 4, l15 = lane & 15, lg = lane >> 4;
    if (l15 < 8) {
#pragma unroll
        for (int t = 0; t < 5; ++t) *(f32x4*)(smem + K2_STG + l15 * 2576 + (w * 80 + t * 16 + lg * 4) * 4) = oacc[t];
    }
    BAR_LDS();
    STAMP(1, 15);
    unsigned hp[5], hnp[5], op[5];
    f32x2 hf[5];
    f32x2 sum2 = {0.f, 0.f};
    {
        const f32x4 a0 = *(const f32x4*)(smem + K2_STG + w * 2576 + lane * 32), a1 = *(const f32x4*)(smem + K2_STG + w * 2576 + lane * 32 + 16);
        const f32x2 a2 = *(const f32x2*)(smem + K2_STG + w * 2576 + 2048 + lane * 8);
        const f32x2 av[5] = {{a0[0], a0[1]}, {a0[2], a0[3]}, {a1[0], a1[1]}, {a1[2], a1[3]}, a2};
#pragma unroll
        for (int i = 0; i < 5; ++i) {
            hp[i] = cvtpk2(unpk(i < 4 ? xa[i] : xb1) + unpk(cvtpk2(av[i])));
            hf[i] = unpk(hp[i]);
        }
    }
    const float c0 = __builtin_bit_cast(float, __builtin_amdgcn_readfirstlane(__builtin_bit_cast(int, hf[0][0])));
    f32x2 sq2 = {0.f, 0.f};
#pragma unroll
    for (int i = 0; i < 5; ++i) { const f32x2 d = hf[i] - c0; sum2 += d; sq2 += d * d; }
    float sd = sum2[0] + sum2[1], sq = sq2[0] + sq2[1];
    wave_sum2(sd, sq);
    sd *= (1.0f / DM);
    const float mean = c0 + sd;
    const float var = fmaxf(sq * (1.0f / DM) - sd * sd, 0.f);
    const float mb = rbf(mean);
    const float sig = rbf(__builtin_amdgcn_sqrtf(rbf(rbf(var) + 1e-5f)));
    const float rsig = __builtin_amdgcn_rcpf(sig);
    f32x2 rp2 = {0.f, 0.f};
    const int token = wg * 8 + w;
#pragma unroll
    for (int i = 0; i < 5; ++i) {
        hnp[i] = cvtpk2(unpk(cvtpk2(hf[i] - mb)) * rsig);
        const f32x2 hn2 = unpk(hnp[i]);
        rp2 += hn2 * unpk(i < 4 ? wa4[i] : wb1);
        op[i] = cvtpk2(hf[i] + hn2);
    }
    const float rsc = wave_sum(rp2[0] + rp2[1]);
    const unsigned rb = f2bf(rsc);
    const unsigned key = (((rb & 0x8000u) ? (~rb & 0xffffu) : (rb | 0x8000u)) << 16) | (unsigned)(2047 - token);
    if (lane == 0) *(unsigned*)(smem + K2_MISC + 16 + w * 4) = key;
    STAMP(1, 19);
    BAR_LDS();
    STAMP(1, 20);
    bool cand;
    {
        const u32x4 ka = *(const u32x4*)(smem + K2_MISC + 16), kb = *(const u32x4*)(smem + K2_MISC + 32);
        const unsigned best = max(max(max(ka[0], ka[1]), max(ka[2], ka[3])), max(max(kb[0], kb[1]), max(kb[2], kb[3])));
        cand = __builtin_amdgcn_readfirstlane(best) == __builtin_amdgcn_readfirstlane(key);
    }
    char* orow = (char*)(out + (size_t)token * DM);
#define OUT_ROW() do { *(u32x4*)(orow + lane * 16) = (u32x4){op[0], op[1], op[2], op[3]}; *(unsigned*)(orow + 1024 + lane * 4) = op[4]; } while (0)
    if (cand) {
        if (lane == 0) __hip_atomic_store((GAS unsigned long long*)(ws + OFF_KEYG) + wg, ((unsigned long long)GTAG << 32) | key, __ATOMIC_RELAXED, __HIP_MEMORY_SCOPE_AGENT);
        const int ro = (int)OFF_CROW + wg * 5120;
        __builtin_amdgcn_raw_buffer_store_b128((u32x4){hnp[0], GTAG, hnp[1], GTAG}, wsr, lane * 32, ro, 16);
        __builtin_amdgcn_raw_buffer_store_b128((u32x4){hnp[2], GTAG, hnp[3], GTAG}, wsr, lane * 32 + 16, ro, 16);
        __builtin_amdgcn_raw_buffer_store_b64((u32x2){hnp[4], GTAG}, wsr, lane * 8, ro + 2048, 16);
        __builtin_amdgcn_raw_buffer_store_b128((u32x4){hp[0], GTAG, hp[1], GTAG}, wsr, lane * 32, ro + 2560, 16);
        __builtin_amdgcn_raw_buffer_store_b128((u32x4){hp[2], GTAG, hp[3], GTAG}, wsr, lane * 32 + 16, ro + 2560, 16);
        __builtin_amdgcn_raw_buffer_store_b64((u32x2){hp[4], GTAG}, wsr, lane * 8, ro + 4608, 16);
    }
    if (!cand) OUT_ROW();
    if (w < 7) {
        if (bi < 216) {
            const char* gp = (const char*)((lane & 1) ? w_up : w_gate) + (size_t)(lane >> 1) * 3456 + gcb * 16;
#pragma unroll
            for (int i = 0; i < 3; ++i) { const int in = min(w + 7 * i, 19); glds16(gp + (size_t)in * 110592, lds0 + K2_GU + in * 1024); }
        } else {
            const unsigned vo2 = (lane >> 1) * 1280 + (lane & 1) * 16;
#pragma unroll
            for (int i = 0; i < 8; ++i) { const int in = min(w + 7 * i, 53); glds16s((const char*)w_down + dcb * 32 + (size_t)in * 40960, vo2, lds0 + K2_TAIL + in * 1024); }
        }
    }
    STAMP(1, 11); RSTAMP(27);
    const int rep = (int)OFF_REPL + (bi & 7) * 256;
    if (w == 7) {
        if (bi < 8) {
            unsigned best = 0;
            for (unsigned spins = 0; spins < (1u << 16); ++spins) {
                bool ok = true; best = 0;
#pragma unroll
                for (int i = 0; i < 2; ++i) {
                    const u32x4 g = __builtin_amdgcn_raw_buffer_load_b128(wsr, lane * 16, (int)OFF_KEYG + 1024 * i, 16);
                    ok = ok && g[1] == GTAG && g[3] == GTAG;
                    best = max(best, max(g[0], g[2]));
                }
                if (__all(ok)) break;
                __builtin_amdgcn_s_sleep(1);
            }
            best = wave_umax(best);
            if (lane == 0) { __builtin_amdgcn_raw_buffer_store_b64((u32x2){best, GTAG}, wsr, 0, rep, 16); *(unsigned*)(smem + K2_MISC + 64) = best; }
        } else {
            u32x2 gk;
            for (unsigned spins = 0; spins < (1u << 16); ++spins) {
                gk = __builtin_amdgcn_raw_buffer_load_b64(wsr, 0, rep, 16);
                if (__builtin_amdgcn_readfirstlane(gk[1]) == GTAG) break;
                __builtin_amdgcn_s_sleep(1);
            }
            if (lane == 0) *(unsigned*)(smem + K2_MISC + 64) = gk[0];
        }
    }
    asm volatile("s_waitcnt vmcnt(0) lgkmcnt(0)\n\ts_barrier" ::: "memory");
    const unsigned wkey = __builtin_amdgcn_readfirstlane(*(const unsigned*)(smem + K2_MISC + 64));
    const int wtok = 2047 - (int)(wkey & 0xffffu);
    const int wro = (int)OFF_CROW + (wtok >> 3) * 5120;
    u32x4 xb[3];
    if (bi < 216) {
        for (unsigned spins = 0; spins < (1u << 16); ++spins) {
            bool ok = true;
#pragma unroll
            for (int i = 0; i < 3; ++i) {
                const int so = wro + min(w + 8 * i, 19) * 128;
                const u32x4 g0 = __builtin_amdgcn_raw_buffer_load_b128(wsr, lg * 32, so, 16);
                const u32x4 g1 = __builtin_amdgcn_raw_buffer_load_b128(wsr, lg * 32 + 16, so, 16);
                ok = ok && g0[1] == GTAG && g0[3] == GTAG && g1[1] == GTAG && g1[3] == GTAG;
                xb[i] = (u32x4){g0[0], g0[2], g1[0], g1[2]};
            }
            if (__all(ok)) break;
            __builtin_amdgcn_s_sleep(1);
        }
    }
    STAMP(1, 16); RSTAMP(26);
    if (bi < 216) {
        f32x4 acc = {0, 0, 0, 0};
        const unsigned ga = lds0 + K2_GU + (8 * lg + (l15 >> 2)) * 32 + (l15 & 3) * 8;
#pragma unroll
        for (int i = 0; i < 3; ++i) {
            if (w + 8 * i < 20) {
                const bf16x4 lo = trread(ga + (w + 8 * i) * 1024), hi = trread(ga + (w + 8 * i) * 1024 + 128);
                acc = MFMA16(__builtin_shufflevector(lo, hi, 0, 1, 2, 3, 4, 5, 6, 7), __builtin_bit_cast(bf16x8, xb[i]), acc);
            }
        }
        if (l15 == 0) *(f32x4*)(smem + K2_MISC + 128 + w * 64 + lg * 16) = acc;
    }
    if (cand && token != wtok) OUT_ROW();
    if (bi < 216) {
        BAR_LDS();
        if (w == 0) {
            float g = 0.f, u = 0.f;
#pragma unroll
            for (int v = 0; v < 8; ++v) { g += *(const float*)(smem + K2_MISC + 128 + v * 64 + (lane & 7) * 4); u += *(const float*)(smem + K2_MISC + 128 + v * 64 + 32 + (lane & 7) * 4); }
            g = rbf(g); u = rbf(u);
            const float sg = rbf(g * __builtin_amdgcn_rcpf(1.0f + __expf(-g)));
            const unsigned a = f2bf(sg * u);
            const unsigned an = (unsigned)__builtin_amdgcn_update_dpp(0, (int)a, 0xB1, 0xf, 0xf, true);
            if (lane < 8 && (lane & 1) == 0)
                __hip_atomic_store((GAS unsigned long long*)(ws + OFF_ACTG) + gcb * 4 + (lane >> 1), ((unsigned long long)GTAG << 32) | a | (an << 16), __ATOMIC_RELAXED, __HIP_MEMORY_SCOPE_AGENT);
        }
    }
    STAMP(1, 17); RSTAMP(25);
    if (bi >= 216) {
        bf16x8 af[7];
        const unsigned da = lds0 + K2_TAIL + (8 * lg + (l15 >> 2)) * 32 + (l15 & 3) * 8;
#pragma unroll
        for (int i = 0; i < 7; ++i) {
            const int ks = min(w + 8 * i, 53);
            const bf16x4 lo = trread(da + ks * 1024), hi = trread(da + ks * 1024 + 128);
            af[i] = __builtin_shufflevector(lo, hi, 0, 1, 2, 3, 4, 5, 6, 7);
        }
        unsigned hh = 0, hnn = 0;
        if (w == 1) {
            u32x4 g[7];
            for (unsigned spins = 0; spins < (1u << 16); ++spins) {
                bool ok = true;
#pragma unroll
                for (int i = 0; i < 7; ++i) {
                    g[i] = __builtin_amdgcn_raw_buffer_load_b128(wsr, min(lane * 16 + i * 1024, 6896), (int)OFF_ACTG, 16);
                    ok = ok && g[i][1] == GTAG && g[i][3] == GTAG;
                }
                if (__all(ok)) break;
                __builtin_amdgcn_s_sleep(1);
            }
#pragma unroll
            for (int i = 0; i < 7; ++i) *(u32x2*)(smem + K2_ACT + min(lane * 8 + i * 512, 3448)) = (u32x2){g[i][0], g[i][2]};
        }
        if (w == 0) {
            for (unsigned spins = 0; spins < (1u << 16); ++spins) {
                const u32x2 g0 = __builtin_amdgcn_raw_buffer_load_b64(wsr, (lane & 7) * 8, wro + dcb * 64, 16);
                const u32x2 g1 = __builtin_amdgcn_raw_buffer_load_b64(wsr, (lane & 7) * 8, wro + 2560 + dcb * 64, 16);
                hnn = g0[0]; hh = g1[0];
                if (__all(g0[1] == GTAG && g1[1] == GTAG)) break;
                __builtin_amdgcn_s_sleep(1);
            }
        }
        asm volatile("s_waitcnt vmcnt(0) lgkmcnt(0)\n\ts_barrier" ::: "memory");
        RSTAMP(23);
        f32x4 acc = {0, 0, 0, 0};
#pragma unroll
        for (int i = 0; i < 7; ++i) {
            if (w + 8 * i < 54) {
                const bf16x8 ab = *(const bf16x8*)(smem + K2_ACT + (w + 8 * i) * 64 + lg * 16);
                acc = MFMA16(af[i], ab, acc);
            }
        }
        if (l15 == 0) *(f32x4*)(smem + K2_MISC + 128 + w * 64 + lg * 16) = acc;
        BAR_LDS();
        if (w == 0 && lane < 8) {
            f32x2 y = {0.f, 0.f};
#pragma unroll
            for (int v = 0; v < 8; ++v) y += *(const f32x2*)(smem + K2_MISC + 128 + v * 64 + lane * 8);
            const unsigned ord = wkey >> 16;
            const float r = bf2f((ushort_t)((ord & 0x8000u) ? (ord & 0x7fffu) : (~ord & 0xffffu)));
            const float sg = rbf(__builtin_amdgcn_rcpf(1.0f + __expf(-r)));
            const f32x2 yy = unpk(cvtpk2(unpk(cvtpk2(y)) * sg));
            *(unsigned*)(out + (size_t)wtok * DM + dcb * 16 + lane * 2) = cvtpk2(unpk(hh) + unpk(cvtpk2(unpk(hnn) + yy)));
        }
    }
    asm volatile(".globl k2_code_end_mark\n\t.protected k2_code_end_mark\nk2_code_end_mark:");
    STAMP(1, 18); RSTAMP(31);
    STAMP_FLUSH(1);
#undef DIAG_LDS
}

extern "C" void kernel_launch(void* const* d_in, const int* in_sizes, int n_in,
                              void* d_out, int out_size, void* d_ws, size_t ws_size,
                              hipStream_t stream) {
    const ushort_t* x = (const ushort_t*)d_in[0];
    const ushort_t* wq = (const ushort_t*)d_in[1];
    const ushort_t* wk = (const ushort_t*)d_in[2];
    const ushort_t* wv = (const ushort_t*)d_in[3];
    const ushort_t* wo = (const ushort_t*)d_in[4];
    const ushort_t* w_r = (const ushort_t*)d_in[6];
    const ushort_t* w_gate = (const ushort_t*)d_in[7];
    const ushort_t* w_up = (const ushort_t*)d_in[8];
    const ushort_t* w_down = (const ushort_t*)d_in[9];
    char* ws = (char*)d_ws;
    ushort_t* out = (ushort_t*)d_out;
    (void)hipFuncSetAttribute((const void*)k1_qkv, hipFuncAttributeMaxDynamicSharedMemorySize, K1_LDS_BYTES);
    (void)hipFuncSetAttribute((const void*)k2_attn, hipFuncAttributeMaxDynamicSharedMemorySize, K2_LDS_BYTES);
    hipLaunchKernelGGL(k1_qkv, dim3(256), dim3(768), K1_LDS_BYTES, stream, x, wq, wk, wv, wo, w_gate, w_up, w_down, ws);
    hipLaunchKernelGGL(k2_attn, dim3(256), dim3(512), K2_LDS_BYTES, stream, x, w_r, w_gate, w_up, w_down, ws, out);
}
```

```cpp
#include <hip/hip_runtime.h>
#include <hip/hip_bf16.h>
#include <stdint.h>

typedef short bf16x8 __attribute__((ext_vector_type(8)));
typedef short bf16x4 __attribute__((ext_vector_type(4)));
typedef float f32x2 __attribute__((ext_vector_type(2)));
typedef float f32x4 __attribute__((ext_vector_type(4)));
typedef float f32x16 __attribute__((ext_vector_type(16)));
typedef unsigned u32x4 __attribute__((ext_vector_type(4)));
typedef unsigned u32x2 __attribute__((ext_vector_type(2)));
typedef unsigned short ushort_t;
#define LAS __attribute__((address_space(3)))
#define GAS __attribute__((address_space(1)))
#define DI __device__ __forceinline__

constexpr int NTOK = 2048, DM = 640, NHEAD = 32, DH = 20, FF = 1728;

constexpr size_t OFF_QF1  = 0;
constexpr size_t OFF_QF2  = OFF_QF1 + 2097152;
constexpr size_t OFF_KF1  = OFF_QF2 + 524288;
constexpr size_t OFF_KF2  = OFF_KF1 + 65536;
constexpr size_t OFF_VF   = OFF_KF2 + 16384;
constexpr size_t OFF_WOF  = OFF_VF + 131072;
constexpr size_t OFF_KEYG = OFF_WOF + 819200;
constexpr size_t OFF_ACTG = OFF_KEYG + 2048;
constexpr size_t OFF_CROW = OFF_KEYG + 9216;
constexpr size_t OFF_REPL = OFF_CROW + 1310720;
constexpr size_t OFF_CONST = OFF_REPL + 2048;
constexpr unsigned GTAG = 0x600DF00Du;
constexpr size_t OFF_DIAG = 64u << 20;

#define STAMP(kid, idx) do {} while (0)
#define STAMP_INIT() do {} while (0)
#define RSTAMP(idx) do {} while (0)
#define STAMP_FLUSH(kid) do {} while (0)
#define DIAG_BYTES 0
DI float bf2f(ushort_t b) { return __uint_as_float(((unsigned)b) << 16); }
typedef __bf16 bf16x2_t __attribute__((ext_vector_type(2)));
DI unsigned cvtpk(float lo, float hi) { return __builtin_bit_cast(unsigned, __builtin_convertvector((f32x2){lo, hi}, bf16x2_t)); }
DI unsigned cvtpk2(f32x2 v) { return __builtin_bit_cast(unsigned, __builtin_convertvector(v, bf16x2_t)); }
DI f32x2 unpk(unsigned p) { return (f32x2){__uint_as_float(p << 16), __uint_as_float(p & 0xffff0000u)}; }
DI float rbf(float x) { return __uint_as_float(cvtpk(x, x) << 16); }
DI ushort_t f2bf(float x) { return (ushort_t)(cvtpk(x, x) & 0xffffu); }
DI float dot2acc(unsigned a, unsigned b, float c) { asm("v_dot2c_f32_bf16 %0, %1, %2" : "+v"(c) : "v"(a), "v"(b)); return c; }
DI void glds16(const void* g, unsigned lds_base) {
    unsigned sv;
    asm volatile("s_mov_b32 %0, m0\n\ts_mov_b32 m0, %2\n\ts_nop 0\n\tglobal_load_lds_dwordx4 %1, off\n\ts_mov_b32 m0, %0"
                 : "=&s"(sv) : "v"(g), "s"(lds_base) : "memory");
}
DI void glds16s(const char* sbase, unsigned voff, unsigned lds_base) {
    unsigned sv;
    asm volatile("s_mov_b32 %0, m0\n\ts_mov_b32 m0, %3\n\ts_nop 0\n\tglobal_load_lds_dwordx4 %1, %2\n\ts_mov_b32 m0, %0"
                 : "=&s"(sv) : "v"(voff), "s"(sbase), "s"(lds_base) : "memory");
}
#define SBAR() __builtin_amdgcn_sched_barrier(0)
#define BAR_LDS() asm volatile("s_waitcnt lgkmcnt(0)\n\ts_barrier" ::: "memory")
#define MFMA32(a, b, c) __builtin_amdgcn_mfma_f32_32x32x16_bf16((a), (b), (c), 0, 0, 0)
#define MFMA32K8(a, b, c) __builtin_amdgcn_mfma_f32_32x32x8bf16_1k((a), (b), (c), 0, 0, 0)
#define MFMA16(a, b, c) __builtin_amdgcn_mfma_f32_16x16x32_bf16((a), (b), (c), 0, 0, 0)
typedef short v4i16_t __attribute__((ext_vector_type(4)));
DI bf16x4 trread(unsigned lds_addr) {
    return __builtin_amdgcn_ds_read_tr16_b64_v4i16((LAS v4i16_t*)(uintptr_t)lds_addr);
}
#define DPP_ADD(v, ctrl, rmask, bc) v += __builtin_bit_cast(float, __builtin_amdgcn_update_dpp(0, __builtin_bit_cast(int, v), ctrl, rmask, 0xf, bc))
DI float wave_sum(float v) {
    DPP_ADD(v, 0x111, 0xf, true); DPP_ADD(v, 0x112, 0xf, true); DPP_ADD(v, 0x114, 0xf, true); DPP_ADD(v, 0x118, 0xf, true);
    DPP_ADD(v, 0x142, 0xa, false); DPP_ADD(v, 0x143, 0xc, false);
    return __builtin_bit_cast(float, __builtin_amdgcn_readlane(__builtin_bit_cast(int, v), 63));
}
DI void wave_sum2(float& a, float& b) {
    DPP_ADD(a, 0x111, 0xf, true); DPP_ADD(b, 0x111, 0xf, true); DPP_ADD(a, 0x112, 0xf, true); DPP_ADD(b, 0x112, 0xf, true);
    DPP_ADD(a, 0x114, 0xf, true); DPP_ADD(b, 0x114, 0xf, true); DPP_ADD(a, 0x118, 0xf, true); DPP_ADD(b, 0x118, 0xf, true);
    DPP_ADD(a, 0x142, 0xa, false); DPP_ADD(b, 0x142, 0xa, false); DPP_ADD(a, 0x143, 0xc, false); DPP_ADD(b, 0x143, 0xc, false);
    a = __builtin_bit_cast(float, __builtin_amdgcn_readlane(__builtin_bit_cast(int, a), 63));
    b = __builtin_bit_cast(float, __builtin_amdgcn_readlane(__builtin_bit_cast(int, b), 63));
}
DI unsigned wave_umax(unsigned v) {
#define DPP_MAX(ctrl, rmask) v = max(v, (unsigned)__builtin_amdgcn_update_dpp(0, (int)v, ctrl, rmask, 0xf, true))
    DPP_MAX(0x111, 0xf); DPP_MAX(0x112, 0xf); DPP_MAX(0x114, 0xf); DPP_MAX(0x118, 0xf); DPP_MAX(0x142, 0xa); DPP_MAX(0x143, 0xc);
    return (unsigned)__builtin_amdgcn_readlane((int)v, 63);
}
DI float xor16(float v) { auto r = __builtin_amdgcn_permlane16_swap(__float_as_uint(v), __float_as_uint(v), false, false);
                          return __uint_as_float(r[0]) + __uint_as_float(r[1]); }
DI float xor32(float v) { auto r = __builtin_amdgcn_permlane32_swap(__float_as_uint(v), __float_as_uint(v), false, false);
                          return __uint_as_float(r[0]) + __uint_as_float(r[1]); }
DI float max32(float v) { auto r = __builtin_amdgcn_permlane32_swap(__float_as_uint(v), __float_as_uint(v), false, false);
                          return __builtin_fmaxf(__uint_as_float(r[0]), __uint_as_float(r[1])); }

constexpr int K1_XB = 0;
constexpr int K1_W = 40960;
constexpr int K1_KV = 143360;
constexpr int K1_WOT = 153664;
constexpr int K1_ST = 158784;
constexpr int K1_CS = K1_ST + 1024;
constexpr int K1_JUNK = K1_CS + 768;
constexpr int K1_END = K1_JUNK + 256;
constexpr int K1_LDS_BYTES = K1_END + DIAG_BYTES;
constexpr int K1_OQ = K1_XB;
constexpr int K1_OKV = K1_XB + 10240;

DI int swap23(int r) { return (r & ~12) | ((r & 4) << 1) | ((r & 8) >> 1); }
DI void glds5(const char* sbase, const unsigned (&v)[5], unsigned lds_base) {
    unsigned sv;
    asm volatile("s_mov_b32 %0, m0\n\ts_mov_b32 m0, %7\n\ts_nop 0\n\tglobal_load_lds_dwordx4 %1, %6\n\t"
                 "s_add_u32 m0, m0, 0x400\n\ts_nop 0\n\tglobal_load_lds_dwordx4 %2, %6\n\t"
                 "s_add_u32 m0, m0, 0x400\n\ts_nop 0\n\tglobal_load_lds_dwordx4 %3, %6\n\t"
                 "s_add_u32 m0, m0, 0x400\n\ts_nop 0\n\tglobal_load_lds_dwordx4 %4, %6\n\t"
                 "s_add_u32 m0, m0, 0x400\n\ts_nop 0\n\tglobal_load_lds_dwordx4 %5, %6\n\t"
                 "s_mov_b32 m0, %0"
                 : "=&s"(sv) : "v"(v[0]), "v"(v[1]), "v"(v[2]), "v"(v[3]), "v"(v[4]), "s"(sbase), "s"(lds_base) : "memory", "scc");
}
#define VMBAR(n) asm volatile("s_waitcnt vmcnt(" #n ") lgkmcnt(0)\n\ts_barrier" ::: "memory")

#define RAWBAR() asm volatile("s_barrier" ::: "memory")
#define K1_SYM "_Z6k1_qkvPKtS0_S0_S0_S0_S0_S0_S0_Pc"
#define K2_SYM "_Z7k2_attnPKtS0_S0_S0_S0_PcPt"
#define CODE_ADDR(dst, sym) asm volatile("s_getpc_b64 s[40:41]\n\ts_add_u32 s40, s40, " sym "@rel32@lo+4\n\ts_addc_u32 s41, s41, " sym "@rel32@hi+12\n\t" \
                                         "s_mov_b64 %0, s[40:41]" : "=s"(dst) :: "scc", "s40", "s41")
DI void k1_loader(char* smem, unsigned lds0, const ushort_t* x, const ushort_t* wq, const ushort_t* wk, const ushort_t* wv,
                  const ushort_t* wo, const ushort_t* w_gate, const ushort_t* w_up, const ushort_t* w_down,
                  char* ws, int d, int lane, int b, int panel, int j) {
#define DIAG_LDS K1_END
    const int rot = (5 * (panel & 3) + 2 * j + (b & 7)) % 20;
    unsigned xo[5], xo2[5], wo5[5];
#pragma unroll
    for (int k = 0; k < 5; ++k) {
        const int p = k * 64 + lane;
        const int r = p / 40, c = p - r * 40;
        const int cb = (((c >> 2) ^ ((r >> 1) & 1)) * 64) + (c & 3) * 16 + rot * 64;
        xo[k] = r * 1280 + (cb >= 1280 ? cb - 1280 : cb);
        const int cb2 = cb + 640;
        xo2[k] = r * 1280 + (cb2 >= 1280 ? cb2 - 1280 : cb2);
    }
    const char* xb = (const char*)(x + (size_t)(panel * 64 + d * 16) * DM);
    glds5(xb, xo, lds0 + K1_XB + d * 10240);
    glds5(xb + 10240, xo, lds0 + K1_XB + d * 10240 + 5120);
#pragma unroll
    for (int k = 0; k < 5; ++k) {
        const int p = k * 64 + lane;
        const int rw = p / 10, c8 = p - rw * 10;
        wo5[k] = swap23(rw) * 1280 + c8 * 16;
    }
    const char* wqb = (const char*)(wq + j * 80);
    const unsigned wl = lds0 + K1_W + d * 5120;
#define K1_WSLOT(s4) glds5(wqb + (size_t)((d + (s4) + rot) % 20) * 40960, wo5, wl + (s4) * 5120)
    K1_WSLOT(0);
    VMBAR(5);
    K1_WSLOT(4);
    VMBAR(5);
    glds5(xb, xo2, lds0 + K1_XB + d * 10240);
    glds5(xb + 10240, xo2, lds0 + K1_XB + d * 10240 + 5120);
    K1_WSLOT(8);
    VMBAR(15);
    K1_WSLOT(12);
    K1_WSLOT(16);
    VMBAR(10);
    {
        const int bo = min(b, 159);
        const char* wob = (const char*)(wo + (size_t)(bo >> 3) * 32 * DM + (bo & 7) * 80);
        const unsigned oa = (d == 0) ? wo5[0] : (d == 1) ? wo5[1] : (d == 2) ? wo5[2] : wo5[3];
        glds16s(wob, oa, lds0 + K1_WOT + d * 1024);
        glds16s(wob, wo5[4], lds0 + K1_WOT + 4096);
    }
    {
        const unsigned sl = (b >> 3) * 4 + d;
#pragma unroll
        for (int i = 0; i < 2; ++i) {
            const unsigned l = min(i * 64u + lane, 76u);
            const char* p;
            if (l < 50u) { const unsigned n = sl * 50 + l;
                p = (const char*)((n >= 3200u) ? w_up : w_gate) + (size_t)((n % 3200u) / 5u) * 3456 + min(((b & 7) * 432u & ~127u) + (n % 5u) * 128u, 3328u); }
            else { const unsigned n = sl * 27 + (l - 50u);
                p = (const char*)w_down + (size_t)(n >> 1) * 1280 + min(((b & 7) * 160u & ~127u) + (n & 1u) * 128u, 1152u); }
            unsigned sv;
            asm volatile("s_mov_b32 %0, m0\n\ts_mov_b32 m0, %2\n\ts_nop 0\n\tglobal_load_lds_dword %1, off\n\ts_mov_b32 m0, %0"
                         : "=&s"(sv) : "v"(p), "s"(lds0 + K1_JUNK) : "memory");
        }
        if (d == 1) {
            unsigned long long c0, c1;
            CODE_ADDR(c0, K2_SYM); CODE_ADDR(c1, "k2_code_end_mark");
            const unsigned off = min(((b >> 3) * 12u + min((unsigned)lane, 11u)) * 128u, (unsigned)(c1 - c0) & ~3u);
            unsigned sv;
            asm volatile("s_mov_b32 %0, m0\n\ts_mov_b32 m0, %3\n\ts_nop 0\n\tglobal_load_lds_dword %1, %2\n\ts_mov_b32 m0, %0"
                         : "=&s"(sv) : "v"(off), "s"(c0), "s"(lds0 + K1_JUNK) : "memory");
        }
        const u32x4 z = {0, 0, 0, 0};
        char* zp = ws + OFF_CROW + (size_t)b * 5120 + d * 1024 + lane * 16;
        asm volatile("global_store_dwordx4 %0, %1, off\n\ts_nop 1" :: "v"(zp), "v"(z) : "memory");
        if (d == 0) zp += 4096; else if (b < 9) zp = ws + OFF_KEYG + b * 1024 + lane * 16; else if (b < 11) zp = ws + OFF_REPL + (b - 9) * 1024 + lane * 16;
        asm volatile("global_store_dwordx4 %0, %1, off\n\ts_nop 1" :: "v"(zp), "v"(z) : "memory");
    }
    VMBAR(6);
    if (d == 2 && b == 0 && lane < 3) {
        const unsigned o2 = 0x3F803F80u;
        *(u32x4*)(ws + OFF_CONST + lane * 16) = (lane == 0) ? (u32x4){o2, o2, o2, o2} : (lane == 1) ? (u32x4){0, 0, 0, 0} : (u32x4){o2, 0x3F80u, 0, 0};
    }
    VMBAR(4);
    if (d == 3 && b < 160) {
        const int l15 = lane & 15, lg = lane >> 4;
        const unsigned a = lds0 + K1_WOT + (16 * (lg >> 1) + 4 * (lg & 1) + (l15 >> 2)) * 160 + (l15 & 3) * 8;
        char* dst = ws + OFF_WOF + (size_t)((b & 7) * 100 + (b >> 3) * 5) * 1024 + lane * 16;
#pragma unroll
        for (int t = 0; t < 5; ++t) {
            const bf16x4 lo = trread(a + t * 32), hi = trread(a + t * 32 + 1280);
            *(bf16x8*)(dst + t * 1024) = __builtin_shufflevector(lo, hi, 0, 1, 2, 3, 4, 5, 6, 7);
        }
    }
    if (d == 1 && j < 6) {
        const u32x4 kv = *(const u32x4*)(smem + K1_OKV + lane * 16);
        const int tile0 = panel * 2;
        if (j < 2) {
            *(u32x4*)(ws + OFF_KF1 + (size_t)((tile0 + (lane >> 5)) * 64 + 32 * j + (lane & 31)) * 16) = kv;
        } else if (j == 2) {
            if (lane < 32) *(u32x4*)(ws + OFF_KF2 + (size_t)tile0 * 256 + lane * 16) = kv;
        } else {
            const int dd = (j == 5) ? 12 + (lane & 7) : 8 * (j - 3) + (lane & 7);
            if (j < 5 || (lane & 7) >= 4)
                *(u32x4*)(ws + OFF_VF + (size_t)((tile0 * 2 + (lane >> 4)) * 64 + 32 * ((lane >> 3) & 1) + dd) * 16) = kv;
        }
    }
    asm volatile("s_waitcnt vmcnt(0)\n\t.globl k1_code_end_mark\n\t.protected k1_code_end_mark\nk1_code_end_mark:" ::: "memory");
#undef DIAG_LDS
}

template <int NH, int LOW>
DI void k1_body(char* smem, unsigned lds0, char* ws, int u, int lane, int j) {
#define DIAG_LDS K1_END
    const int l15 = lane & 15, lg = lane >> 4;
    const int m = u >> 1, rho = (m ^ NH) & 1;
    bf16x8 xf[10];
    const char* xr0 = smem + K1_XB + (m * 16 + l15) * 640 + lg * 16;
    const int e = rho ^ ((l15 >> 1) & 1);
    const char* xe = xr0 + e * 64;
    const char* xod = xr0 - e * 64;
    float s1 = 0.f, s2 = 0.f;
    const unsigned ones2 = 0x3F803F80u;
    f32x4 acc[3], cs[3];
#pragma unroll
    for (int t = 0; t < 3; ++t) { acc[t] = (f32x4){0, 0, 0, 0}; cs[t] = (f32x4){0, 0, 0, 0}; }
    bf16x8 onesf;
#pragma unroll
    for (int i = 0; i < 8; ++i) onesf[i] = (short)0x3F80;
    asm volatile("" : "+v"(onesf));
    const int rrow = 16 * (lg >> 1) + 4 * (lg & 1) + (l15 >> 2);
    const unsigned aW = lds0 + K1_W + rrow * 160 + (l15 & 3) * 8 + NH * 96;
    const unsigned aW4[4] = {aW + rho * 5120, aW - rho * 5120, aW + rho * 5120 + 51200, aW - rho * 5120 + 51200};
    const unsigned aK = lds0 + K1_KV + (8 * lg + (l15 >> 2)) * 16 + (l15 & 3) * 8;
    const unsigned aKe = aK + rho * 512, aKo = aK - rho * 512;
    bf16x8 wf[4][3];
#define K1_STATS() do { _Pragma("unroll") for (int i_ = 0; i_ < 10; i_ += 2) { const u32x4 v_ = __builtin_bit_cast(u32x4, xf[i_]); \
        _Pragma("unroll") for (int q_ = 0; q_ < 4; ++q_) { s1 = dot2acc(v_[q_], ones2, s1); s2 = dot2acc(v_[q_], v_[q_], s2); } } SBAR(); } while (0)
#define K1_READ(i) do { \
        const unsigned ab_ = aW4[((i) >= 10 ? 2 : 0) + ((i) & 1)] + ((i) % 10) * 5120; \
        _Pragma("unroll") for (int t_ = 0; t_ < (NH ? 2 : 3); ++t_) { \
            const bf16x4 lo_ = trread(ab_ + t_ * 32), hi_ = trread(ab_ + t_ * 32 + 1280); \
            wf[(i) & 3][t_] = __builtin_shufflevector(lo_, hi_, 0, 1, 2, 3, 4, 5, 6, 7); } \
        if (NH) { const unsigned ak_ = (((i) & 1) ? aKo : aKe) + (i) * 512; \
            const bf16x4 lo_ = trread(ak_), hi_ = trread(ak_ + 64); \
            wf[(i) & 3][2] = __builtin_shufflevector(lo_, hi_, 0, 1, 2, 3, 4, 5, 6, 7); } \
    } while (0)
#define K1_MMA(i, XF) do { \
        _Pragma("unroll") for (int t_ = 0; t_ < 3; ++t_) { \
            acc[t_] = MFMA16(wf[(i) & 3][t_], XF[(i) % 10], acc[t_]); \
            if ((i) >= 12 || (LOW && ((i) & 1) == 0)) cs[t_] = MFMA16(wf[(i) & 3][t_], onesf, cs[t_]); } \
    } while (0)
#define K1_CHUNK(i0, XA, XB) do { K1_READ(i0); K1_READ((i0) + 1); K1_READ((i0) + 2); K1_READ((i0) + 3); SBAR(); \
        K1_MMA(i0, XA); K1_MMA((i0) + 1, XA); K1_MMA((i0) + 2, XB); K1_MMA((i0) + 3, XB); SBAR(); } while (0)
    RAWBAR();
    STAMP(0, 11);
#pragma unroll
    for (int i = 0; i < 10; ++i) xf[i] = *(const bf16x8*)(((i & 1) ? xod : xe) + i * 64);
    VMBAR(0);
    K1_CHUNK(0, xf, xf);
    K1_STATS();
    STAMP(0, 1);
    RAWBAR();
    K1_CHUNK(4, xf, xf);
    RAWBAR();
    bf16x8 xg[10];
#pragma unroll
    for (int i = 0; i < 10; ++i) xg[i] = *(const bf16x8*)(((i & 1) ? xod : xe) + i * 64);
    K1_CHUNK(8, xf, xg);
#pragma unroll
    for (int i = 0; i < 10; ++i) xf[i] = xg[i];
    K1_STATS();
    s1 = xor32(xor16(s1));
    s2 = xor32(xor16(s2));
    if (lg == 0) *(f32x2*)(smem + K1_ST + (NH * 64 + m * 16 + l15) * 8) = (f32x2){s1, s2};
    if (LOW && l15 == 0) {
#pragma unroll
        for (int t = 0; t < 3; ++t) *(f32x4*)(smem + K1_CS + (m * 96 + NH * 48 + t * 16 + lg * 4) * 4) = cs[t];
    }
    STAMP(0, 2);
    BAR_LDS();
    const int tl = m * 16 + l15;
    K1_READ(12); K1_READ(13);
    const f32x2 sa = *(const f32x2*)(smem + K1_ST + tl * 8);
    const f32x2 sb = *(const f32x2*)(smem + K1_ST + (64 + tl) * 8);
#pragma unroll
    for (int t = 0; t < 3; ++t)
        cs[t] = *(const f32x4*)(smem + K1_CS + (NH * 48 + t * 16 + lg * 4) * 4) +
                *(const f32x4*)(smem + K1_CS + (96 + NH * 48 + t * 16 + lg * 4) * 4);
    SBAR();
#pragma unroll
    for (int i = 12; i < 20; ++i) { if (i + 2 < 20) K1_READ(i + 2); SBAR(); K1_MMA(i, xf); SBAR(); }
    const float mean = (sa[0] + sb[0]) * (1.0f / DM);
    const float var = fmaxf((sa[1] + sb[1]) * (1.0f / DM) - mean * mean, 0.f);
    const float mb = rbf(mean);
    const float sig = rbf(__builtin_amdgcn_sqrtf(rbf(rbf(var) + 1e-5f)));
    const float rs = __builtin_amdgcn_rcpf(sig);
    STAMP(0, 4);
    const float QSCALE = 0.22360679774997896f * 1.4426950408889634f;
    const int tok = tl & 7;
#pragma unroll
    for (int t = 0; t < 3; ++t) {
        const f32x4 csum = cs[t];
        float v[4];
#pragma unroll
        for (int r = 0; r < 4; ++r) v[r] = (acc[t][r] - mb * csum[r]) * rs;
        if (t == 2 && NH == 1) {
            if (lg < 2) {
                const int kk = tl & 31;
                if (j < 3) {
                    u32x2 pk; pk[0] = cvtpk(v[0], v[1]); pk[1] = cvtpk(v[2], v[3]);
                    if (j < 2) *(u32x2*)(smem + K1_OKV + tl * 16 + lg * 8) = pk;
                    else if (lg == 1) *(u32x2*)(smem + K1_OKV + tl * 8) = pk;
                } else {
                    const int s = kk >> 4, h = (kk >> 2) & 1, jj = ((kk >> 3) & 1) * 4 + (kk & 3);
                    char* d = smem + K1_OKV + (((((tl >> 5) * 2 + s) * 2 + h) * 8 + lg * 4) * 16) + jj * 2;
#pragma unroll
                    for (int r = 0; r < 4; ++r) *(ushort_t*)(d + r * 16) = f2bf(v[r]);
                }
            }
        } else {
            const int nl = (NH * 3 + t) * 16 + lg * 4;
            const int hl = nl / 20, dq = (nl - hl * 20) >> 2;
            u32x2 pk;
            pk[0] = cvtpk(v[0] * QSCALE, v[1] * QSCALE);
            pk[1] = cvtpk(v[2] * QSCALE, v[3] * QSCALE);
            const int off = (dq < 4) ? ((hl * 8 + tok + 32 * (dq >> 1)) * 16 + (dq & 1) * 8) : (1024 + (hl * 8 + tok) * 8);
            *(u32x2*)(smem + K1_OQ + (tl >> 3) * 1280 + off) = pk;
        }
    }
    BAR_LDS();
    STAMP(0, 5);
#undef DIAG_LDS
}

__global__ __launch_bounds__(768, 3) void k1_qkv(const ushort_t* __restrict__ x, const ushort_t* __restrict__ wq,
                                                 const ushort_t* __restrict__ wk, const ushort_t* __restrict__ wv,
                                                 const ushort_t* __restrict__ wo, const ushort_t* __restrict__ w_gate,
                                                 const ushort_t* __restrict__ w_up, const ushort_t* __restrict__ w_down,
                                                 char* __restrict__ ws) {
    extern __shared__ __attribute__((aligned(16))) char smem[];
    const unsigned lds0 = (unsigned)(uintptr_t)smem;
    const int lane = threadIdx.x & 63;
    const int u = __builtin_amdgcn_readfirstlane(threadIdx.x >> 6);
    const int b = blockIdx.x;
    const int panel = (b & 7) * 4 + ((b >> 3) >> 3), j = (b >> 3) & 7;
    if (u >= 8) { k1_loader(smem, lds0, x, wq, wk, wv, wo, w_gate, w_up, w_down, ws, u - 8, lane, b, panel, j); return; }
#define DIAG_LDS K1_END
    STAMP_INIT();
    STAMP(0, 0); RSTAMP(30);
    if (u >= 2 && u < 5) {
        unsigned long long c0, c1;
        CODE_ADDR(c0, K1_SYM); CODE_ADDR(c1, "k1_code_end_mark");
        const unsigned off = min(((u - 2) * 64u + lane) * 128u, (unsigned)(c1 - c0) & ~3u);
        unsigned sv;
        asm volatile("s_mov_b32 %0, m0\n\ts_mov_b32 m0, %3\n\ts_nop 0\n\tglobal_load_lds_dword %1, %2\n\ts_mov_b32 m0, %0"
                     : "=&s"(sv) : "v"(off), "s"(c0), "s"(lds0 + K1_JUNK) : "memory");
    }
    {
        const int rot = (5 * (panel & 3) + 2 * j + (b & 7)) % 20;
        const int jq = (j >= 3) ? ((j >= 6) ? 0 : j - 3) : j;
        const char* kvb = (const char*)((j >= 3 && j < 6) ? wv : wk) + (jq == 2 ? 24 : jq * 16);
        int row = u * 64 + lane + rot * 32;
        glds16s(kvb, (unsigned)((row >= 640 ? row - 640 : row) * 40), lds0 + K1_KV + u * 1024);
        if (u < 2) {
            row += 512;
            glds16s(kvb, (unsigned)((row >= 640 ? row - 640 : row) * 40), lds0 + K1_KV + (8 + u) * 1024);
        }
    }
    if (u < 4) {
        if (u & 1) k1_body<1, 1>(smem, lds0, ws, u, lane, j);
        else k1_body<0, 1>(smem, lds0, ws, u, lane, j);
    } else {
        if (u & 1) k1_body<1, 0>(smem, lds0, ws, u, lane, j);
        else k1_body<0, 0>(smem, lds0, ws, u, lane, j);
    }
    {
        const int wg2 = panel * 8 + u;
        const u32x4 v = *(const u32x4*)(smem + K1_OQ + u * 1280 + lane * 16);
        *(u32x4*)(ws + OFF_QF1 + (size_t)(wg2 * 8 + j) * 1024 + lane * 16) = v;
        if (lane < 16) {
            const u32x4 v2 = *(const u32x4*)(smem + K1_OQ + u * 1280 + 1024 + lane * 16);
            *(u32x4*)(ws + OFF_QF2 + (size_t)(wg2 * 8 + j) * 256 + lane * 16) = v2;
        }
    }
    STAMP(0, 6); RSTAMP(31);
    STAMP_FLUSH(0);
#undef DIAG_LDS
}

constexpr int K2_PBUF = 0;
constexpr int K2_QL = 49152;
constexpr int K2_OT = 61440;
constexpr int K2_MISC = 71680;
constexpr int K2_TAIL = K2_MISC + 1152;
constexpr int K2_GU = K2_TAIL;
constexpr int K2_ACT = K2_TAIL + 57344;
constexpr int K2_STG = K2_PBUF;
constexpr int K2_JUNK = K2_TAIL + 81920;
constexpr int K2_END = K2_JUNK + 1024;
constexpr int K2_LDS_BYTES = K2_END + DIAG_BYTES;

DI void split3(float v, ushort_t& a, ushort_t& b, ushort_t& c) {
    a = f2bf(v); const float r1 = v - bf2f(a);
    b = f2bf(r1); const float r2 = r1 - bf2f(b);
    c = f2bf(r2);
}
#define MX3(a, b, c) __builtin_fmaxf(__builtin_fmaxf((a), (b)), (c))
DI float max16(const f32x16& s) {
    float a = MX3(s[0], s[1], s[2]), b = MX3(s[3], s[4], s[5]);
    a = MX3(a, s[6], s[7]); b = MX3(b, s[8], s[9]);
    a = MX3(a, s[10], s[11]); b = MX3(b, s[12], s[13]);
    a = MX3(a, s[14], s[15]);
    return __builtin_fmaxf(a, b);
}

__global__ __launch_bounds__(512, 2) void k2_attn(const ushort_t* __restrict__ x, const ushort_t* __restrict__ w_r,
                                                   const ushort_t* __restrict__ w_gate, const ushort_t* __restrict__ w_up,
                                                   const ushort_t* __restrict__ w_down,
                                                   char* __restrict__ ws, ushort_t* __restrict__ out) {
    extern __shared__ __attribute__((aligned(16))) char smem[];
    const unsigned lds0 = (unsigned)(uintptr_t)smem;
    const int w_0 = __builtin_amdgcn_readfirstlane(threadIdx.x >> 6);
    const int wg_0 = (blockIdx.x & 7) * 32 + (blockIdx.x >> 3);
    const unsigned lane16 = (threadIdx.x & 63) * 16;
#define FRESH_IDS int w = w_0, wg = wg_0; asm volatile("" : "+s"(w), "+s"(wg));
#define DIAG_LDS K2_END
    STAMP_INIT();
    STAMP(1, 0); RSTAMP(30);
    bf16x8 q1w = *(const bf16x8*)(ws + OFF_QF1 + (size_t)(wg_0 * 8 + w_0) * 1024 + lane16);
    bf16x4 q2w = *(const bf16x4*)(ws + (lane16 < 512u ? (unsigned)OFF_QF2 + (unsigned)(wg_0 * 8 + w_0) * 256u + (lane16 >> 1) : (unsigned)OFF_CONST + 16u));
    bf16x8 k1w = *(const bf16x8*)(ws + OFF_KF1 + (size_t)w_0 * 8192 + lane16);
    bf16x4 k2w = *(const bf16x4*)(ws + (lane16 < 512u ? (unsigned)OFF_KF2 + (unsigned)w_0 * 2048u + (lane16 >> 1) : (unsigned)OFF_CONST + 32u));
    SBAR();
    if (w_0 == 7) {
        unsigned long long c0, c1;
        CODE_ADDR(c0, K2_SYM); CODE_ADDR(c1, "k2_code_end_mark");
        const unsigned off = min(((blockIdx.x >> 3) * 8u + ((lane16 >> 4) & 7u)) * 128u, (unsigned)(c1 - c0) & ~3u);
        unsigned sv;
        asm volatile("s_mov_b32 %0, m0\n\ts_mov_b32 m0, %3\n\ts_nop 0\n\tglobal_load_lds_dword %1, %2\n\ts_mov_b32 m0, %0"
                     : "=&s"(sv) : "v"(off), "s"(c0), "s"(lds0 + K2_JUNK) : "memory");
    }
    const __amdgpu_buffer_rsrc_t wrs = __builtin_amdgcn_make_buffer_rsrc((void*)(ws + OFF_WOF + (size_t)w_0 * 102400), 0, 102400 + 5120, 0x00020000);
    const __amdgpu_buffer_rsrc_t wsr = __builtin_amdgcn_make_buffer_rsrc((void*)ws, 0, 8 << 20, 0x00020000);
    unsigned lo4[4] = {lane16, lane16 + 1024, lane16 + 2048, lane16 + 3072};
    asm volatile("" : "+v"(lo4[1]), "+v"(lo4[2]), "+v"(lo4[3]));
#define WOLOAD(off) __builtin_bit_cast(bf16x8, __builtin_amdgcn_raw_buffer_load_b128(wrs, lo4[((off) >> 10) & 3], (off) & ~4095, 0))
#define OPQ16() ({ unsigned l_ = lane16; asm volatile("" : "+v"(l_)); l_; })

    const int bi = blockIdx.x;
    const int gcb = (bi & 7) * 27 + min(bi >> 3, 26);
    const int dcb = (bi & 7) * 5 + max((bi >> 3) - 27, 0);
    u32x4 xa, wa4; unsigned xb1, wb1;
    f32x4 oacc[5];
    const f32x16 zero16 = {};

    for (int attempt = 0; attempt < 2; ++attempt) {
        FRESH_IDS
        bf16x8 kf1[8]; bf16x4 kf2[8]; bf16x8 vf[8][2];
        unsigned k2o, k2s, vo, vs;
        STAMP(1, 14);
        {
            const unsigned l16 = OPQ16();
            const unsigned lane = l16 >> 4, r32 = lane & 31, hi = lane >> 5;
            k2o = hi == 0 ? (unsigned)OFF_KF2 + (unsigned)(w * 256 + r32) * 8u : (unsigned)OFF_CONST + 32u;
            k2s = hi == 0 ? 256u : 0u;
            vo = r32 < DH ? (unsigned)OFF_VF + (unsigned)(w * 1024) * 16u + l16 : (unsigned)OFF_CONST + (r32 == DH ? 0u : 16u);
            vs = r32 < DH ? 1024u : 0u;
#define KLOAD(t) do { kf1[t] = __builtin_bit_cast(bf16x8, __builtin_amdgcn_raw_buffer_load_b128(wsr, lane16, (int)OFF_KF1 + w * 8192 + (t) * 1024, 0)); \
            kf2[t] = __builtin_bit_cast(bf16x4, __builtin_amdgcn_raw_buffer_load_b64(wsr, k2o + (t) * k2s, 0, 0)); } while (0)
#define VLOAD(t) do { vf[t][0] = __builtin_bit_cast(bf16x8, __builtin_amdgcn_raw_buffer_load_b128(wsr, vo + (2 * (t)) * vs, 0, 0)); \
            vf[t][1] = __builtin_bit_cast(bf16x8, __builtin_amdgcn_raw_buffer_load_b128(wsr, vo + (2 * (t) + 1) * vs, 0, 0)); } while (0)
            KLOAD(0); SBAR(); KLOAD(1); SBAR(); VLOAD(0); SBAR(); KLOAD(2); SBAR(); VLOAD(1); SBAR();
            if (attempt == 0) {
                f32x16 s = MFMA32(k1w, q1w, zero16);
                s = MFMA32K8(k2w, q2w, s);
                const float m = max32(max16(s));
                if (hi == 1) { ushort_t a, b2, c; split3(-m, a, b2, c); q2w = (bf16x4){(short)a, (short)b2, (short)c, 0}; }
                *(bf16x8*)(smem + K2_QL + w * 1536 + l16) = q1w;
                *(bf16x4*)(smem + K2_QL + w * 1536 + 1024 + (l16 >> 1)) = q2w;
            }
            if (w == 0) *(unsigned*)(smem + K2_MISC) = 0u;
        }
#pragma unroll
        for (int t = 0; t < 5; ++t) oacc[t] = (f32x4){0, 0, 0, 0};
        BAR_LDS();
        STAMP(1, 1); RSTAMP(28);

        unsigned long long badm = 0ull;
        f32x16 sc, o;
        u32x4 p0, p1;
        bf16x8 wa[5];
        bf16x8 q1 = *(const bf16x8*)(smem + K2_QL + OPQ16());
        bf16x4 q2 = *(const bf16x4*)(smem + K2_QL + 1024 + (OPQ16() >> 1));
        const char* otp; { const unsigned l_ = OPQ16(); otp = smem + K2_OT + ((l_ >> 1) & 0x180) + (l_ & 0x70); }
        char* pbv = smem + K2_PBUF + w * 3072 + OPQ16();
        const char* q2v = smem + K2_QL + 1024 + (OPQ16() >> 1);
        const char *s2pa, *s2pl; char* s2dst;
        {
            const unsigned l_ = OPQ16() >> 4;
            const unsigned it_ = w * 20 + min(l_ >> 1, 19u);
            s2pa = smem + K2_PBUF + (l_ & 1) * 12288 + it_ * 16;
            s2pl = smem + K2_PBUF + (l_ & 1) * 12288 + (160 + (it_ & 31)) * 16;
            const unsigned kr_ = 20 * ((it_ & 31) >> 3) + 4 * (it_ >> 5);
            s2dst = smem + K2_OT + ((kr_ >> 3) * 8 + (it_ & 7)) * 16 + (kr_ & 7) * 2;
        }
#define PHASE_LOCALS bf16x8 q1n; bf16x4 q2n; bf16x8 ob; f32x4 sa0, sa1, sa2, sa3; float sl0, sl1, sl2, sl3;
        sc = MFMA32(kf1[0], q1, zero16);
        sc = MFMA32K8(kf2[0], q2, sc);
#define PRIO(n) __builtin_amdgcn_s_setprio(n)
#define LSTAMP(a, b) STAMP(a, b)

#define EXP4(S, i) do { S[i] = __builtin_amdgcn_exp2f(S[i]); S[i + 1] = __builtin_amdgcn_exp2f(S[i + 1]); \
                        S[i + 2] = __builtin_amdgcn_exp2f(S[i + 2]); S[i + 3] = __builtin_amdgcn_exp2f(S[i + 3]); } while (0)
#define PACK4(P, S, i) do { P[0] = cvtpk(S[i], S[i + 1]); P[1] = cvtpk(S[i + 2], S[i + 3]); \
                            P[2] = cvtpk(S[i + 4], S[i + 5]); P[3] = cvtpk(S[i + 6], S[i + 7]); } while (0)
#define BF8(P) __builtin_bit_cast(bf16x8, P)
#define WP_ADDR(PAR) char* pb_ = pbv + (PAR) * 24576
#define WP_ST(i) *(f32x4*)(pb_ + (i) * 1024) = (f32x4){o[4 * (i)], o[4 * (i) + 1], o[4 * (i) + 2], o[4 * (i) + 3]}
#define WRITE_PARTIAL(PAR) do { WP_ADDR(PAR); asm volatile("" : "+v"(o)); WP_ST(0); WP_ST(1); WP_ST(2); } while (0)
#define STEP2A(PAR) do { const char* a_ = s2pa + (PAR) * 24576; const char* l_ = s2pl + (PAR) * 24576; \
            sa0 = *(const f32x4*)(a_); sa1 = *(const f32x4*)(a_ + 3072); sa2 = *(const f32x4*)(a_ + 6144); sa3 = *(const f32x4*)(a_ + 9216); \
            sl0 = *(const float*)(l_); sl1 = *(const float*)(l_ + 3072); sl2 = *(const float*)(l_ + 6144); sl3 = *(const float*)(l_ + 9216); \
        } while (0)
#define STEP2B() do { \
            sa0 = (sa0 + sa1) + (sa2 + sa3); sl0 += sl1; asm volatile("" : "+v"(sl0)); sl2 += sl3; asm volatile("" : "+v"(sl2)); sl0 += sl2; \
            DPP_ADD(sl0, 0xB1, 0xf, true); \
            badm |= __ballot(!(sl0 < 1.8446744e19f)); \
            sl0 = __builtin_amdgcn_rcpf(sl0); \
            asm volatile("" : "+v"(sa0), "+v"(sl0)); \
        } while (0)
#define STEP2C(G) do { \
            float a0_ = sa0[0], a1_ = sa0[1], a2_ = sa0[2], a3_ = sa0[3]; \
            DPP_ADD(a0_, 0xB1, 0xf, true); DPP_ADD(a1_, 0xB1, 0xf, true); DPP_ADD(a2_, 0xB1, 0xf, true); DPP_ADD(a3_, 0xB1, 0xf, true); \
            u32x2 pk_; pk_[0] = cvtpk(a0_ * sl0, a1_ * sl0); pk_[1] = cvtpk(a2_ * sl0, a3_ * sl0); \
            *(u32x2*)(s2dst + (G) * 1280) = pk_; \
        } while (0)
#define TAILDMA(i) glds16s(ws + OFF_WOF + (size_t)w * 102400 + 92160 + (i) * 1024, lane16, lds0 + K2_TAIL + w * 10240 + (i) * 1024)
#define WARMDMA(i) glds16s(ws + OFF_WOF + (size_t)((blockIdx.x >> 3) * 25 + min(w + 8 * (i), 24)) * 1024, lane16, lds0 + K2_JUNK)
#define GUDMA(i) do { const unsigned l_ = OPQ16() >> 4; const int in_ = min(w + 8 * (i), 19); \
            glds16((const char*)((l_ & 1) ? w_up : w_gate) + (size_t)(in_ * 32 + (l_ >> 1)) * 3456 + gcb * 16, lds0 + K2_GU + in_ * 1024); } while (0)
#define WARMXW() do { const unsigned l_ = OPQ16() >> 4; \
            const char* p_ = (l_ & 32) ? (const char*)w_r + min(l_ & 31u, 9u) * 128 : (const char*)(x + (size_t)(wg * 8 + w) * DM) + min(l_, 9u) * 128; \
            unsigned sv_; asm volatile("s_mov_b32 %0, m0\n\ts_mov_b32 m0, %2\n\ts_nop 0\n\tglobal_load_lds_dword %1, off\n\ts_mov_b32 m0, %0" \
                                       : "=&s"(sv_) : "v"(p_), "s"(lds0 + K2_JUNK) : "memory"); } while (0)
#define XWLOAD() do { const unsigned l_ = OPQ16(); const char* xrow_ = (const char*)(x + (size_t)(wg * 8 + w) * DM); \
            xa = *(const u32x4*)(xrow_ + l_); xb1 = *(const unsigned*)(xrow_ + 1024 + (l_ >> 2)); \
            wa4 = *(const u32x4*)((const char*)w_r + l_); wb1 = *(const unsigned*)((const char*)w_r + 1024 + (l_ >> 2)); } while (0)
#define OPJ(...) __VA_ARGS__
#define LOOPBAR() BAR_LDS()
#define S2(...) __VA_ARGS__

        {
            const int gn = 1;
            PHASE_LOCALS
            FRESH_IDS
            PRIO(1); EXP4(sc, 0); SBAR(); PRIO(2);
            EXP4(sc, 4); PACK4(p0, sc, 0); SBAR(); EXP4(sc, 8); EXP4(sc, 12); PACK4(p1, sc, 8); SBAR(); PRIO(3);
            sc = MFMA32(kf1[1], q1, zero16); SBAR();
            sc = MFMA32K8(kf2[1], q2, sc); SBAR();
            KLOAD(3);
            SBAR();
            o = MFMA32(vf[0][0], BF8(p0), zero16); SBAR();
            VLOAD(2);
            SBAR();
            o = MFMA32(vf[0][1], BF8(p1), o); SBAR();
            PRIO(1); EXP4(sc, 0); SBAR(); PRIO(2);
            EXP4(sc, 4); PACK4(p0, sc, 0); SBAR(); EXP4(sc, 8); EXP4(sc, 12); PACK4(p1, sc, 8); SBAR(); PRIO(3);
            sc = MFMA32(kf1[2], q1, zero16); SBAR();
            sc = MFMA32K8(kf2[2], q2, sc); SBAR();
            KLOAD(4);
            SBAR();
            o = MFMA32(vf[1][0], BF8(p0), o); SBAR();
            VLOAD(3);
            SBAR();
            o = MFMA32(vf[1][1], BF8(p1), o); SBAR();
            PRIO(1); EXP4(sc, 0); SBAR(); PRIO(2);
            EXP4(sc, 4); PACK4(p0, sc, 0); SBAR(); EXP4(sc, 8); EXP4(sc, 12); PACK4(p1, sc, 8); SBAR(); PRIO(3);
            sc = MFMA32(kf1[3], q1, zero16); SBAR();
            sc = MFMA32K8(kf2[3], q2, sc); SBAR();
            KLOAD(5);
            SBAR();
            o = MFMA32(vf[2][0], BF8(p0), o); SBAR();
            VLOAD(4);
            SBAR();
            o = MFMA32(vf[2][1], BF8(p1), o); SBAR();
            PRIO(1); EXP4(sc, 0); SBAR(); PRIO(2);
            EXP4(sc, 4); PACK4(p0, sc, 0); SBAR(); EXP4(sc, 8); EXP4(sc, 12); PACK4(p1, sc, 8); SBAR(); PRIO(3);
            sc = MFMA32(kf1[4], q1, zero16); SBAR();
            sc = MFMA32K8(kf2[4], q2, sc); SBAR();
            KLOAD(6);
            SBAR();
            o = MFMA32(vf[3][0], BF8(p0), o); SBAR();
            VLOAD(5);
            SBAR();
            o = MFMA32(vf[3][1], BF8(p1), o); SBAR();
            PRIO(1); EXP4(sc, 0); SBAR(); PRIO(2);
            EXP4(sc, 4); PACK4(p0, sc, 0); SBAR(); EXP4(sc, 8); EXP4(sc, 12); PACK4(p1, sc, 8); SBAR(); PRIO(3);
            sc = MFMA32(kf1[5], q1, zero16); SBAR();
            sc = MFMA32K8(kf2[5], q2, sc); SBAR();
            KLOAD(7);
            SBAR();
            o = MFMA32(vf[4][0], BF8(p0), o); SBAR();
            VLOAD(6);
            SBAR();
            o = MFMA32(vf[4][1], BF8(p1), o); SBAR();
            PRIO(1); EXP4(sc, 0); SBAR(); PRIO(2);
            EXP4(sc, 4); PACK4(p0, sc, 0); SBAR(); EXP4(sc, 8); EXP4(sc, 12); PACK4(p1, sc, 8); SBAR(); PRIO(3);
            sc = MFMA32(kf1[6], q1, zero16); SBAR();
            sc = MFMA32K8(kf2[6], q2, sc); SBAR();
            o = MFMA32(vf[5][0], BF8(p0), o); SBAR();
            VLOAD(7);
            SBAR();
            o = MFMA32(vf[5][1], BF8(p1), o); SBAR();
            PRIO(1); EXP4(sc, 0); SBAR(); PRIO(2);
            EXP4(sc, 4); PACK4(p0, sc, 0); SBAR(); EXP4(sc, 8); EXP4(sc, 12); PACK4(p1, sc, 8); SBAR(); PRIO(3);
            sc = MFMA32(kf1[7], q1, zero16); SBAR();
            sc = MFMA32K8(kf2[7], q2, sc); SBAR();
            o = MFMA32(vf[6][0], BF8(p0), o); SBAR();
            q1n = *(const bf16x8*)(smem + K2_QL + gn * 1536 + lane16); q2n = *(const bf16x4*)(q2v + gn * 1536);
            SBAR();
            o = MFMA32(vf[6][1], BF8(p1), o); SBAR();
            PRIO(1); EXP4(sc, 0); SBAR(); PRIO(2);
            EXP4(sc, 4); PACK4(p0, sc, 0); SBAR(); EXP4(sc, 8); EXP4(sc, 12); PACK4(p1, sc, 8); SBAR(); PRIO(3);
            sc = MFMA32(kf1[0], q1n, zero16); SBAR();
            sc = MFMA32K8(kf2[0], q2n, sc); SBAR();
            o = MFMA32(vf[7][0], BF8(p0), o); SBAR();
            o = MFMA32(vf[7][1], BF8(p1), o); SBAR();
            q1 = q1n; q2 = q2n;
            LSTAMP(1, 2);
        }
        {
            const int gn = 2;
            PHASE_LOCALS
            FRESH_IDS
            PRIO(1); EXP4(sc, 0); SBAR(); PRIO(2); WP_ADDR(0); WP_ST(0); SBAR();
            EXP4(sc, 4); SBAR(); WP_ST(1); SBAR(); PACK4(p0, sc, 0); EXP4(sc, 8); SBAR(); WP_ST(2); SBAR(); EXP4(sc, 12); PACK4(p1, sc, 8); SBAR(); PRIO(3);
            asm volatile("" : "+v"(p0), "+v"(p1)); if (w >= 4) LOOPBAR();
            sc = MFMA32(kf1[1], q1, zero16); SBAR();
            WARMXW();
            SBAR();
            sc = MFMA32K8(kf2[1], q2, sc); SBAR();
            OPJ(WARMDMA(0);)
            SBAR();
            o = MFMA32(vf[0][0], BF8(p0), zero16); SBAR();
            OPJ(WARMDMA(1);)
            SBAR();
            o = MFMA32(vf[0][1], BF8(p1), o); SBAR();
            asm volatile("" : "+v"(o), "+v"(sc)); if (w < 4) LOOPBAR();
            PRIO(1); EXP4(sc, 0); SBAR(); PRIO(2);
            EXP4(sc, 4); PACK4(p0, sc, 0); SBAR(); EXP4(sc, 8); EXP4(sc, 12); PACK4(p1, sc, 8); SBAR(); PRIO(3);
            sc = MFMA32(kf1[2], q1, zero16); SBAR();
            S2(STEP2A(0);)
            SBAR();
            sc = MFMA32K8(kf2[2], q2, sc); SBAR();
            OPJ(WARMDMA(2);)
            OPJ(WARMDMA(3);)
            SBAR();
            o = MFMA32(vf[1][0], BF8(p0), o); SBAR();
            OPJ(TAILDMA(0);)
            SBAR();
            o = MFMA32(vf[1][1], BF8(p1), o); SBAR();
            PRIO(1); EXP4(sc, 0); SBAR(); PRIO(2);
            EXP4(sc, 4); PACK4(p0, sc, 0); SBAR(); EXP4(sc, 8); EXP4(sc, 12); PACK4(p1, sc, 8); SBAR(); PRIO(3);
            sc = MFMA32(kf1[3], q1, zero16); SBAR();
            S2(STEP2B();)
            SBAR();
            sc = MFMA32K8(kf2[3], q2, sc); SBAR();
            OPJ(TAILDMA(1);)
            SBAR();
            o = MFMA32(vf[2][0], BF8(p0), o); SBAR();
            OPJ(TAILDMA(2);)
            SBAR();
            o = MFMA32(vf[2][1], BF8(p1), o); SBAR();
            PRIO(1); EXP4(sc, 0); SBAR(); PRIO(2);
            EXP4(sc, 4); PACK4(p0, sc, 0); SBAR(); EXP4(sc, 8); EXP4(sc, 12); PACK4(p1, sc, 8); SBAR(); PRIO(3);
            sc = MFMA32(kf1[4], q1, zero16); SBAR();
            S2(STEP2C(gn - 2);)
            SBAR();
            sc = MFMA32K8(kf2[4], q2, sc); SBAR();
            OPJ(TAILDMA(3);)
            SBAR();
            o = MFMA32(vf[3][0], BF8(p0), o); SBAR();
            OPJ(TAILDMA(4);)
            SBAR();
            o = MFMA32(vf[3][1], BF8(p1), o); SBAR();
            PRIO(1); EXP4(sc, 0); SBAR(); PRIO(2);
            EXP4(sc, 4); PACK4(p0, sc, 0); SBAR(); EXP4(sc, 8); EXP4(sc, 12); PACK4(p1, sc, 8); SBAR(); PRIO(3);
            sc = MFMA32(kf1[5], q1, zero16); SBAR();
            OPJ(TAILDMA(5);)
            SBAR();
            sc = MFMA32K8(kf2[5], q2, sc); SBAR();
            OPJ(TAILDMA(6);)
            SBAR();
            o = MFMA32(vf[4][0], BF8(p0), o); SBAR();
            OPJ(TAILDMA(7);)
            SBAR();
            o = MFMA32(vf[4][1], BF8(p1), o); SBAR();
            PRIO(1); EXP4(sc, 0); SBAR(); PRIO(2);
            EXP4(sc, 4); PACK4(p0, sc, 0); SBAR(); EXP4(sc, 8); EXP4(sc, 12); PACK4(p1, sc, 8); SBAR(); PRIO(3);
            sc = MFMA32(kf1[6], q1, zero16); SBAR();
            OPJ(TAILDMA(8);)
            SBAR();
            sc = MFMA32K8(kf2[6], q2, sc); SBAR();
            OPJ(TAILDMA(9);)
            SBAR();
            o = MFMA32(vf[5][0], BF8(p0), o); SBAR();
            OPJ(wa[0] = WOLOAD(0);)
            SBAR();
            o = MFMA32(vf[5][1], BF8(p1), o); SBAR();
            PRIO(1); EXP4(sc, 0); SBAR(); PRIO(2);
            EXP4(sc, 4); PACK4(p0, sc, 0); SBAR(); EXP4(sc, 8); EXP4(sc, 12); PACK4(p1, sc, 8); SBAR(); PRIO(3);
            sc = MFMA32(kf1[7], q1, zero16); SBAR();
            OPJ(wa[1] = WOLOAD(1024);)
            SBAR();
            sc = MFMA32K8(kf2[7], q2, sc); SBAR();
            OPJ(wa[2] = WOLOAD(2048);)
            SBAR();
            o = MFMA32(vf[6][0], BF8(p0), o); SBAR();
            q1n = *(const bf16x8*)(smem + K2_QL + gn * 1536 + lane16); q2n = *(const bf16x4*)(q2v + gn * 1536);
            SBAR();
            o = MFMA32(vf[6][1], BF8(p1), o); SBAR();
            PRIO(1); EXP4(sc, 0); SBAR(); PRIO(2);
            EXP4(sc, 4); PACK4(p0, sc, 0); SBAR(); EXP4(sc, 8); EXP4(sc, 12); PACK4(p1, sc, 8); SBAR(); PRIO(3);
            sc = MFMA32(kf1[0], q1n, zero16); SBAR();
            OPJ(wa[3] = WOLOAD(3072);)
            SBAR();
            sc = MFMA32K8(kf2[0], q2n, sc); SBAR();
            OPJ(wa[4] = WOLOAD(4096);)
            SBAR();
            o = MFMA32(vf[7][0], BF8(p0), o); SBAR();
            o = MFMA32(vf[7][1], BF8(p1), o); SBAR();
            q1 = q1n; q2 = q2n;
            LSTAMP(1, 3);
        }
        {
            const int gn = 3;
            PHASE_LOCALS
            FRESH_IDS
            PRIO(1); EXP4(sc, 0); SBAR(); PRIO(2); WP_ADDR(1); WP_ST(0); SBAR();
            EXP4(sc, 4); SBAR(); WP_ST(1); SBAR(); PACK4(p0, sc, 0); EXP4(sc, 8); SBAR(); WP_ST(2); SBAR(); EXP4(sc, 12); PACK4(p1, sc, 8); SBAR(); PRIO(3);
            asm volatile("" : "+v"(p0), "+v"(p1)); if (w >= 4) LOOPBAR();
            sc = MFMA32(kf1[1], q1, zero16); SBAR();
            sc = MFMA32K8(kf2[1], q2, sc); SBAR();
            o = MFMA32(vf[0][0], BF8(p0), zero16); SBAR();
            o = MFMA32(vf[0][1], BF8(p1), o); SBAR();
            asm volatile("" : "+v"(o), "+v"(sc)); if (w < 4) LOOPBAR();
            PRIO(1); EXP4(sc, 0); SBAR(); PRIO(2);
            EXP4(sc, 4); PACK4(p0, sc, 0); SBAR(); EXP4(sc, 8); EXP4(sc, 12); PACK4(p1, sc, 8); SBAR(); PRIO(3);
            sc = MFMA32(kf1[2], q1, zero16); SBAR();
            OPJ(ob = *(const bf16x8*)(otp + 0);)
            S2(STEP2A(1);)
            SBAR();
            sc = MFMA32K8(kf2[2], q2, sc); SBAR();
            o = MFMA32(vf[1][0], BF8(p0), o); SBAR();
            OPJ(oacc[0] = MFMA16(wa[0], ob, oacc[0]);)
            OPJ(wa[0] = WOLOAD(5120);)
            SBAR();
            o = MFMA32(vf[1][1], BF8(p1), o); SBAR();
            PRIO(1); EXP4(sc, 0); SBAR(); PRIO(2);
            EXP4(sc, 4); PACK4(p0, sc, 0); SBAR(); EXP4(sc, 8); EXP4(sc, 12); PACK4(p1, sc, 8); SBAR(); PRIO(3);
            sc = MFMA32(kf1[3], q1, zero16); SBAR();
            S2(STEP2B();)
            SBAR();
            sc = MFMA32K8(kf2[3], q2, sc); SBAR();
            o = MFMA32(vf[2][0], BF8(p0), o); SBAR();
            OPJ(oacc[1] = MFMA16(wa[1], ob, oacc[1]);)
            OPJ(wa[1] = WOLOAD(6144);)
            SBAR();
            o = MFMA32(vf[2][1], BF8(p1), o); SBAR();
            PRIO(1); EXP4(sc, 0); SBAR(); PRIO(2);
            EXP4(sc, 4); PACK4(p0, sc, 0); SBAR(); EXP4(sc, 8); EXP4(sc, 12); PACK4(p1, sc, 8); SBAR(); PRIO(3);
            sc = MFMA32(kf1[4], q1, zero16); SBAR();
            S2(STEP2C(gn - 2);)
            SBAR();
            sc = MFMA32K8(kf2[4], q2, sc); SBAR();
            o = MFMA32(vf[3][0], BF8(p0), o); SBAR();
            OPJ(oacc[2] = MFMA16(wa[2], ob, oacc[2]);)
            OPJ(wa[2] = WOLOAD(7168);)
            SBAR();
            o = MFMA32(vf[3][1], BF8(p1), o); SBAR();
            PRIO(1); EXP4(sc, 0); SBAR(); PRIO(2);
            EXP4(sc, 4); PACK4(p0, sc, 0); SBAR(); EXP4(sc, 8); EXP4(sc, 12); PACK4(p1, sc, 8); SBAR(); PRIO(3);
            sc = MFMA32(kf1[5], q1, zero16); SBAR();
            sc = MFMA32K8(kf2[5], q2, sc); SBAR();
            OPJ(oacc[3] = MFMA16(wa[3], ob, oacc[3]);)
            OPJ(wa[3] = WOLOAD(8192);)
            SBAR();
            o = MFMA32(vf[4][0], BF8(p0), o); SBAR();
            OPJ(oacc[4] = MFMA16(wa[4], ob, oacc[4]);)
            OPJ(wa[4] = WOLOAD(9216);)
            OPJ(ob = *(const bf16x8*)(otp + 512);)
            SBAR();
            o = MFMA32(vf[4][1], BF8(p1), o); SBAR();
            PRIO(1); EXP4(sc, 0); SBAR(); PRIO(2);
            EXP4(sc, 4); PACK4(p0, sc, 0); SBAR(); EXP4(sc, 8); EXP4(sc, 12); PACK4(p1, sc, 8); SBAR(); PRIO(3);
            sc = MFMA32(kf1[6], q1, zero16); SBAR();
            sc = MFMA32K8(kf2[6], q2, sc); SBAR();
            OPJ(oacc[0] = MFMA16(wa[0], ob, oacc[0]);)
            OPJ(wa[0] = WOLOAD(10240);)
            SBAR();
            o = MFMA32(vf[5][0], BF8(p0), o); SBAR();
            OPJ(oacc[1] = MFMA16(wa[1], ob, oacc[1]);)
            OPJ(wa[1] = WOLOAD(11264);)
            SBAR();
            o = MFMA32(vf[5][1], BF8(p1), o); SBAR();
            PRIO(1); EXP4(sc, 0); SBAR(); PRIO(2);
            EXP4(sc, 4); PACK4(p0, sc, 0); SBAR(); EXP4(sc, 8); EXP4(sc, 12); PACK4(p1, sc, 8); SBAR(); PRIO(3);
            sc = MFMA32(kf1[7], q1, zero16); SBAR();
            sc = MFMA32K8(kf2[7], q2, sc); SBAR();
            OPJ(oacc[2] = MFMA16(wa[2], ob, oacc[2]);)
            OPJ(wa[2] = WOLOAD(12288);)
            SBAR();
            o = MFMA32(vf[6][0], BF8(p0), o); SBAR();
            q1n = *(const bf16x8*)(smem + K2_QL + gn * 1536 + lane16); q2n = *(const bf16x4*)(q2v + gn * 1536);
            SBAR();
            o = MFMA32(vf[6][1], BF8(p1), o); SBAR();
            PRIO(1); EXP4(sc, 0); SBAR(); PRIO(2);
            EXP4(sc, 4); PACK4(p0, sc, 0); SBAR(); EXP4(sc, 8); EXP4(sc, 12); PACK4(p1, sc, 8); SBAR(); PRIO(3);
            sc = MFMA32(kf1[0], q1n, zero16); SBAR();
            sc = MFMA32K8(kf2[0], q2n, sc); SBAR();
            OPJ(oacc[3] = MFMA16(wa[3], ob, oacc[3]);)
            OPJ(wa[3] = WOLOAD(13312);)
            SBAR();
            o = MFMA32(vf[7][0], BF8(p0), o); SBAR();
            OPJ(oacc[4] = MFMA16(wa[4], ob, oacc[4]);)
            OPJ(wa[4] = WOLOAD(14336);)
            SBAR();
            o = MFMA32(vf[7][1], BF8(p1), o); SBAR();
            q1 = q1n; q2 = q2n;
            LSTAMP(1, 4);
        }
        {
            const int gn = 4;
            PHASE_LOCALS
            FRESH_IDS
            PRIO(1); EXP4(sc, 0); SBAR(); PRIO(2); WP_ADDR(0); WP_ST(0); SBAR();
            EXP4(sc, 4); SBAR(); WP_ST(1); SBAR(); PACK4(p0, sc, 0); EXP4(sc, 8); SBAR(); WP_ST(2); SBAR(); EXP4(sc, 12); PACK4(p1, sc, 8); SBAR(); PRIO(3);
            asm volatile("" : "+v"(p0), "+v"(p1)); if (w >= 4) LOOPBAR();
            sc = MFMA32(kf1[1], q1, zero16); SBAR();
            sc = MFMA32K8(kf2[1], q2, sc); SBAR();
            o = MFMA32(vf[0][0], BF8(p0), zero16); SBAR();
            o = MFMA32(vf[0][1], BF8(p1), o); SBAR();
            asm volatile("" : "+v"(o), "+v"(sc)); if (w < 4) LOOPBAR();
            PRIO(1); EXP4(sc, 0); SBAR(); PRIO(2);
            EXP4(sc, 4); PACK4(p0, sc, 0); SBAR(); EXP4(sc, 8); EXP4(sc, 12); PACK4(p1, sc, 8); SBAR(); PRIO(3);
            sc = MFMA32(kf1[2], q1, zero16); SBAR();
            OPJ(ob = *(const bf16x8*)(otp + 1024);)
            S2(STEP2A(0);)
            SBAR();
            sc = MFMA32K8(kf2[2], q2, sc); SBAR();
            o = MFMA32(vf[1][0], BF8(p0), o); SBAR();
            OPJ(oacc[0] = MFMA16(wa[0], ob, oacc[0]);)
            OPJ(wa[0] = WOLOAD(15360);)
            SBAR();
            o = MFMA32(vf[1][1], BF8(p1), o); SBAR();
            PRIO(1); EXP4(sc, 0); SBAR(); PRIO(2);
            EXP4(sc, 4); PACK4(p0, sc, 0); SBAR(); EXP4(sc, 8); EXP4(sc, 12); PACK4(p1, sc, 8); SBAR(); PRIO(3);
            sc = MFMA32(kf1[3], q1, zero16); SBAR();
            S2(STEP2B();)
            SBAR();
            sc = MFMA32K8(kf2[3], q2, sc); SBAR();
            OPJ(oacc[1] = MFMA16(wa[1], ob, oacc[1]);)
            OPJ(wa[1] = WOLOAD(16384);)
            SBAR();
            o = MFMA32(vf[2][0], BF8(p0), o); SBAR();
            OPJ(oacc[2] = MFMA16(wa[2], ob, oacc[2]);)
            OPJ(wa[2] = WOLOAD(17408);)
            SBAR();
            o = MFMA32(vf[2][1], BF8(p1), o); SBAR();
            PRIO(1); EXP4(sc, 0); SBAR(); PRIO(2);
            EXP4(sc, 4); PACK4(p0, sc, 0); SBAR(); EXP4(sc, 8); EXP4(sc, 12); PACK4(p1, sc, 8); SBAR(); PRIO(3);
            sc = MFMA32(kf1[4], q1, zero16); SBAR();
            S2(STEP2C(gn - 2);)
            SBAR();
            sc = MFMA32K8(kf2[4], q2, sc); SBAR();
            OPJ(oacc[3] = MFMA16(wa[3], ob, oacc[3]);)
            OPJ(wa[3] = WOLOAD(18432);)
            SBAR();
            o = MFMA32(vf[3][0], BF8(p0), o); SBAR();
            OPJ(oacc[4] = MFMA16(wa[4], ob, oacc[4]);)
            OPJ(wa[4] = WOLOAD(19456);)
            OPJ(ob = *(const bf16x8*)(otp + 1536);)
            SBAR();
            o = MFMA32(vf[3][1], BF8(p1), o); SBAR();
            PRIO(1); EXP4(sc, 0); SBAR(); PRIO(2);
            EXP4(sc, 4); PACK4(p0, sc, 0); SBAR(); EXP4(sc, 8); EXP4(sc, 12); PACK4(p1, sc, 8); SBAR(); PRIO(3);
            sc = MFMA32(kf1[5], q1, zero16); SBAR();
            OPJ(oacc[0] = MFMA16(wa[0], ob, oacc[0]);)
            OPJ(wa[0] = WOLOAD(20480);)
            SBAR();
            sc = MFMA32K8(kf2[5], q2, sc); SBAR();
            OPJ(oacc[1] = MFMA16(wa[1], ob, oacc[1]);)
            OPJ(wa[1] = WOLOAD(21504);)
            SBAR();
            o = MFMA32(vf[4][0], BF8(p0), o); SBAR();
            o = MFMA32(vf[4][1], BF8(p1), o); SBAR();
            PRIO(1); EXP4(sc, 0); SBAR(); PRIO(2);
            EXP4(sc, 4); PACK4(p0, sc, 0); SBAR(); EXP4(sc, 8); EXP4(sc, 12); PACK4(p1, sc, 8); SBAR(); PRIO(3);
            sc = MFMA32(kf1[6], q1, zero16); SBAR();
            OPJ(oacc[2] = MFMA16(wa[2], ob, oacc[2]);)
            OPJ(wa[2] = WOLOAD(22528);)
            SBAR();
            sc = MFMA32K8(kf2[6], q2, sc); SBAR();
            OPJ(oacc[3] = MFMA16(wa[3], ob, oacc[3]);)
            OPJ(wa[3] = WOLOAD(23552);)
            SBAR();
            o = MFMA32(vf[5][0], BF8(p0), o); SBAR();
            OPJ(oacc[4] = MFMA16(wa[4], ob, oacc[4]);)
            OPJ(wa[4] = WOLOAD(24576);)
            OPJ(ob = *(const bf16x8*)(otp + 2048);)
            SBAR();
            o = MFMA32(vf[5][1], BF8(p1), o); SBAR();
            PRIO(1); EXP4(sc, 0); SBAR(); PRIO(2);
            EXP4(sc, 4); PACK4(p0, sc, 0); SBAR(); EXP4(sc, 8); EXP4(sc, 12); PACK4(p1, sc, 8); SBAR(); PRIO(3);
            sc = MFMA32(kf1[7], q1, zero16); SBAR();
            OPJ(oacc[0] = MFMA16(wa[0], ob, oacc[0]);)
            OPJ(wa[0] = WOLOAD(25600);)
            SBAR();
            sc = MFMA32K8(kf2[7], q2, sc); SBAR();
            OPJ(oacc[1] = MFMA16(wa[1], ob, oacc[1]);)
            OPJ(wa[1] = WOLOAD(26624);)
            SBAR();
            o = MFMA32(vf[6][0], BF8(p0), o); SBAR();
            q1n = *(const bf16x8*)(smem + K2_QL + gn * 1536 + lane16); q2n = *(const bf16x4*)(q2v + gn * 1536);
            SBAR();
            o = MFMA32(vf[6][1], BF8(p1), o); SBAR();
            PRIO(1); EXP4(sc, 0); SBAR(); PRIO(2);
            EXP4(sc, 4); PACK4(p0, sc, 0); SBAR(); EXP4(sc, 8); EXP4(sc, 12); PACK4(p1, sc, 8); SBAR(); PRIO(3);
            sc = MFMA32(kf1[0], q1n, zero16); SBAR();
            OPJ(oacc[2] = MFMA16(wa[2], ob, oacc[2]);)
            OPJ(wa[2] = WOLOAD(27648);)
            SBAR();
            sc = MFMA32K8(kf2[0], q2n, sc); SBAR();
            OPJ(oacc[3] = MFMA16(wa[3], ob, oacc[3]);)
            OPJ(wa[3] = WOLOAD(28672);)
            SBAR();
            o = MFMA32(vf[7][0], BF8(p0), o); SBAR();
            OPJ(oacc[4] = MFMA16(wa[4], ob, oacc[4]);)
            OPJ(wa[4] = WOLOAD(29696);)
            SBAR();
            o = MFMA32(vf[7][1], BF8(p1), o); SBAR();
            q1 = q1n; q2 = q2n;
            LSTAMP(1, 5);
        }
        {
            const int gn = 5;
            PHASE_LOCALS
            FRESH_IDS
            PRIO(1); EXP4(sc, 0); SBAR(); PRIO(2); WP_ADDR(1); WP_ST(0); SBAR();
            EXP4(sc, 4); SBAR(); WP_ST(1); SBAR(); PACK4(p0, sc, 0); EXP4(sc, 8); SBAR(); WP_ST(2); SBAR(); EXP4(sc, 12); PACK4(p1, sc, 8); SBAR(); PRIO(3);
            asm volatile("" : "+v"(p0), "+v"(p1)); if (w >= 4) LOOPBAR();
            sc = MFMA32(kf1[1], q1, zero16); SBAR();
            sc = MFMA32K8(kf2[1], q2, sc); SBAR();
            o = MFMA32(vf[0][0], BF8(p0), zero16); SBAR();
            o = MFMA32(vf[0][1], BF8(p1), o); SBAR();
            asm volatile("" : "+v"(o), "+v"(sc)); if (w < 4) LOOPBAR();
            PRIO(1); EXP4(sc, 0); SBAR(); PRIO(2);
            EXP4(sc, 4); PACK4(p0, sc, 0); SBAR(); EXP4(sc, 8); EXP4(sc, 12); PACK4(p1, sc, 8); SBAR(); PRIO(3);
            sc = MFMA32(kf1[2], q1, zero16); SBAR();
            OPJ(ob = *(const bf16x8*)(otp + 2560);)
            S2(STEP2A(1);)
            SBAR();
            sc = MFMA32K8(kf2[2], q2, sc); SBAR();
            o = MFMA32(vf[1][0], BF8(p0), o); SBAR();
            OPJ(oacc[0] = MFMA16(wa[0], ob, oacc[0]);)
            OPJ(wa[0] = WOLOAD(30720);)
            SBAR();
            o = MFMA32(vf[1][1], BF8(p1), o); SBAR();
            PRIO(1); EXP4(sc, 0); SBAR(); PRIO(2);
            EXP4(sc, 4); PACK4(p0, sc, 0); SBAR(); EXP4(sc, 8); EXP4(sc, 12); PACK4(p1, sc, 8); SBAR(); PRIO(3);
            sc = MFMA32(kf1[3], q1, zero16); SBAR();
            S2(STEP2B();)
            SBAR();
            sc = MFMA32K8(kf2[3], q2, sc); SBAR();
            o = MFMA32(vf[2][0], BF8(p0), o); SBAR();
            OPJ(oacc[1] = MFMA16(wa[1], ob, oacc[1]);)
            OPJ(wa[1] = WOLOAD(31744);)
            SBAR();
            o = MFMA32(vf[2][1], BF8(p1), o); SBAR();
            PRIO(1); EXP4(sc, 0); SBAR(); PRIO(2);
            EXP4(sc, 4); PACK4(p0, sc, 0); SBAR(); EXP4(sc, 8); EXP4(sc, 12); PACK4(p1, sc, 8); SBAR(); PRIO(3);
            sc = MFMA32(kf1[4], q1, zero16); SBAR();
            S2(STEP2C(gn - 2);)
            SBAR();
            sc = MFMA32K8(kf2[4], q2, sc); SBAR();
            o = MFMA32(vf[3][0], BF8(p0), o); SBAR();
            OPJ(oacc[2] = MFMA16(wa[2], ob, oacc[2]);)
            OPJ(wa[2] = WOLOAD(32768);)
            SBAR();
            o = MFMA32(vf[3][1], BF8(p1), o); SBAR();
            PRIO(1); EXP4(sc, 0); SBAR(); PRIO(2);
            EXP4(sc, 4); PACK4(p0, sc, 0); SBAR(); EXP4(sc, 8); EXP4(sc, 12); PACK4(p1, sc, 8); SBAR(); PRIO(3);
            sc = MFMA32(kf1[5], q1, zero16); SBAR();
            sc = MFMA32K8(kf2[5], q2, sc); SBAR();
            OPJ(oacc[3] = MFMA16(wa[3], ob, oacc[3]);)
            OPJ(wa[3] = WOLOAD(33792);)
            SBAR();
            o = MFMA32(vf[4][0], BF8(p0), o); SBAR();
            OPJ(oacc[4] = MFMA16(wa[4], ob, oacc[4]);)
            OPJ(wa[4] = WOLOAD(34816);)
            OPJ(ob = *(const bf16x8*)(otp + 3072);)
            SBAR();
            o = MFMA32(vf[4][1], BF8(p1), o); SBAR();
            PRIO(1); EXP4(sc, 0); SBAR(); PRIO(2);
            EXP4(sc, 4); PACK4(p0, sc, 0); SBAR(); EXP4(sc, 8); EXP4(sc, 12); PACK4(p1, sc, 8); SBAR(); PRIO(3);
            sc = MFMA32(kf1[6], q1, zero16); SBAR();
            sc = MFMA32K8(kf2[6], q2, sc); SBAR();
            OPJ(oacc[0] = MFMA16(wa[0], ob, oacc[0]);)
            OPJ(wa[0] = WOLOAD(35840);)
            SBAR();
            o = MFMA32(vf[5][0], BF8(p0), o); SBAR();
            OPJ(oacc[1] = MFMA16(wa[1], ob, oacc[1]);)
            OPJ(wa[1] = WOLOAD(36864);)
            SBAR();
            o = MFMA32(vf[5][1], BF8(p1), o); SBAR();
            PRIO(1); EXP4(sc, 0); SBAR(); PRIO(2);
            EXP4(sc, 4); PACK4(p0, sc, 0); SBAR(); EXP4(sc, 8); EXP4(sc, 12); PACK4(p1, sc, 8); SBAR(); PRIO(3);
            sc = MFMA32(kf1[7], q1, zero16); SBAR();
            sc = MFMA32K8(kf2[7], q2, sc); SBAR();
            OPJ(oacc[2] = MFMA16(wa[2], ob, oacc[2]);)
            OPJ(wa[2] = WOLOAD(37888);)
            SBAR();
            o = MFMA32(vf[6][0], BF8(p0), o); SBAR();
            q1n = *(const bf16x8*)(smem + K2_QL + gn * 1536 + lane16); q2n = *(const bf16x4*)(q2v + gn * 1536);
            SBAR();
            o = MFMA32(vf[6][1], BF8(p1), o); SBAR();
            PRIO(1); EXP4(sc, 0); SBAR(); PRIO(2);
            EXP4(sc, 4); PACK4(p0, sc, 0); SBAR(); EXP4(sc, 8); EXP4(sc, 12); PACK4(p1, sc, 8); SBAR(); PRIO(3);
            sc = MFMA32(kf1[0], q1n, zero16); SBAR();
            sc = MFMA32K8(kf2[0], q2n, sc); SBAR();
            OPJ(oacc[3] = MFMA16(wa[3], ob, oacc[3]);)
            OPJ(wa[3] = WOLOAD(38912);)
            SBAR();
            o = MFMA32(vf[7][0], BF8(p0), o); SBAR();
            OPJ(oacc[4] = MFMA16(wa[4], ob, oacc[4]);)
            OPJ(wa[4] = WOLOAD(39936);)
            SBAR();
            o = MFMA32(vf[7][1], BF8(p1), o); SBAR();
            q1 = q1n; q2 = q2n;
            LSTAMP(1, 6);
        }
        {
            const int gn = 6;
            PHASE_LOCALS
            FRESH_IDS
            PRIO(1); EXP4(sc, 0); SBAR(); PRIO(2); WP_ADDR(0); WP_ST(0); SBAR();
            EXP4(sc, 4); SBAR(); WP_ST(1); SBAR(); PACK4(p0, sc, 0); EXP4(sc, 8); SBAR(); WP_ST(2); SBAR(); EXP4(sc, 12); PACK4(p1, sc, 8); SBAR(); PRIO(3);
            asm volatile("" : "+v"(p0), "+v"(p1)); if (w >= 4) LOOPBAR();
            sc = MFMA32(kf1[1], q1, zero16); SBAR();
            sc = MFMA32K8(kf2[1], q2, sc); SBAR();
            o = MFMA32(vf[0][0], BF8(p0), zero16); SBAR();
            o = MFMA32(vf[0][1], BF8(p1), o); SBAR();
            asm volatile("" : "+v"(o), "+v"(sc)); if (w < 4) LOOPBAR();
            PRIO(1); EXP4(sc, 0); SBAR(); PRIO(2);
            EXP4(sc, 4); PACK4(p0, sc, 0); SBAR(); EXP4(sc, 8); EXP4(sc, 12); PACK4(p1, sc, 8); SBAR(); PRIO(3);
            sc = MFMA32(kf1[2], q1, zero16); SBAR();
            OPJ(ob = *(const bf16x8*)(otp + 3584);)
            S2(STEP2A(0);)
            SBAR();
            sc = MFMA32K8(kf2[2], q2, sc); SBAR();
            o = MFMA32(vf[1][0], BF8(p0), o); SBAR();
            OPJ(oacc[0] = MFMA16(wa[0], ob, oacc[0]);)
            OPJ(wa[0] = WOLOAD(40960);)
            SBAR();
            o = MFMA32(vf[1][1], BF8(p1), o); SBAR();
            PRIO(1); EXP4(sc, 0); SBAR(); PRIO(2);
            EXP4(sc, 4); PACK4(p0, sc, 0); SBAR(); EXP4(sc, 8); EXP4(sc, 12); PACK4(p1, sc, 8); SBAR(); PRIO(3);
            sc = MFMA32(kf1[3], q1, zero16); SBAR();
            S2(STEP2B();)
            SBAR();
            sc = MFMA32K8(kf2[3], q2, sc); SBAR();
            OPJ(oacc[1] = MFMA16(wa[1], ob, oacc[1]);)
            OPJ(wa[1] = WOLOAD(41984);)
            SBAR();
            o = MFMA32(vf[2][0], BF8(p0), o); SBAR();
            OPJ(oacc[2] = MFMA16(wa[2], ob, oacc[2]);)
            OPJ(wa[2] = WOLOAD(43008);)
            SBAR();
            o = MFMA32(vf[2][1], BF8(p1), o); SBAR();
            PRIO(1); EXP4(sc, 0); SBAR(); PRIO(2);
            EXP4(sc, 4); PACK4(p0, sc, 0); SBAR(); EXP4(sc, 8); EXP4(sc, 12); PACK4(p1, sc, 8); SBAR(); PRIO(3);
            sc = MFMA32(kf1[4], q1, zero16); SBAR();
            S2(STEP2C(gn - 2);)
            SBAR();
            sc = MFMA32K8(kf2[4], q2, sc); SBAR();
            OPJ(oacc[3] = MFMA16(wa[3], ob, oacc[3]);)
            OPJ(wa[3] = WOLOAD(44032);)
            SBAR();
            o = MFMA32(vf[3][0], BF8(p0), o); SBAR();
            OPJ(oacc[4] = MFMA16(wa[4], ob, oacc[4]);)
            OPJ(wa[4] = WOLOAD(45056);)
            OPJ(ob = *(const bf16x8*)(otp + 4096);)
            SBAR();
            o = MFMA32(vf[3][1], BF8(p1), o); SBAR();
            PRIO(1); EXP4(sc, 0); SBAR(); PRIO(2);
            EXP4(sc, 4); PACK4(p0, sc, 0); SBAR(); EXP4(sc, 8); EXP4(sc, 12); PACK4(p1, sc, 8); SBAR(); PRIO(3);
            sc = MFMA32(kf1[5], q1, zero16); SBAR();
            OPJ(oacc[0] = MFMA16(wa[0], ob, oacc[0]);)
            OPJ(wa[0] = WOLOAD(46080);)
            SBAR();
            sc = MFMA32K8(kf2[5], q2, sc); SBAR();
            OPJ(oacc[1] = MFMA16(wa[1], ob, oacc[1]);)
            OPJ(wa[1] = WOLOAD(47104);)
            SBAR();
            o = MFMA32(vf[4][0], BF8(p0), o); SBAR();
            o = MFMA32(vf[4][1], BF8(p1), o); SBAR();
            PRIO(1); EXP4(sc, 0); SBAR(); PRIO(2);
            EXP4(sc, 4); PACK4(p0, sc, 0); SBAR(); EXP4(sc, 8); EXP4(sc, 12); PACK4(p1, sc, 8); SBAR(); PRIO(3);
            sc = MFMA32(kf1[6], q1, zero16); SBAR();
            OPJ(oacc[2] = MFMA16(wa[2], ob, oacc[2]);)
            OPJ(wa[2] = WOLOAD(48128);)
            SBAR();
            sc = MFMA32K8(kf2[6], q2, sc); SBAR();
            OPJ(oacc[3] = MFMA16(wa[3], ob, oacc[3]);)
            OPJ(wa[3] = WOLOAD(49152);)
            SBAR();
            o = MFMA32(vf[5][0], BF8(p0), o); SBAR();
            OPJ(oacc[4] = MFMA16(wa[4], ob, oacc[4]);)
            OPJ(wa[4] = WOLOAD(50176);)
            OPJ(ob = *(const bf16x8*)(otp + 4608);)
            SBAR();
            o = MFMA32(vf[5][1], BF8(p1), o); SBAR();
            PRIO(1); EXP4(sc, 0); SBAR(); PRIO(2);
            EXP4(sc, 4); PACK4(p0, sc, 0); SBAR(); EXP4(sc, 8); EXP4(sc, 12); PACK4(p1, sc, 8); SBAR(); PRIO(3);
            sc = MFMA32(kf1[7], q1, zero16); SBAR();
            OPJ(oacc[0] = MFMA16(wa[0], ob, oacc[0]);)
            OPJ(wa[0] = WOLOAD(51200);)
            SBAR();
            sc = MFMA32K8(kf2[7], q2, sc); SBAR();
            OPJ(oacc[1] = MFMA16(wa[1], ob, oacc[1]);)
            OPJ(wa[1] = WOLOAD(52224);)
            SBAR();
            o = MFMA32(vf[6][0], BF8(p0), o); SBAR();
            q1n = *(const bf16x8*)(smem + K2_QL + gn * 1536 + lane16); q2n = *(const bf16x4*)(q2v + gn * 1536);
            SBAR();
            o = MFMA32(vf[6][1], BF8(p1), o); SBAR();
            PRIO(1); EXP4(sc, 0); SBAR(); PRIO(2);
            EXP4(sc, 4); PACK4(p0, sc, 0); SBAR(); EXP4(sc, 8); EXP4(sc, 12); PACK4(p1, sc, 8); SBAR(); PRIO(3);
            sc = MFMA32(kf1[0], q1n, zero16); SBAR();
            OPJ(oacc[2] = MFMA16(wa[2], ob, oacc[2]);)
            OPJ(wa[2] = WOLOAD(53248);)
            SBAR();
            sc = MFMA32K8(kf2[0], q2n, sc); SBAR();
            OPJ(oacc[3] = MFMA16(wa[3], ob, oacc[3]);)
            OPJ(wa[3] = WOLOAD(54272);)
            SBAR();
            o = MFMA32(vf[7][0], BF8(p0), o); SBAR();
            OPJ(oacc[4] = MFMA16(wa[4], ob, oacc[4]);)
            OPJ(wa[4] = WOLOAD(55296);)
            SBAR();
            o = MFMA32(vf[7][1], BF8(p1), o); SBAR();
            q1 = q1n; q2 = q2n;
            LSTAMP(1, 7);
        }
        {
            const int gn = 7;
            PHASE_LOCALS
            FRESH_IDS
            PRIO(1); EXP4(sc, 0); SBAR(); PRIO(2); WP_ADDR(1); WP_ST(0); SBAR();
            EXP4(sc, 4); SBAR(); WP_ST(1); SBAR(); PACK4(p0, sc, 0); EXP4(sc, 8); SBAR(); WP_ST(2); SBAR(); EXP4(sc, 12); PACK4(p1, sc, 8); SBAR(); PRIO(3);
            asm volatile("" : "+v"(p0), "+v"(p1)); if (w >= 4) LOOPBAR();
            sc = MFMA32(kf1[1], q1, zero16); SBAR();
            sc = MFMA32K8(kf2[1], q2, sc); SBAR();
            o = MFMA32(vf[0][0], BF8(p0), zero16); SBAR();
            o = MFMA32(vf[0][1], BF8(p1), o); SBAR();
            asm volatile("" : "+v"(o), "+v"(sc)); if (w < 4) LOOPBAR();
            PRIO(1); EXP4(sc, 0); SBAR(); PRIO(2);
            EXP4(sc, 4); PACK4(p0, sc, 0); SBAR(); EXP4(sc, 8); EXP4(sc, 12); PACK4(p1, sc, 8); SBAR(); PRIO(3);
            sc = MFMA32(kf1[2], q1, zero16); SBAR();
            OPJ(ob = *(const bf16x8*)(otp + 5120);)
            S2(STEP2A(1);)
            SBAR();
            sc = MFMA32K8(kf2[2], q2, sc); SBAR();
            o = MFMA32(vf[1][0], BF8(p0), o); SBAR();
            OPJ(oacc[0] = MFMA16(wa[0], ob, oacc[0]);)
            OPJ(wa[0] = WOLOAD(56320);)
            SBAR();
            o = MFMA32(vf[1][1], BF8(p1), o); SBAR();
            PRIO(1); EXP4(sc, 0); SBAR(); PRIO(2);
            EXP4(sc, 4); PACK4(p0, sc, 0); SBAR(); EXP4(sc, 8); EXP4(sc, 12); PACK4(p1, sc, 8); SBAR(); PRIO(3);
            sc = MFMA32(kf1[3], q1, zero16); SBAR();
            S2(STEP2B();)
            SBAR();
            sc = MFMA32K8(kf2[3], q2, sc); SBAR();
            o = MFMA32(vf[2][0], BF8(p0), o); SBAR();
            OPJ(oacc[1] = MFMA16(wa[1], ob, oacc[1]);)
            OPJ(wa[1] = WOLOAD(57344);)
            SBAR();
            o = MFMA32(vf[2][1], BF8(p1), o); SBAR();
            PRIO(1); EXP4(sc, 0); SBAR(); PRIO(2);
            EXP4(sc, 4); PACK4(p0, sc, 0); SBAR(); EXP4(sc, 8); EXP4(sc, 12); PACK4(p1, sc, 8); SBAR(); PRIO(3);
            sc = MFMA32(kf1[4], q1, zero16); SBAR();
            S2(STEP2C(gn - 2);)
            SBAR();
            sc = MFMA32K8(kf2[4], q2, sc); SBAR();
            o = MFMA32(vf[3][0], BF8(p0), o); SBAR();
            OPJ(oacc[2] = MFMA16(wa[2], ob, oacc[2]);)
            OPJ(wa[2] = WOLOAD(58368);)
            SBAR();
            o = MFMA32(vf[3][1], BF8(p1), o); SBAR();
            PRIO(1); EXP4(sc, 0); SBAR(); PRIO(2);
            EXP4(sc, 4); PACK4(p0, sc, 0); SBAR(); EXP4(sc, 8); EXP4(sc, 12); PACK4(p1, sc, 8); SBAR(); PRIO(3);
            sc = MFMA32(kf1[5], q1, zero16); SBAR();
            sc = MFMA32K8(kf2[5], q2, sc); SBAR();
            OPJ(oacc[3] = MFMA16(wa[3], ob, oacc[3]);)
            OPJ(wa[3] = WOLOAD(59392);)
            SBAR();
            o = MFMA32(vf[4][0], BF8(p0), o); SBAR();
            OPJ(oacc[4] = MFMA16(wa[4], ob, oacc[4]);)
            OPJ(wa[4] = WOLOAD(60416);)
            OPJ(ob = *(const bf16x8*)(otp + 5632);)
            SBAR();
            o = MFMA32(vf[4][1], BF8(p1), o); SBAR();
            PRIO(1); EXP4(sc, 0); SBAR(); PRIO(2);
            EXP4(sc, 4); PACK4(p0, sc, 0); SBAR(); EXP4(sc, 8); EXP4(sc, 12); PACK4(p1, sc, 8); SBAR(); PRIO(3);
            sc = MFMA32(kf1[6], q1, zero16); SBAR();
            sc = MFMA32K8(kf2[6], q2, sc); SBAR();
            OPJ(oacc[0] = MFMA16(wa[0], ob, oacc[0]);)
            OPJ(wa[0] = WOLOAD(61440);)
            SBAR();
            o = MFMA32(vf[5][0], BF8(p0), o); SBAR();
            OPJ(oacc[1] = MFMA16(wa[1], ob, oacc[1]);)
            OPJ(wa[1] = WOLOAD(62464);)
            SBAR();
            o = MFMA32(vf[5][1], BF8(p1), o); SBAR();
            PRIO(1); EXP4(sc, 0); SBAR(); PRIO(2);
            EXP4(sc, 4); PACK4(p0, sc, 0); SBAR(); EXP4(sc, 8); EXP4(sc, 12); PACK4(p1, sc, 8); SBAR(); PRIO(3);
            sc = MFMA32(kf1[7], q1, zero16); SBAR();
            sc = MFMA32K8(kf2[7], q2, sc); SBAR();
            OPJ(oacc[2] = MFMA16(wa[2], ob, oacc[2]);)
            OPJ(wa[2] = WOLOAD(63488);)
            SBAR();
            o = MFMA32(vf[6][0], BF8(p0), o); SBAR();
            q1n = *(const bf16x8*)(smem + K2_QL + gn * 1536 + lane16); q2n = *(const bf16x4*)(q2v + gn * 1536);
            SBAR();
            o = MFMA32(vf[6][1], BF8(p1), o); SBAR();
            PRIO(1); EXP4(sc, 0); SBAR(); PRIO(2);
            EXP4(sc, 4); PACK4(p0, sc, 0); SBAR(); EXP4(sc, 8); EXP4(sc, 12); PACK4(p1, sc, 8); SBAR(); PRIO(3);
            sc = MFMA32(kf1[0], q1n, zero16); SBAR();
            sc = MFMA32K8(kf2[0], q2n, sc); SBAR();
            OPJ(oacc[3] = MFMA16(wa[3], ob, oacc[3]);)
            OPJ(wa[3] = WOLOAD(64512);)
            SBAR();
            o = MFMA32(vf[7][0], BF8(p0), o); SBAR();
            OPJ(oacc[4] = MFMA16(wa[4], ob, oacc[4]);)
            OPJ(wa[4] = WOLOAD(65536);)
            SBAR();
            o = MFMA32(vf[7][1], BF8(p1), o); SBAR();
            q1 = q1n; q2 = q2n;
            LSTAMP(1, 8);
        }

        bf16x8 wl[10];
        {
            const int gn = 8;
            PHASE_LOCALS
            PRIO(1); EXP4(sc, 0); SBAR(); PRIO(2); WP_ADDR(0); WP_ST(0); SBAR();
            EXP4(sc, 4); SBAR(); WP_ST(1); SBAR(); PACK4(p0, sc, 0); EXP4(sc, 8); SBAR(); WP_ST(2); SBAR(); EXP4(sc, 12); PACK4(p1, sc, 8); SBAR(); PRIO(3);
            asm volatile("" : "+v"(p0), "+v"(p1)); if (w >= 4) LOOPBAR();
            sc = MFMA32(kf1[1], q1, zero16); SBAR();
            sc = MFMA32K8(kf2[1], q2, sc); SBAR();
            o = MFMA32(vf[0][0], BF8(p0), zero16); SBAR();
            o = MFMA32(vf[0][1], BF8(p1), o); SBAR();
            asm volatile("" : "+v"(o), "+v"(sc)); if (w < 4) LOOPBAR();
            PRIO(1); EXP4(sc, 0); SBAR(); PRIO(2);
            EXP4(sc, 4); PACK4(p0, sc, 0); SBAR(); EXP4(sc, 8); EXP4(sc, 12); PACK4(p1, sc, 8); SBAR(); PRIO(3);
            sc = MFMA32(kf1[2], q1, zero16); SBAR();
            OPJ(ob = *(const bf16x8*)(otp + 6144);)
            S2(STEP2A(0);)
            SBAR();
            sc = MFMA32K8(kf2[2], q2, sc); SBAR();
            o = MFMA32(vf[1][0], BF8(p0), o); SBAR();
            OPJ(oacc[0] = MFMA16(wa[0], ob, oacc[0]);)
            OPJ(wa[0] = WOLOAD(66560);)
            SBAR();
            o = MFMA32(vf[1][1], BF8(p1), o); SBAR();
            PRIO(1); EXP4(sc, 0); SBAR(); PRIO(2);
            EXP4(sc, 4); PACK4(p0, sc, 0); SBAR(); EXP4(sc, 8); EXP4(sc, 12); PACK4(p1, sc, 8); SBAR(); PRIO(3);
            sc = MFMA32(kf1[3], q1, zero16); SBAR();
            S2(STEP2B();)
            wl[0] = WOLOAD(81920);
            SBAR();
            sc = MFMA32K8(kf2[3], q2, sc); SBAR();
            OPJ(oacc[1] = MFMA16(wa[1], ob, oacc[1]);)
            OPJ(wa[1] = WOLOAD(67584);)
            SBAR();
            o = MFMA32(vf[2][0], BF8(p0), o); SBAR();
            OPJ(oacc[2] = MFMA16(wa[2], ob, oacc[2]);)
            OPJ(wa[2] = WOLOAD(68608);)
            SBAR();
            o = MFMA32(vf[2][1], BF8(p1), o); SBAR();
            PRIO(1); EXP4(sc, 0); SBAR(); PRIO(2);
            EXP4(sc, 4); PACK4(p0, sc, 0); SBAR(); EXP4(sc, 8); EXP4(sc, 12); PACK4(p1, sc, 8); SBAR(); PRIO(3);
            sc = MFMA32(kf1[4], q1, zero16); SBAR();
            S2(STEP2C(gn - 2);)
            wl[1] = WOLOAD(82944);
            SBAR();
            sc = MFMA32K8(kf2[4], q2, sc); SBAR();
            OPJ(oacc[3] = MFMA16(wa[3], ob, oacc[3]);)
            OPJ(wa[3] = WOLOAD(69632);)
            SBAR();
            o = MFMA32(vf[3][0], BF8(p0), o); SBAR();
            OPJ(oacc[4] = MFMA16(wa[4], ob, oacc[4]);)
            OPJ(wa[4] = WOLOAD(70656);)
            OPJ(ob = *(const bf16x8*)(otp + 6656);)
            wl[2] = WOLOAD(83968);
            SBAR();
            o = MFMA32(vf[3][1], BF8(p1), o); SBAR();
            PRIO(1); EXP4(sc, 0); SBAR(); PRIO(2);
            EXP4(sc, 4); PACK4(p0, sc, 0); SBAR(); EXP4(sc, 8); EXP4(sc, 12); PACK4(p1, sc, 8); SBAR(); PRIO(3);
            sc = MFMA32(kf1[5], q1, zero16); SBAR();
            OPJ(oacc[0] = MFMA16(wa[0], ob, oacc[0]);)
            OPJ(wa[0] = WOLOAD(71680);)
            wl[3] = WOLOAD(84992);
            SBAR();
            sc = MFMA32K8(kf2[5], q2, sc); SBAR();
            OPJ(oacc[1] = MFMA16(wa[1], ob, oacc[1]);)
            OPJ(wa[1] = WOLOAD(72704);)
            SBAR();
            o = MFMA32(vf[4][0], BF8(p0), o); SBAR();
            wl[4] = WOLOAD(86016);
            SBAR();
            o = MFMA32(vf[4][1], BF8(p1), o); SBAR();
            PRIO(1); EXP4(sc, 0); SBAR(); PRIO(2);
            EXP4(sc, 4); PACK4(p0, sc, 0); SBAR(); EXP4(sc, 8); EXP4(sc, 12); PACK4(p1, sc, 8); SBAR(); PRIO(3);
            sc = MFMA32(kf1[6], q1, zero16); SBAR();
            OPJ(oacc[2] = MFMA16(wa[2], ob, oacc[2]);)
            OPJ(wa[2] = WOLOAD(73728);)
            wl[5] = WOLOAD(87040);
            SBAR();
            sc = MFMA32K8(kf2[6], q2, sc); SBAR();
            OPJ(oacc[3] = MFMA16(wa[3], ob, oacc[3]);)
            OPJ(wa[3] = WOLOAD(74752);)
            SBAR();
            o = MFMA32(vf[5][0], BF8(p0), o); SBAR();
            OPJ(oacc[4] = MFMA16(wa[4], ob, oacc[4]);)
            OPJ(wa[4] = WOLOAD(75776);)
            OPJ(ob = *(const bf16x8*)(otp + 7168);)
            wl[6] = WOLOAD(88064);
            SBAR();
            o = MFMA32(vf[5][1], BF8(p1), o); SBAR();
            PRIO(1); EXP4(sc, 0); SBAR(); PRIO(2);
            EXP4(sc, 4); PACK4(p0, sc, 0); SBAR(); EXP4(sc, 8); EXP4(sc, 12); PACK4(p1, sc, 8); SBAR(); PRIO(3);
            sc = MFMA32(kf1[7], q1, zero16); SBAR();
            OPJ(oacc[0] = MFMA16(wa[0], ob, oacc[0]);)
            OPJ(wa[0] = WOLOAD(76800);)
            wl[7] = WOLOAD(89088);
            SBAR();
            sc = MFMA32K8(kf2[7], q2, sc); SBAR();
            OPJ(oacc[1] = MFMA16(wa[1], ob, oacc[1]);)
            OPJ(wa[1] = WOLOAD(77824);)
            SBAR();
            o = MFMA32(vf[6][0], BF8(p0), o); SBAR();
            wl[8] = WOLOAD(90112);
            SBAR();
            o = MFMA32(vf[6][1], BF8(p1), o); SBAR();
            PRIO(1); EXP4(sc, 0); SBAR(); PRIO(2);
            EXP4(sc, 4); PACK4(p0, sc, 0); SBAR(); EXP4(sc, 8); EXP4(sc, 12); PACK4(p1, sc, 8); SBAR(); PRIO(3);
            OPJ(oacc[2] = MFMA16(wa[2], ob, oacc[2]);)
            OPJ(wa[2] = WOLOAD(78848);)
            wl[9] = WOLOAD(91136);
            SBAR();
            OPJ(oacc[3] = MFMA16(wa[3], ob, oacc[3]);)
            OPJ(wa[3] = WOLOAD(79872);)
            SBAR();
            o = MFMA32(vf[7][0], BF8(p0), o); SBAR();
            OPJ(oacc[4] = MFMA16(wa[4], ob, oacc[4]);)
            OPJ(wa[4] = WOLOAD(80896);)
            SBAR();
            o = MFMA32(vf[7][1], BF8(p1), o); SBAR();

            STAMP(1, 9); RSTAMP(29);
            otp += 7680;
        }
        PRIO(0);
        XWLOAD();
        WRITE_PARTIAL(1);
        bf16x8 wt[10];
        {
            const char* tp = smem + K2_TAIL + w * 10240 + OPQ16();
#pragma unroll
            for (int f = 0; f < 10; ++f) wt[f] = *(const bf16x8*)(tp + f * 1024);
        }
        BAR_LDS();
        STAMP(1, 12);
        {
            PHASE_LOCALS
            STEP2A(1); SBAR();
            const bf16x8 ob0 = *(const bf16x8*)(otp), ob1 = *(const bf16x8*)(otp + 512);
#pragma unroll
            for (int t = 0; t < 5; ++t) oacc[t] = MFMA16(wa[t], ob0, oacc[t]);
            STEP2B(); STEP2C(7);
            if (badm != 0ull) *(unsigned*)(smem + K2_MISC) = 1u;
#pragma unroll
            for (int t = 0; t < 5; ++t) oacc[t] = MFMA16(wl[t], ob1, oacc[t]);
            asm volatile("" : "+v"(oacc[0]), "+v"(oacc[1]), "+v"(oacc[2]), "+v"(oacc[3]), "+v"(oacc[4]));
        }
        BAR_LDS();
        STAMP(1, 13);
        {
            const bf16x8 ob2 = *(const bf16x8*)(otp + 1024), ob3 = *(const bf16x8*)(otp + 1536), ob4 = *(const bf16x8*)(otp + 2048);
#pragma unroll
            for (int t = 0; t < 5; ++t) oacc[t] = MFMA16(wl[5 + t], ob2, oacc[t]);
#pragma unroll
            for (int t = 0; t < 5; ++t) oacc[t] = MFMA16(wt[t], ob3, oacc[t]);
#pragma unroll
            for (int t = 0; t < 5; ++t) oacc[t] = MFMA16(wt[5 + t], ob4, oacc[t]);
        }
        const unsigned bad = *(const unsigned*)(smem + K2_MISC);
        asm volatile("" : "+v"(xa), "+v"(xb1), "+v"(wa4), "+v"(wb1));
        if (__builtin_amdgcn_readfirstlane(bad) == 0u) break;

        {
            FRESH_IDS
            const unsigned l16 = OPQ16();
            const unsigned lane = l16 >> 4, r32 = lane & 31, hi = lane >> 5;
            for (int g = 0; g < 8; ++g) {
                const bf16x8 qa = *(const bf16x8*)(smem + K2_QL + g * 1536 + l16);
                bf16x4 qb = *(const bf16x4*)(smem + K2_QL + g * 1536 + 1024 + (l16 >> 1));
                if (hi == 1) qb = (bf16x4){0, 0, 0, 0};
                float m = -INFINITY;
                for (int t = 0; t < 8; ++t) {
                    const bf16x8 ka = *(const bf16x8*)(ws + OFF_KF1 + (size_t)(w * 8 + t) * 1024 + l16);
                    const bf16x4 kb = *(const bf16x4*)(ws + OFF_KF2 + (size_t)((w * 8 + t) * 32 + r32) * 8);
                    f32x16 s = MFMA32(ka, qa, zero16);
                    s = MFMA32K8(kb, qb, s);
                    m = fmaxf(m, max16(s));
                }
                m = max32(m);
                if (hi == 0) *(float*)(smem + K2_PBUF + ((w * 8 + g) * 32 + r32) * 4) = m;
            }
            BAR_LDS();
            if (hi == 1) {
                float m = -INFINITY;
#pragma unroll
                for (int u = 0; u < 8; ++u) m = fmaxf(m, *(const float*)(smem + K2_PBUF + ((u * 8 + w) * 32 + r32) * 4));
                ushort_t a, b2, c; split3(-m, a, b2, c);
                *(bf16x4*)(smem + K2_QL + w * 1536 + 1024 + (l16 >> 1)) = (bf16x4){(short)a, (short)b2, (short)c, 0};
            }
        }
        asm volatile("" : "=v"(q1w), "=v"(q2w), "=v"(k1w), "=v"(k2w));
    }

    STAMP(1, 10); RSTAMP(24);
    FRESH_IDS
    const int lane = OPQ16() >> 4, l15 = lane & 15, lg = lane >> 4;
    if (l15 < 8) {
#pragma unroll
        for (int t = 0; t < 5; ++t) *(f32x4*)(smem + K2_STG + l15 * 2576 + (w * 80 + t * 16 + lg * 4) * 4) = oacc[t];
    }
    BAR_LDS();
    STAMP(1, 15);
    unsigned hp[5], hnp[5], op[5];
    f32x2 hf[5];
    f32x2 sum2 = {0.f, 0.f};
    {
        const f32x4 a0 = *(const f32x4*)(smem + K2_STG + w * 2576 + lane * 32), a1 = *(const f32x4*)(smem + K2_STG + w * 2576 + lane * 32 + 16);
        const f32x2 a2 = *(const f32x2*)(smem + K2_STG + w * 2576 + 2048 + lane * 8);
        const f32x2 av[5] = {{a0[0], a0[1]}, {a0[2], a0[3]}, {a1[0], a1[1]}, {a1[2], a1[3]}, a2};
#pragma unroll
        for (int i = 0; i < 5; ++i) {
            hp[i] = cvtpk2(unpk(i < 4 ? xa[i] : xb1) + unpk(cvtpk2(av[i])));
            hf[i] = unpk(hp[i]);
        }
    }
    const float c0 = __builtin_bit_cast(float, __builtin_amdgcn_readfirstlane(__builtin_bit_cast(int, hf[0][0])));
    f32x2 sq2 = {0.f, 0.f};
#pragma unroll
    for (int i = 0; i < 5; ++i) { const f32x2 d = hf[i] - c0; sum2 += d; sq2 += d * d; }
    float sd = sum2[0] + sum2[1], sq = sq2[0] + sq2[1];
    wave_sum2(sd, sq);
    sd *= (1.0f / DM);
    const float mean = c0 + sd;
    const float var = fmaxf(sq * (1.0f / DM) - sd * sd, 0.f);
    const float mb = rbf(mean);
    const float sig = rbf(__builtin_amdgcn_sqrtf(rbf(rbf(var) + 1e-5f)));
    const float rsig = __builtin_amdgcn_rcpf(sig);
    f32x2 rp2 = {0.f, 0.f};
    const int token = wg * 8 + w;
#pragma unroll
    for (int i = 0; i < 5; ++i) {
        hnp[i] = cvtpk2(unpk(cvtpk2(hf[i] - mb)) * rsig);
        const f32x2 hn2 = unpk(hnp[i]);
        rp2 += hn2 * unpk(i < 4 ? wa4[i] : wb1);
        op[i] = cvtpk2(hf[i] + hn2);
    }
    const float rsc = wave_sum(rp2[0] + rp2[1]);
    const unsigned rb = f2bf(rsc);
    const unsigned key = (((rb & 0x8000u) ? (~rb & 0xffffu) : (rb | 0x8000u)) << 16) | (unsigned)(2047 - token);
    if (lane == 0) *(unsigned*)(smem + K2_MISC + 16 + w * 4) = key;
    STAMP(1, 19);
    BAR_LDS();
    STAMP(1, 20);
    bool cand;
    {
        const u32x4 ka = *(const u32x4*)(smem + K2_MISC + 16), kb = *(const u32x4*)(smem + K2_MISC + 32);
        const unsigned best = max(max(max(ka[0], ka[1]), max(ka[2], ka[3])), max(max(kb[0], kb[1]), max(kb[2], kb[3])));
        cand = __builtin_amdgcn_readfirstlane(best) == __builtin_amdgcn_readfirstlane(key);
    }
    char* orow = (char*)(out + (size_t)token * DM);
#define OUT_ROW() do { *(u32x4*)(orow + lane * 16) = (u32x4){op[0], op[1], op[2], op[3]}; *(unsigned*)(orow + 1024 + lane * 4) = op[4]; } while (0)
    if (cand) {
        if (lane == 0) __hip_atomic_store((GAS unsigned long long*)(ws + OFF_KEYG) + wg, ((unsigned long long)GTAG << 32) | key, __ATOMIC_RELAXED, __HIP_MEMORY_SCOPE_AGENT);
        const int ro = (int)OFF_CROW + wg * 5120;
        __builtin_amdgcn_raw_buffer_store_b128((u32x4){hnp[0], GTAG, hnp[1], GTAG}, wsr, lane * 32, ro, 16);
        __builtin_amdgcn_raw_buffer_store_b128((u32x4){hnp[2], GTAG, hnp[3], GTAG}, wsr, lane * 32 + 16, ro, 16);
        __builtin_amdgcn_raw_buffer_store_b64((u32x2){hnp[4], GTAG}, wsr, lane * 8, ro + 2048, 16);
        __builtin_amdgcn_raw_buffer_store_b128((u32x4){hp[0], GTAG, hp[1], GTAG}, wsr, lane * 32, ro + 2560, 16);
        __builtin_amdgcn_raw_buffer_store_b128((u32x4){hp[2], GTAG, hp[3], GTAG}, wsr, lane * 32 + 16, ro + 2560, 16);
        __builtin_amdgcn_raw_buffer_store_b64((u32x2){hp[4], GTAG}, wsr, lane * 8, ro + 4608, 16);
    }
    if (!cand) OUT_ROW();
    if (w < 7) {
        if (bi < 216) {
            const char* gp = (const char*)((lane & 1) ? w_up : w_gate) + (size_t)(lane >> 1) * 3456 + gcb * 16;
#pragma unroll
            for (int i = 0; i < 3; ++i) { const int in = min(w + 7 * i, 19); glds16(gp + (size_t)in * 110592, lds0 + K2_GU + in * 1024); }
        } else {
            const unsigned vo2 = (lane >> 1) * 1280 + (lane & 1) * 16;
#pragma unroll
            for (int i = 0; i < 8; ++i) { const int in = min(w + 7 * i, 53); glds16s((const char*)w_down + dcb * 32 + (size_t)in * 40960, vo2, lds0 + K2_TAIL + in * 1024); }
        }
    }
    STAMP(1, 11); RSTAMP(27);
    const int rep = (int)OFF_REPL + (bi & 7) * 256;
    if (w == 7) {
        if (bi < 8) {
            unsigned best = 0;
            for (unsigned spins = 0; spins < (1u << 16); ++spins) {
                bool ok = true; best = 0;
#pragma unroll
                for (int i = 0; i < 2; ++i) {
                    const u32x4 g = __builtin_amdgcn_raw_buffer_load_b128(wsr, lane * 16, (int)OFF_KEYG + 1024 * i, 16);
                    ok = ok && g[1] == GTAG && g[3] == GTAG;
                    best = max(best, max(g[0], g[2]));
                }
                if (__all(ok)) break;
                __builtin_amdgcn_s_sleep(1);
            }
            best = wave_umax(best);
            if (lane == 0) { __builtin_amdgcn_raw_buffer_store_b64((u32x2){best, GTAG}, wsr, 0, rep, 16); *(unsigned*)(smem + K2_MISC + 64) = best; }
        } else {
            u32x2 gk;
            for (unsigned spins = 0; spins < (1u << 16); ++spins) {
                gk = __builtin_amdgcn_raw_buffer_load_b64(wsr, 0, rep, 16);
                if (__builtin_amdgcn_readfirstlane(gk[1]) == GTAG) break;
                __builtin_amdgcn_s_sleep(1);
            }
            if (lane == 0) *(unsigned*)(smem + K2_MISC + 64) = gk[0];
        }
    }
    asm volatile("s_waitcnt vmcnt(0) lgkmcnt(0)\n\ts_barrier" ::: "memory");
    const unsigned wkey = __builtin_amdgcn_readfirstlane(*(const unsigned*)(smem + K2_MISC + 64));
    const int wtok = 2047 - (int)(wkey & 0xffffu);
    const int wro = (int)OFF_CROW + (wtok >> 3) * 5120;
    u32x4 xb[3];
    if (bi < 216) {
        for (unsigned spins = 0; spins < (1u << 16); ++spins) {
            bool ok = true;
#pragma unroll
            for (int i = 0; i < 3; ++i) {
                const int so = wro + min(w + 8 * i, 19) * 128;
                const u32x4 g0 = __builtin_amdgcn_raw_buffer_load_b128(wsr, lg * 32, so, 16);
                const u32x4 g1 = __builtin_amdgcn_raw_buffer_load_b128(wsr, lg * 32 + 16, so, 16);
                ok = ok && g0[1] == GTAG && g0[3] == GTAG && g1[1] == GTAG && g1[3] == GTAG;
                xb[i] = (u32x4){g0[0], g0[2], g1[0], g1[2]};
            }
            if (__all(ok)) break;
            __builtin_amdgcn_s_sleep(1);
        }
    }
    STAMP(1, 16); RSTAMP(26);
    if (bi < 216) {
        f32x4 acc = {0, 0, 0, 0};
        const unsigned ga = lds0 + K2_GU + (8 * lg + (l15 >> 2)) * 32 + (l15 & 3) * 8;
#pragma unroll
        for (int i = 0; i < 3; ++i) {
            if (w + 8 * i < 20) {
                const bf16x4 lo = trread(ga + (w + 8 * i) * 1024), hi = trread(ga + (w + 8 * i) * 1024 + 128);
                acc = MFMA16(__builtin_shufflevector(lo, hi, 0, 1, 2, 3, 4, 5, 6, 7), __builtin_bit_cast(bf16x8, xb[i]), acc);
            }
        }
        if (l15 == 0) *(f32x4*)(smem + K2_MISC + 128 + w * 64 + lg * 16) = acc;
    }
    if (cand && token != wtok) OUT_ROW();
    if (bi < 216) {
        BAR_LDS();
        if (w == 0) {
            float g = 0.f, u = 0.f;
#pragma unroll
            for (int v = 0; v < 8; ++v) { g += *(const float*)(smem + K2_MISC + 128 + v * 64 + (lane & 7) * 4); u += *(const float*)(smem + K2_MISC + 128 + v * 64 + 32 + (lane & 7) * 4); }
            g = rbf(g); u = rbf(u);
            const float sg = rbf(g * __builtin_amdgcn_rcpf(1.0f + __expf(-g)));
            const unsigned a = f2bf(sg * u);
            const unsigned an = (unsigned)__builtin_amdgcn_update_dpp(0, (int)a, 0xB1, 0xf, 0xf, true);
            if (lane < 8 && (lane & 1) == 0)
                __hip_atomic_store((GAS unsigned long long*)(ws + OFF_ACTG) + gcb * 4 + (lane >> 1), ((unsigned long long)GTAG << 32) | a | (an << 16), __ATOMIC_RELAXED, __HIP_MEMORY_SCOPE_AGENT);
        }
    }
    STAMP(1, 17); RSTAMP(25);
    if (bi >= 216) {
        bf16x8 af[7];
        const unsigned da = lds0 + K2_TAIL + (8 * lg + (l15 >> 2)) * 32 + (l15 & 3) * 8;
#pragma unroll
        for (int i = 0; i < 7; ++i) {
            const int ks = min(w + 8 * i, 53);
            const bf16x4 lo = trread(da + ks * 1024), hi = trread(da + ks * 1024 + 128);
            af[i] = __builtin_shufflevector(lo, hi, 0, 1, 2, 3, 4, 5, 6, 7);
        }
        unsigned hh = 0, hnn = 0;
        if (w == 1) {
            u32x4 g[7];
            for (unsigned spins = 0; spins < (1u << 16); ++spins) {
                bool ok = true;
#pragma unroll
                for (int i = 0; i < 7; ++i) {
                    g[i] = __builtin_amdgcn_raw_buffer_load_b128(wsr, min(lane * 16 + i * 1024, 6896), (int)OFF_ACTG, 16);
                    ok = ok && g[i][1] == GTAG && g[i][3] == GTAG;
                }
                if (__all(ok)) break;
                __builtin_amdgcn_s_sleep(1);
            }
#pragma unroll
            for (int i = 0; i < 7; ++i) *(u32x2*)(smem + K2_ACT + min(lane * 8 + i * 512, 3448)) = (u32x2){g[i][0], g[i][2]};
        }
        if (w == 0) {
            for (unsigned spins = 0; spins < (1u << 16); ++spins) {
                const u32x2 g0 = __builtin_amdgcn_raw_buffer_load_b64(wsr, (lane & 7) * 8, wro + dcb * 64, 16);
                const u32x2 g1 = __builtin_amdgcn_raw_buffer_load_b64(wsr, (lane & 7) * 8, wro + 2560 + dcb * 64, 16);
                hnn = g0[0]; hh = g1[0];
                if (__all(g0[1] == GTAG && g1[1] == GTAG)) break;
                __builtin_amdgcn_s_sleep(1);
            }
        }
        asm volatile("s_waitcnt vmcnt(0) lgkmcnt(0)\n\ts_barrier" ::: "memory");
        RSTAMP(23);
        f32x4 acc = {0, 0, 0, 0};
#pragma unroll
        for (int i = 0; i < 7; ++i) {
            if (w + 8 * i < 54) {
                const bf16x8 ab = *(const bf16x8*)(smem + K2_ACT + (w + 8 * i) * 64 + lg * 16);
                acc = MFMA16(af[i], ab, acc);
            }
        }
        if (l15 == 0) *(f32x4*)(smem + K2_MISC + 128 + w * 64 + lg * 16) = acc;
        BAR_LDS();
        if (w == 0 && lane < 8) {
            f32x2 y = {0.f, 0.f};
#pragma unroll
            for (int v = 0; v < 8; ++v) y += *(const f32x2*)(smem + K2_MISC + 128 + v * 64 + lane * 8);
            const unsigned ord = wkey >> 16;
            const float r = bf2f((ushort_t)((ord & 0x8000u) ? (ord & 0x7fffu) : (~ord & 0xffffu)));
            const float sg = rbf(__builtin_amdgcn_rcpf(1.0f + __expf(-r)));
            const f32x2 yy = unpk(cvtpk2(unpk(cvtpk2(y)) * sg));
            *(unsigned*)(out + (size_t)wtok * DM + dcb * 16 + lane * 2) = cvtpk2(unpk(hh) + unpk(cvtpk2(unpk(hnn) + yy)));
        }
    }
    asm volatile(".globl k2_code_end_mark\n\t.protected k2_code_end_mark\nk2_code_end_mark:");
    STAMP(1, 18); RSTAMP(31);
    STAMP_FLUSH(1);
#undef DIAG_LDS
}

extern "C" void kernel_launch(void* const* d_in, const int* in_sizes, int n_in,
                              void* d_out, int out_size, void* d_ws, size_t ws_size,
                              hipStream_t stream) {
    const ushort_t* x = (const ushort_t*)d_in[0];
    const ushort_t* wq = (const ushort_t*)d_in[1];
    const ushort_t* wk = (const ushort_t*)d_in[2];
    const ushort_t* wv = (const ushort_t*)d_in[3];
    const ushort_t* wo = (const ushort_t*)d_in[4];
    const ushort_t* w_r = (const ushort_t*)d_in[6];
    const ushort_t* w_gate = (const ushort_t*)d_in[7];
    const ushort_t* w_up = (const ushort_t*)d_in[8];
    const ushort_t* w_down = (const ushort_t*)d_in[9];
    char* ws = (char*)d_ws;
    ushort_t* out = (ushort_t*)d_out;
    (void)hipFuncSetAttribute((const void*)k1_qkv, hipFuncAttributeMaxDynamicSharedMemorySize, K1_LDS_BYTES);
    (void)hipFuncSetAttribute((const void*)k2_attn, hipFuncAttributeMaxDynamicSharedMemorySize, K2_LDS_BYTES);
    hipLaunchKernelGGL(k1_qkv, dim3(256), dim3(768), K1_LDS_BYTES, stream, x, wq, wk, wv, wo, w_gate, w_up, w_down, ws);
    hipLaunchKernelGGL(k2_attn, dim3(256), dim3(512), K2_LDS_BYTES, stream, x, w_r, w_gate, w_up, w_down, ws, out);
}
```

```cpp
#include <hip/hip_runtime.h>
#include <hip/hip_bf16.h>
#include <stdint.h>

typedef short bf16x8 __attribute__((ext_vector_type(8)));
typedef short bf16x4 __attribute__((ext_vector_type(4)));
typedef float f32x2 __attribute__((ext_vector_type(2)));
typedef float f32x4 __attribute__((ext_vector_type(4)));
typedef float f32x16 __attribute__((ext_vector_type(16)));
typedef unsigned u32x4 __attribute__((ext_vector_type(4)));
typedef unsigned u32x2 __attribute__((ext_vector_type(2)));
typedef unsigned short ushort_t;
#define LAS __attribute__((address_space(3)))
#define GAS __attribute__((address_space(1)))
#define DI __device__ __forceinline__

constexpr int NTOK = 2048, DM = 640, NHEAD = 32, DH = 20, FF = 1728;

constexpr size_t OFF_QF1  = 0;
constexpr size_t OFF_QF2  = OFF_QF1 + 2097152;
constexpr size_t OFF_KF1  = OFF_QF2 + 524288;
constexpr size_t OFF_KF2  = OFF_KF1 + 65536;
constexpr size_t OFF_VF   = OFF_KF2 + 16384;
constexpr size_t OFF_WOF  = OFF_VF + 131072;
constexpr size_t OFF_KEYG = OFF_WOF + 819200;
constexpr size_t OFF_ACTG = OFF_KEYG + 2048;
constexpr size_t OFF_CROW = OFF_KEYG + 9216;
constexpr size_t OFF_REPL = OFF_CROW + 1310720;
constexpr size_t OFF_CONST = OFF_REPL + 2048;
constexpr unsigned GTAG = 0x600DF00Du;
constexpr size_t OFF_DIAG = 64u << 20;

#define STAMP(kid, idx) do {} while (0)
#define STAMP_INIT() do {} while (0)
#define RSTAMP(idx) do {} while (0)
#define STAMP_FLUSH(kid) do {} while (0)
#define DIAG_BYTES 0
DI float bf2f(ushort_t b) { return __uint_as_float(((unsigned)b) << 16); }
typedef __bf16 bf16x2_t __attribute__((ext_vector_type(2)));
DI unsigned cvtpk(float lo, float hi) { return __builtin_bit_cast(unsigned, __builtin_convertvector((f32x2){lo, hi}, bf16x2_t)); }
DI unsigned cvtpk2(f32x2 v) { return __builtin_bit_cast(unsigned, __builtin_convertvector(v, bf16x2_t)); }
DI f32x2 unpk(unsigned p) { return (f32x2){__uint_as_float(p << 16), __uint_as_float(p & 0xffff0000u)}; }
DI float rbf(float x) { return __uint_as_float(cvtpk(x, x) << 16); }
DI ushort_t f2bf(float x) { return (ushort_t)(cvtpk(x, x) & 0xffffu); }
DI float dot2acc(unsigned a, unsigned b, float c) { asm("v_dot2c_f32_bf16 %0, %1, %2" : "+v"(c) : "v"(a), "v"(b)); return c; }
DI void glds16(const void* g, unsigned lds_base) {
    unsigned sv;
    asm volatile("s_mov_b32 %0, m0\n\ts_mov_b32 m0, %2\n\ts_nop 0\n\tglobal_load_lds_dwordx4 %1, off\n\ts_mov_b32 m0, %0"
                 : "=&s"(sv) : "v"(g), "s"(lds_base) : "memory");
}
DI void glds16s(const char* sbase, unsigned voff, unsigned lds_base) {
    unsigned sv;
    asm volatile("s_mov_b32 %0, m0\n\ts_mov_b32 m0, %3\n\ts_nop 0\n\tglobal_load_lds_dwordx4 %1, %2\n\ts_mov_b32 m0, %0"
                 : "=&s"(sv) : "v"(voff), "s"(sbase), "s"(lds_base) : "memory");
}
#define SBAR() __builtin_amdgcn_sched_barrier(0)
#define BAR_LDS() asm volatile("s_waitcnt lgkmcnt(0)\n\ts_barrier" ::: "memory")
#define MFMA32(a, b, c) __builtin_amdgcn_mfma_f32_32x32x16_bf16((a), (b), (c), 0, 0, 0)
#define MFMA32K8(a, b, c) __builtin_amdgcn_mfma_f32_32x32x8bf16_1k((a), (b), (c), 0, 0, 0)
#define MFMA16(a, b, c) __builtin_amdgcn_mfma_f32_16x16x32_bf16((a), (b), (c), 0, 0, 0)
typedef short v4i16_t __attribute__((ext_vector_type(4)));
DI bf16x4 trread(unsigned lds_addr) {
    return __builtin_amdgcn_ds_read_tr16_b64_v4i16((LAS v4i16_t*)(uintptr_t)lds_addr);
}
#define DPP_ADD(v, ctrl, rmask, bc) v += __builtin_bit_cast(float, __builtin_amdgcn_update_dpp(0, __builtin_bit_cast(int, v), ctrl, rmask, 0xf, bc))
DI float wave_sum(float v) {
    DPP_ADD(v, 0x111, 0xf, true); DPP_ADD(v, 0x112, 0xf, true); DPP_ADD(v, 0x114, 0xf, true); DPP_ADD(v, 0x118, 0xf, true);
    DPP_ADD(v, 0x142, 0xa, false); DPP_ADD(v, 0x143, 0xc, false);
    return __builtin_bit_cast(float, __builtin_amdgcn_readlane(__builtin_bit_cast(int, v), 63));
}
DI void wave_sum2(float& a, float& b) {
    DPP_ADD(a, 0x111, 0xf, true); DPP_ADD(b, 0x111, 0xf, true); DPP_ADD(a, 0x112, 0xf, true); DPP_ADD(b, 0x112, 0xf, true);
    DPP_ADD(a, 0x114, 0xf, true); DPP_ADD(b, 0x114, 0xf, true); DPP_ADD(a, 0x118, 0xf, true); DPP_ADD(b, 0x118, 0xf, true);
    DPP_ADD(a, 0x142, 0xa, false); DPP_ADD(b, 0x142, 0xa, false); DPP_ADD(a, 0x143, 0xc, false); DPP_ADD(b, 0x143, 0xc, false);
    a = __builtin_bit_cast(float, __builtin_amdgcn_readlane(__builtin_bit_cast(int, a), 63));
    b = __builtin_bit_cast(float, __builtin_amdgcn_readlane(__builtin_bit_cast(int, b), 63));
}
DI unsigned wave_umax(unsigned v) {
#define DPP_MAX(ctrl, rmask) v = max(v, (unsigned)__builtin_amdgcn_update_dpp(0, (int)v, ctrl, rmask, 0xf, true))
    DPP_MAX(0x111, 0xf); DPP_MAX(0x112, 0xf); DPP_MAX(0x114, 0xf); DPP_MAX(0x118, 0xf); DPP_MAX(0x142, 0xa); DPP_MAX(0x143, 0xc);
    return (unsigned)__builtin_amdgcn_readlane((int)v, 63);
}
DI float xor16(float v) { auto r = __builtin_amdgcn_permlane16_swap(__float_as_uint(v), __float_as_uint(v), false, false);
                          return __uint_as_float(r[0]) + __uint_as_float(r[1]); }
DI float xor32(float v) { auto r = __builtin_amdgcn_permlane32_swap(__float_as_uint(v), __float_as_uint(v), false, false);
                          return __uint_as_float(r[0]) + __uint_as_float(r[1]); }
DI float max32(float v) { auto r = __builtin_amdgcn_permlane32_swap(__float_as_uint(v), __float_as_uint(v), false, false);
                          return __builtin_fmaxf(__uint_as_float(r[0]), __uint_as_float(r[1])); }

constexpr int K1_XB = 0;
constexpr int K1_W = 40960;
constexpr int K1_KV = 143360;
constexpr int K1_WOT = 153664;
constexpr int K1_ST = 158784;
constexpr int K1_CS = K1_ST + 1024;
constexpr int K1_JUNK = K1_CS + 768;
constexpr int K1_END = K1_JUNK + 256;
constexpr int K1_LDS_BYTES = K1_END + DIAG_BYTES;
constexpr int K1_OQ = K1_XB;
constexpr int K1_OKV = K1_XB + 10240;

DI int swap23(int r) { return (r & ~12) | ((r & 4) << 1) | ((r & 8) >> 1); }
DI void glds5(const char* sbase, const unsigned (&v)[5], unsigned lds_base) {
    unsigned sv;
    asm volatile("s_mov_b32 %0, m0\n\ts_mov_b32 m0, %7\n\ts_nop 0\n\tglobal_load_lds_dwordx4 %1, %6\n\t"
                 "s_add_u32 m0, m0, 0x400\n\ts_nop 0\n\tglobal_load_lds_dwordx4 %2, %6\n\t"
                 "s_add_u32 m0, m0, 0x400\n\ts_nop 0\n\tglobal_load_lds_dwordx4 %3, %6\n\t"
                 "s_add_u32 m0, m0, 0x400\n\ts_nop 0\n\tglobal_load_lds_dwordx4 %4, %6\n\t"
                 "s_add_u32 m0, m0, 0x400\n\ts_nop 0\n\tglobal_load_lds_dwordx4 %5, %6\n\t"
                 "s_mov_b32 m0, %0"
                 : "=&s"(sv) : "v"(v[0]), "v"(v[1]), "v"(v[2]), "v"(v[3]), "v"(v[4]), "s"(sbase), "s"(lds_base) : "memory", "scc");
}
#define VMBAR(n) asm volatile("s_waitcnt vmcnt(" #n ") lgkmcnt(0)\n\ts_barrier" ::: "memory")

#define RAWBAR() asm volatile("s_barrier" ::: "memory")
#define K1_SYM "_Z6k1_qkvPKtS0_S0_S0_S0_S0_S0_S0_Pc"
#define K2_SYM "_Z7k2_attnPKtS0_S0_S0_S0_PcPt"
#define CODE_ADDR(dst, sym) asm volatile("s_getpc_b64 s[40:41]\n\ts_add_u32 s40, s40, " sym "@rel32@lo+4\n\ts_addc_u32 s41, s41, " sym "@rel32@hi+12\n\t" \
                                         "s_mov_b64 %0, s[40:41]" : "=s"(dst) :: "scc", "s40", "s41")
DI void k1_loader(char* smem, unsigned lds0, const ushort_t* x, const ushort_t* wq, const ushort_t* wk, const ushort_t* wv,
                  const ushort_t* wo, const ushort_t* w_gate, const ushort_t* w_up, const ushort_t* w_down,
                  char* ws, int d, int lane, int b, int panel, int j) {
#define DIAG_LDS K1_END
    const int rot = (5 * (panel & 3) + 2 * j + (b & 7)) % 20;
    unsigned xo[5], xo2[5], wo5[5];
#pragma unroll
    for (int k = 0; k < 5; ++k) {
        const int p = k * 64 + lane;
        const int r = p / 40, c = p - r * 40;
        const int cb = (((c >> 2) ^ ((r >> 1) & 1)) * 64) + (c & 3) * 16 + rot * 64;
        xo[k] = r * 1280 + (cb >= 1280 ? cb - 1280 : cb);
        const int cb2 = cb + 640;
        xo2[k] = r * 1280 + (cb2 >= 1280 ? cb2 - 1280 : cb2);
    }
    const char* xb = (const char*)(x + (size_t)(panel * 64 + d * 16) * DM);
    glds5(xb, xo, lds0 + K1_XB + d * 10240);
    glds5(xb + 10240, xo, lds0 + K1_XB + d * 10240 + 5120);
#pragma unroll
    for (int k = 0; k < 5; ++k) {
        const int p = k * 64 + lane;
        const int rw = p / 10, c8 = p - rw * 10;
        wo5[k] = swap23(rw) * 1280 + c8 * 16;
    }
    const char* wqb = (const char*)(wq + j * 80);
    const unsigned wl = lds0 + K1_W + d * 5120;
#define K1_WSLOT(s4) glds5(wqb + (size_t)((d + (s4) + rot) % 20) * 40960, wo5, wl + (s4) * 5120)
    K1_WSLOT(0);
    VMBAR(5);
    K1_WSLOT(4);
    VMBAR(5);
    glds5(xb, xo2, lds0 + K1_XB + d * 10240);
    glds5(xb + 10240, xo2, lds0 + K1_XB + d * 10240 + 5120);
    K1_WSLOT(8);
    VMBAR(15);
    K1_WSLOT(12);
    K1_WSLOT(16);
    VMBAR(10);
    {
        const int bo = min(b, 159);
        const char* wob = (const char*)(wo + (size_t)(bo >> 3) * 32 * DM + (bo & 7) * 80);
        const unsigned oa = (d == 0) ? wo5[0] : (d == 1) ? wo5[1] : (d == 2) ? wo5[2] : wo5[3];
        glds16s(wob, oa, lds0 + K1_WOT + d * 1024);
        glds16s(wob, wo5[4], lds0 + K1_WOT + 4096);
    }
    {
        const unsigned sl = (b >> 3) * 4 + d;
#pragma unroll
        for (int i = 0; i < 2; ++i) {
            const unsigned l = min(i * 64u + lane, 76u);
            const char* p;
            if (l < 50u) { const unsigned n = sl * 50 + l;
                p = (const char*)((n >= 3200u) ? w_up : w_gate) + (size_t)((n % 3200u) / 5u) * 3456 + min(((b & 7) * 432u & ~127u) + (n % 5u) * 128u, 3328u); }
            else { const unsigned n = sl * 27 + (l - 50u);
                p = (const char*)w_down + (size_t)(n >> 1) * 1280 + min(((b & 7) * 160u & ~127u) + (n & 1u) * 128u, 1152u); }
            unsigned sv;
            asm volatile("s_mov_b32 %0, m0\n\ts_mov_b32 m0, %2\n\ts_nop 0\n\tglobal_load_lds_dword %1, off\n\ts_mov_b32 m0, %0"
                         : "=&s"(sv) : "v"(p), "s"(lds0 + K1_JUNK) : "memory");
        }
        if (d == 1) {
            unsigned long long c0, c1;
            CODE_ADDR(c0, K2_SYM); CODE_ADDR(c1, "k2_code_end_mark");
            const unsigned off = min(((b >> 3) * 12u + min((unsigned)lane, 11u)) * 128u, (unsigned)(c1 - c0) & ~3u);
            unsigned sv;
            asm volatile("s_mov_b32 %0, m0\n\ts_mov_b32 m0, %3\n\ts_nop 0\n\tglobal_load_lds_dword %1, %2\n\ts_mov_b32 m0, %0"
                         : "=&s"(sv) : "v"(off), "s"(c0), "s"(lds0 + K1_JUNK) : "memory");
        }
        const u32x4 z = {0, 0, 0, 0};
        char* zp = ws + OFF_CROW + (size_t)b * 5120 + d * 1024 + lane * 16;
        asm volatile("global_store_dwordx4 %0, %1, off\n\ts_nop 1" :: "v"(zp), "v"(z) : "memory");
        if (d == 0) zp += 4096; else if (b < 9) zp = ws + OFF_KEYG + b * 1024 + lane * 16; else if (b < 11) zp = ws + OFF_REPL + (b - 9) * 1024 + lane * 16;
        asm volatile("global_store_dwordx4 %0, %1, off\n\ts_nop 1" :: "v"(zp), "v"(z) : "memory");
    }
    VMBAR(6);
    if (d == 2 && b == 0 && lane < 3) {
        const unsigned o2 = 0x3F803F80u;
        *(u32x4*)(ws + OFF_CONST + lane * 16) = (lane == 0) ? (u32x4){o2, o2, o2, o2} : (lane == 1) ? (u32x4){0, 0, 0, 0} : (u32x4){o2, 0x3F80u, 0, 0};
    }
    VMBAR(4);
    if (d == 3 && b < 160) {
        const int l15 = lane & 15, lg = lane >> 4;
        const unsigned a = lds0 + K1_WOT + (16 * (lg >> 1) + 4 * (lg & 1) + (l15 >> 2)) * 160 + (l15 & 3) * 8;
        char* dst = ws + OFF_WOF + (size_t)((b & 7) * 100 + (b >> 3) * 5) * 1024 + lane * 16;
#pragma unroll
        for (int t = 0; t < 5; ++t) {
            const bf16x4 lo = trread(a + t * 32), hi = trread(a + t * 32 + 1280);
            *(bf16x8*)(dst + t * 1024) = __builtin_shufflevector(lo, hi, 0, 1, 2, 3, 4, 5, 6, 7);
        }
    }
    if (d == 1 && j < 6) {
        const u32x4 kv = *(const u32x4*)(smem + K1_OKV + lane * 16);
        const int tile0 = panel * 2;
        if (j < 2) {
            *(u32x4*)(ws + OFF_KF1 + (size_t)((tile0 + (lane >> 5)) * 64 + 32 * j + (lane & 31)) * 16) = kv;
        } else if (j == 2) {
            if (lane < 32) *(u32x4*)(ws + OFF_KF2 + (size_t)tile0 * 256 + lane * 16) = kv;
        } else {
            const int dd = (j == 5) ? 12 + (lane & 7) : 8 * (j - 3) + (lane & 7);
            if (j < 5 || (lane & 7) >= 4)
                *(u32x4*)(ws + OFF_VF + (size_t)((tile0 * 2 + (lane >> 4)) * 64 + 32 * ((lane >> 3) & 1) + dd) * 16) = kv;
        }
    }
    asm volatile("s_waitcnt vmcnt(0)\n\t.globl k1_code_end_mark\n\t.protected k1_code_end_mark\nk1_code_end_mark:" ::: "memory");
#undef DIAG_LDS
}

template <int NH, int LOW>
DI void k1_body(char* smem, unsigned lds0, char* ws, int u, int lane, int j) {
#define DIAG_LDS K1_END
    const int l15 = lane & 15, lg = lane >> 4;
    const int m = u >> 1, rho = (m ^ NH) & 1;
    bf16x8 xf[10];
    const char* xr0 = smem + K1_XB + (m * 16 + l15) * 640 + lg * 16;
    const int e = rho ^ ((l15 >> 1) & 1);
    const char* xe = xr0 + e * 64;
    const char* xod = xr0 - e * 64;
    float s1 = 0.f, s2 = 0.f;
    const unsigned ones2 = 0x3F803F80u;
    f32x4 acc[3], cs[3];
#pragma unroll
    for (int t = 0; t < 3; ++t) { acc[t] = (f32x4){0, 0, 0, 0}; cs[t] = (f32x4){0, 0, 0, 0}; }
    bf16x8 onesf;
#pragma unroll
    for (int i = 0; i < 8; ++i) onesf[i] = (short)0x3F80;
    asm volatile("" : "+v"(onesf));
    const int rrow = 16 * (lg >> 1) + 4 * (lg & 1) + (l15 >> 2);
    const unsigned aW = lds0 + K1_W + rrow * 160 + (l15 & 3) * 8 + NH * 96;
    const unsigned aW4[4] = {aW + rho * 5120, aW - rho * 5120, aW + rho * 5120 + 51200, aW - rho * 5120 + 51200};
    const unsigned aK = lds0 + K1_KV + (8 * lg + (l15 >> 2)) * 16 + (l15 & 3) * 8;
    const unsigned aKe = aK + rho * 512, aKo = aK - rho * 512;
    bf16x8 wf[4][3];
#define K1_STATS() do { _Pragma("unroll") for (int i_ = 0; i_ < 10; i_ += 2) { const u32x4 v_ = __builtin_bit_cast(u32x4, xf[i_]); \
        _Pragma("unroll") for (int q_ = 0; q_ < 4; ++q_) { s1 = dot2acc(v_[q_], ones2, s1); s2 = dot2acc(v_[q_], v_[q_], s2); } } SBAR(); } while (0)
#define K1_READ(i) do { \
        const unsigned ab_ = aW4[((i) >= 10 ? 2 : 0) + ((i) & 1)] + ((i) % 10) * 5120; \
        _Pragma("unroll") for (int t_ = 0; t_ < (NH ? 2 : 3); ++t_) { \
            const bf16x4 lo_ = trread(ab_ + t_ * 32), hi_ = trread(ab_ + t_ * 32 + 1280); \
            wf[(i) & 3][t_] = __builtin_shufflevector(lo_, hi_, 0, 1, 2, 3, 4, 5, 6, 7); } \
        if (NH) { const unsigned ak_ = (((i) & 1) ? aKo : aKe) + (i) * 512; \
            const bf16x4 lo_ = trread(ak_), hi_ = trread(ak_ + 64); \
            wf[(i) & 3][2] = __builtin_shufflevector(lo_, hi_, 0, 1, 2, 3, 4, 5, 6, 7); } \
    } while (0)
#define K1_MMA(i, XF) do { \
        _Pragma("unroll") for (int t_ = 0; t_ < 3; ++t_) { \
            acc[t_] = MFMA16(wf[(i) & 3][t_], XF[(i) % 10], acc[t_]); \
            if ((i) >= 12 || (LOW && ((i) & 1) == 0)) cs[t_] = MFMA16(wf[(i) & 3][t_], onesf, cs[t_]); } \
    } while (0)
#define K1_CHUNK(i0, XA, XB) do { K1_READ(i0); K1_READ((i0) + 1); K1_READ((i0) + 2); K1_READ((i0) + 3); SBAR(); \
        K1_MMA(i0, XA); K1_MMA((i0) + 1, XA); K1_MMA((i0) + 2, XB); K1_MMA((i0) + 3, XB); SBAR(); } while (0)
    RAWBAR();
    STAMP(0, 11);
#pragma unroll
    for (int i = 0; i < 10; ++i) xf[i] = *(const bf16x8*)(((i & 1) ? xod : xe) + i * 64);
    VMBAR(0);
    K1_CHUNK(0, xf, xf);
    K1_STATS();
    STAMP(0, 1);
    RAWBAR();
    K1_CHUNK(4, xf, xf);
    RAWBAR();
    bf16x8 xg[10];
#pragma unroll
    for (int i = 0; i < 10; ++i) xg[i] = *(const bf16x8*)(((i & 1) ? xod : xe) + i * 64);
    K1_CHUNK(8, xf, xg);
#pragma unroll
    for (int i = 0; i < 10; ++i) xf[i] = xg[i];
    K1_STATS();
    s1 = xor32(xor16(s1));
    s2 = xor32(xor16(s2));
    if (lg == 0) *(f32x2*)(smem + K1_ST + (NH * 64 + m * 16 + l15) * 8) = (f32x2){s1, s2};
    if (LOW && l15 == 0) {
#pragma unroll
        for (int t = 0; t < 3; ++t) *(f32x4*)(smem + K1_CS + (m * 96 + NH * 48 + t * 16 + lg * 4) * 4) = cs[t];
    }
    STAMP(0, 2);
    BAR_LDS();
    const int tl = m * 16 + l15;
    K1_READ(12); K1_READ(13);
    const f32x2 sa = *(const f32x2*)(smem + K1_ST + tl * 8);
    const f32x2 sb = *(const f32x2*)(smem + K1_ST + (64 + tl) * 8);
#pragma unroll
    for (int t = 0; t < 3; ++t)
        cs[t] = *(const f32x4*)(smem + K1_CS + (NH * 48 + t * 16 + lg * 4) * 4) +
                *(const f32x4*)(smem + K1_CS + (96 + NH * 48 + t * 16 + lg * 4) * 4);
    SBAR();
#pragma unroll
    for (int i = 12; i < 20; ++i) { if (i + 2 < 20) K1_READ(i + 2); SBAR(); K1_MMA(i, xf); SBAR(); }
    const float mean = (sa[0] + sb[0]) * (1.0f / DM);
    const float var = fmaxf((sa[1] + sb[1]) * (1.0f / DM) - mean * mean, 0.f);
    const float mb = rbf(mean);
    const float sig = rbf(__builtin_amdgcn_sqrtf(rbf(rbf(var) + 1e-5f)));
    const float rs = __builtin_amdgcn_rcpf(sig);
    STAMP(0, 4);
    const float QSCALE = 0.22360679774997896f * 1.4426950408889634f;
    const int tok = tl & 7;
#pragma unroll
    for (int t = 0; t < 3; ++t) {
        const f32x4 csum = cs[t];
        float v[4];
#pragma unroll
        for (int r = 0; r < 4; ++r) v[r] = (acc[t][r] - mb * csum[r]) * rs;
        if (t == 2 && NH == 1) {
            if (lg < 2) {
                const int kk = tl & 31;
                if (j < 3) {
                    u32x2 pk; pk[0] = cvtpk(v[0], v[1]); pk[1] = cvtpk(v[2], v[3]);
                    if (j < 2) *(u32x2*)(smem + K1_OKV + tl * 16 + lg * 8) = pk;
                    else if (lg == 1) *(u32x2*)(smem + K1_OKV + tl * 8) = pk;
                } else {
                    const int s = kk >> 4, h = (kk >> 2) & 1, jj = ((kk >> 3) & 1) * 4 + (kk & 3);
                    char* d = smem + K1_OKV + (((((tl >> 5) * 2 + s) * 2 + h) * 8 + lg * 4) * 16) + jj * 2;
#pragma unroll
                    for (int r = 0; r < 4; ++r) *(ushort_t*)(d + r * 16) = f2bf(v[r]);
                }
            }
        } else {
            const int nl = (NH * 3 + t) * 16 + lg * 4;
            const int hl = nl / 20, dq = (nl - hl * 20) >> 2;
            u32x2 pk;
            pk[0] = cvtpk(v[0] * QSCALE, v[1] * QSCALE);
            pk[1] = cvtpk(v[2] * QSCALE, v[3] * QSCALE);
            const int off = (dq < 4) ? ((hl * 8 + tok + 32 * (dq >> 1)) * 16 + (dq & 1) * 8) : (1024 + (hl * 8 + tok) * 8);
            *(u32x2*)(smem + K1_OQ + (tl >> 3) * 1280 + off) = pk;
        }
    }
    BAR_LDS();
    STAMP(0, 5);
#undef DIAG_LDS
}

__global__ __launch_bounds__(768, 3) void k1_qkv(const ushort_t* __restrict__ x, const ushort_t* __restrict__ wq,
                                                 const ushort_t* __restrict__ wk, const ushort_t* __restrict__ wv,
                                                 const ushort_t* __restrict__ wo, const ushort_t* __restrict__ w_gate,
                                                 const ushort_t* __restrict__ w_up, const ushort_t* __restrict__ w_down,
                                                 char* __restrict__ ws) {
    extern __shared__ __attribute__((aligned(16))) char smem[];
    const unsigned lds0 = (unsigned)(uintptr_t)smem;
    const int lane = threadIdx.x & 63;
    const int u = __builtin_amdgcn_readfirstlane(threadIdx.x >> 6);
    if (u < 3) {
        unsigned long long c0, c1;
        CODE_ADDR(c0, K1_SYM); CODE_ADDR(c1, "k1_code_end_mark");
        const unsigned off = min(threadIdx.x * 128u, (unsigned)(c1 - c0) & ~3u);
        unsigned sv;
        asm volatile("s_mov_b32 %0, m0\n\ts_mov_b32 m0, %3\n\ts_nop 0\n\tglobal_load_lds_dword %1, %2\n\ts_mov_b32 m0, %0"
                     : "=&s"(sv) : "v"(off), "s"(c0), "s"(lds0 + K1_JUNK) : "memory");
    }
    const int b = blockIdx.x;
    const int panel = (b & 7) * 4 + ((b >> 3) >> 3), j = (b >> 3) & 7;
    if (u >= 8) { k1_loader(smem, lds0, x, wq, wk, wv, wo, w_gate, w_up, w_down, ws, u - 8, lane, b, panel, j); return; }
#define DIAG_LDS K1_END
    STAMP_INIT();
    STAMP(0, 0); RSTAMP(30);
    {
        const int rot = (5 * (panel & 3) + 2 * j + (b & 7)) % 20;
        const int jq = (j >= 3) ? ((j >= 6) ? 0 : j - 3) : j;
        const char* kvb = (const char*)((j >= 3 && j < 6) ? wv : wk) + (jq == 2 ? 24 : jq * 16);
        int row = u * 64 + lane + rot * 32;
        glds16s(kvb, (unsigned)((row >= 640 ? row - 640 : row) * 40), lds0 + K1_KV + u * 1024);
        if (u < 2) {
            row += 512;
            glds16s(kvb, (unsigned)((row >= 640 ? row - 640 : row) * 40), lds0 + K1_KV + (8 + u) * 1024);
        }
    }
    if (u < 4) {
        if (u & 1) k1_body<1, 1>(smem, lds0, ws, u, lane, j);
        else k1_body<0, 1>(smem, lds0, ws, u, lane, j);
    } else {
        if (u & 1) k1_body<1, 0>(smem, lds0, ws, u, lane, j);
        else k1_body<0, 0>(smem, lds0, ws, u, lane, j);
    }
    {
        const int wg2 = panel * 8 + u;
        const u32x4 v = *(const u32x4*)(smem + K1_OQ + u * 1280 + lane * 16);
        *(u32x4*)(ws + OFF_QF1 + (size_t)(wg2 * 8 + j) * 1024 + lane * 16) = v;
        if (lane < 16) {
            const u32x4 v2 = *(const u32x4*)(smem + K1_OQ + u * 1280 + 1024 + lane * 16);
            *(u32x4*)(ws + OFF_QF2 + (size_t)(wg2 * 8 + j) * 256 + lane * 16) = v2;
        }
    }
    STAMP(0, 6); RSTAMP(31);
    STAMP_FLUSH(0);
#undef DIAG_LDS
}

constexpr int K2_PBUF = 0;
constexpr int K2_QL = 49152;
constexpr int K2_OT = 61440;
constexpr int K2_MISC = 71680;
constexpr int K2_TAIL = K2_MISC + 1152;
constexpr int K2_GU = K2_TAIL;
constexpr int K2_ACT = K2_TAIL + 57344;
constexpr int K2_STG = K2_PBUF;
constexpr int K2_JUNK = K2_TAIL + 81920;
constexpr int K2_END = K2_JUNK + 1024;
constexpr int K2_LDS_BYTES = K2_END + DIAG_BYTES;

DI void split3(float v, ushort_t& a, ushort_t& b, ushort_t& c) {
    a = f2bf(v); const float r1 = v - bf2f(a);
    b = f2bf(r1); const float r2 = r1 - bf2f(b);
    c = f2bf(r2);
}
#define MX3(a, b, c) __builtin_fmaxf(__builtin_fmaxf((a), (b)), (c))
DI float max16(const f32x16& s) {
    float a = MX3(s[0], s[1], s[2]), b = MX3(s[3], s[4], s[5]);
    a = MX3(a, s[6], s[7]); b = MX3(b, s[8], s[9]);
    a = MX3(a, s[10], s[11]); b = MX3(b, s[12], s[13]);
    a = MX3(a, s[14], s[15]);
    return __builtin_fmaxf(a, b);
}

__global__ __launch_bounds__(512, 2) void k2_attn(const ushort_t* __restrict__ x, const ushort_t* __restrict__ w_r,
                                                   const ushort_t* __restrict__ w_gate, const ushort_t* __restrict__ w_up,
                                                   const ushort_t* __restrict__ w_down,
                                                   char* __restrict__ ws, ushort_t* __restrict__ out) {
    extern __shared__ __attribute__((aligned(16))) char smem[];
    const unsigned lds0 = (unsigned)(uintptr_t)smem;
    const int w_0 = __builtin_amdgcn_readfirstlane(threadIdx.x >> 6);
    const int wg_0 = (blockIdx.x & 7) * 32 + (blockIdx.x >> 3);
    const unsigned lane16 = (threadIdx.x & 63) * 16;
#define FRESH_IDS int w = w_0, wg = wg_0; asm volatile("" : "+s"(w), "+s"(wg));
#define DIAG_LDS K2_END
    STAMP_INIT();
    STAMP(1, 0); RSTAMP(30);
    bf16x8 q1w = *(const bf16x8*)(ws + OFF_QF1 + (size_t)(wg_0 * 8 + w_0) * 1024 + lane16);
    bf16x4 q2w = *(const bf16x4*)(ws + (lane16 < 512u ? (unsigned)OFF_QF2 + (unsigned)(wg_0 * 8 + w_0) * 256u + (lane16 >> 1) : (unsigned)OFF_CONST + 16u));
    bf16x8 k1w = *(const bf16x8*)(ws + OFF_KF1 + (size_t)w_0 * 8192 + lane16);
    bf16x4 k2w = *(const bf16x4*)(ws + (lane16 < 512u ? (unsigned)OFF_KF2 + (unsigned)w_0 * 2048u + (lane16 >> 1) : (unsigned)OFF_CONST + 32u));
    SBAR();
    const __amdgpu_buffer_rsrc_t wrs = __builtin_amdgcn_make_buffer_rsrc((void*)(ws + OFF_WOF + (size_t)w_0 * 102400), 0, 102400 + 5120, 0x00020000);
    const __amdgpu_buffer_rsrc_t wsr = __builtin_amdgcn_make_buffer_rsrc((void*)ws, 0, 8 << 20, 0x00020000);
    unsigned lo4[4] = {lane16, lane16 + 1024, lane16 + 2048, lane16 + 3072};
    asm volatile("" : "+v"(lo4[1]), "+v"(lo4[2]), "+v"(lo4[3]));
#define WOLOAD(off) __builtin_bit_cast(bf16x8, __builtin_amdgcn_raw_buffer_load_b128(wrs, lo4[((off) >> 10) & 3], (off) & ~4095, 0))
#define OPQ16() ({ unsigned l_ = lane16; asm volatile("" : "+v"(l_)); l_; })

    const int bi = blockIdx.x;
    const int gcb = (bi & 7) * 27 + min(bi >> 3, 26);
    const int dcb = (bi & 7) * 5 + max((bi >> 3) - 27, 0);
    u32x4 xa, wa4; unsigned xb1, wb1;
    f32x4 oacc[5];
    const f32x16 zero16 = {};

    for (int attempt = 0; attempt < 2; ++attempt) {
        FRESH_IDS
        bf16x8 kf1[8]; bf16x4 kf2[8]; bf16x8 vf[8][2];
        unsigned k2o, k2s, vo, vs;
        STAMP(1, 14);
        {
            const unsigned l16 = OPQ16();
            const unsigned lane = l16 >> 4, r32 = lane & 31, hi = lane >> 5;
            k2o = hi == 0 ? (unsigned)OFF_KF2 + (unsigned)(w * 256 + r32) * 8u : (unsigned)OFF_CONST + 32u;
            k2s = hi == 0 ? 256u : 0u;
            vo = r32 < DH ? (unsigned)OFF_VF + (unsigned)(w * 1024) * 16u + l16 : (unsigned)OFF_CONST + (r32 == DH ? 0u : 16u);
            vs = r32 < DH ? 1024u : 0u;
#define KLOAD(t) do { kf1[t] = __builtin_bit_cast(bf16x8, __builtin_amdgcn_raw_buffer_load_b128(wsr, lane16, (int)OFF_KF1 + w * 8192 + (t) * 1024, 0)); \
            kf2[t] = __builtin_bit_cast(bf16x4, __builtin_amdgcn_raw_buffer_load_b64(wsr, k2o + (t) * k2s, 0, 0)); } while (0)
#define VLOAD(t) do { vf[t][0] = __builtin_bit_cast(bf16x8, __builtin_amdgcn_raw_buffer_load_b128(wsr, vo + (2 * (t)) * vs, 0, 0)); \
            vf[t][1] = __builtin_bit_cast(bf16x8, __builtin_amdgcn_raw_buffer_load_b128(wsr, vo + (2 * (t) + 1) * vs, 0, 0)); } while (0)
            KLOAD(0); SBAR(); KLOAD(1); SBAR(); VLOAD(0); SBAR(); KLOAD(2); SBAR(); VLOAD(1); SBAR();
            if (attempt == 0) {
                f32x16 s = MFMA32(k1w, q1w, zero16);
                s = MFMA32K8(k2w, q2w, s);
                const float m = max32(max16(s));
                if (hi == 1) { ushort_t a, b2, c; split3(-m, a, b2, c); q2w = (bf16x4){(short)a, (short)b2, (short)c, 0}; }
                *(bf16x8*)(smem + K2_QL + w * 1536 + l16) = q1w;
                *(bf16x4*)(smem + K2_QL + w * 1536 + 1024 + (l16 >> 1)) = q2w;
            }
            if (w == 0) *(unsigned*)(smem + K2_MISC) = 0u;
        }
#pragma unroll
        for (int t = 0; t < 5; ++t) oacc[t] = (f32x4){0, 0, 0, 0};
        BAR_LDS();
        STAMP(1, 1); RSTAMP(28);

        unsigned long long badm = 0ull;
        f32x16 sc, o;
        u32x4 p0, p1;
        bf16x8 wa[5];
        bf16x8 q1 = *(const bf16x8*)(smem + K2_QL + OPQ16());
        bf16x4 q2 = *(const bf16x4*)(smem + K2_QL + 1024 + (OPQ16() >> 1));
        const char* otp; { const unsigned l_ = OPQ16(); otp = smem + K2_OT + ((l_ >> 1) & 0x180) + (l_ & 0x70); }
        char* pbv = smem + K2_PBUF + w * 3072 + OPQ16();
        const char* q2v = smem + K2_QL + 1024 + (OPQ16() >> 1);
        const char *s2pa, *s2pl; char* s2dst;
        {
            const unsigned l_ = OPQ16() >> 4;
            const unsigned it_ = w * 20 + min(l_ >> 1, 19u);
            s2pa = smem + K2_PBUF + (l_ & 1) * 12288 + it_ * 16;
            s2pl = smem + K2_PBUF + (l_ & 1) * 12288 + (160 + (it_ & 31)) * 16;
            const unsigned kr_ = 20 * ((it_ & 31) >> 3) + 4 * (it_ >> 5);
            s2dst = smem + K2_OT + ((kr_ >> 3) * 8 + (it_ & 7)) * 16 + (kr_ & 7) * 2;
        }
#define PHASE_LOCALS bf16x8 q1n; bf16x4 q2n; bf16x8 ob; f32x4 sa0, sa1, sa2, sa3; float sl0, sl1, sl2, sl3;
        sc = MFMA32(kf1[0], q1, zero16);
        sc = MFMA32K8(kf2[0], q2, sc);
#define PRIO(n) __builtin_amdgcn_s_setprio(n)
#define LSTAMP(a, b) STAMP(a, b)

#define EXP4(S, i) do { S[i] = __builtin_amdgcn_exp2f(S[i]); S[i + 1] = __builtin_amdgcn_exp2f(S[i + 1]); \
                        S[i + 2] = __builtin_amdgcn_exp2f(S[i + 2]); S[i + 3] = __builtin_amdgcn_exp2f(S[i + 3]); } while (0)
#define PACK4(P, S, i) do { P[0] = cvtpk(S[i], S[i + 1]); P[1] = cvtpk(S[i + 2], S[i + 3]); \
                            P[2] = cvtpk(S[i + 4], S[i + 5]); P[3] = cvtpk(S[i + 6], S[i + 7]); } while (0)
#define BF8(P) __builtin_bit_cast(bf16x8, P)
#define WP_ADDR(PAR) char* pb_ = pbv + (PAR) * 24576
#define WP_ST(i) *(f32x4*)(pb_ + (i) * 1024) = (f32x4){o[4 * (i)], o[4 * (i) + 1], o[4 * (i) + 2], o[4 * (i) + 3]}
#define WRITE_PARTIAL(PAR) do { WP_ADDR(PAR); asm volatile("" : "+v"(o)); WP_ST(0); WP_ST(1); WP_ST(2); } while (0)
#define STEP2A(PAR) do { const char* a_ = s2pa + (PAR) * 24576; const char* l_ = s2pl + (PAR) * 24576; \
            sa0 = *(const f32x4*)(a_); sa1 = *(const f32x4*)(a_ + 3072); sa2 = *(const f32x4*)(a_ + 6144); sa3 = *(const f32x4*)(a_ + 9216); \
            sl0 = *(const float*)(l_); sl1 = *(const float*)(l_ + 3072); sl2 = *(const float*)(l_ + 6144); sl3 = *(const float*)(l_ + 9216); \
        } while (0)
#define STEP2B() do { \
            sa0 = (sa0 + sa1) + (sa2 + sa3); sl0 += sl1; asm volatile("" : "+v"(sl0)); sl2 += sl3; asm volatile("" : "+v"(sl2)); sl0 += sl2; \
            DPP_ADD(sl0, 0xB1, 0xf, true); \
            badm |= __ballot(!(sl0 < 1.8446744e19f)); \
            sl0 = __builtin_amdgcn_rcpf(sl0); \
            asm volatile("" : "+v"(sa0), "+v"(sl0)); \
        } while (0)
#define STEP2C(G) do { \
            float a0_ = sa0[0], a1_ = sa0[1], a2_ = sa0[2], a3_ = sa0[3]; \
            DPP_ADD(a0_, 0xB1, 0xf, true); DPP_ADD(a1_, 0xB1, 0xf, true); DPP_ADD(a2_, 0xB1, 0xf, true); DPP_ADD(a3_, 0xB1, 0xf, true); \
            u32x2 pk_; pk_[0] = cvtpk(a0_ * sl0, a1_ * sl0); pk_[1] = cvtpk(a2_ * sl0, a3_ * sl0); \
            *(u32x2*)(s2dst + (G) * 1280) = pk_; \
        } while (0)
#define TAILDMA(i) glds16s(ws + OFF_WOF + (size_t)w * 102400 + 92160 + (i) * 1024, lane16, lds0 + K2_TAIL + w * 10240 + (i) * 1024)
#define WARMDMA(i) glds16s(ws + OFF_WOF + (size_t)((blockIdx.x >> 3) * 25 + min(w + 8 * (i), 24)) * 1024, lane16, lds0 + K2_JUNK)
#define GUDMA(i) do { const unsigned l_ = OPQ16() >> 4; const int in_ = min(w + 8 * (i), 19); \
            glds16((const char*)((l_ & 1) ? w_up : w_gate) + (size_t)(in_ * 32 + (l_ >> 1)) * 3456 + gcb * 16, lds0 + K2_GU + in_ * 1024); } while (0)
#define WARMXW() do { const unsigned l_ = OPQ16() >> 4; \
            const char* p_ = (l_ & 32) ? (const char*)w_r + min(l_ & 31u, 9u) * 128 : (const char*)(x + (size_t)(wg * 8 + w) * DM) + min(l_, 9u) * 128; \
            unsigned sv_; asm volatile("s_mov_b32 %0, m0\n\ts_mov_b32 m0, %2\n\ts_nop 0\n\tglobal_load_lds_dword %1, off\n\ts_mov_b32 m0, %0" \
                                       : "=&s"(sv_) : "v"(p_), "s"(lds0 + K2_JUNK) : "memory"); } while (0)
#define XWLOAD() do { const unsigned l_ = OPQ16(); const char* xrow_ = (const char*)(x + (size_t)(wg * 8 + w) * DM); \
            xa = *(const u32x4*)(xrow_ + l_); xb1 = *(const unsigned*)(xrow_ + 1024 + (l_ >> 2)); \
            wa4 = *(const u32x4*)((const char*)w_r + l_); wb1 = *(const unsigned*)((const char*)w_r + 1024 + (l_ >> 2)); } while (0)
#define OPJ(...) __VA_ARGS__
#define LOOPBAR() BAR_LDS()
#define S2(...) __VA_ARGS__

        {
            const int gn = 1;
            PHASE_LOCALS
            FRESH_IDS
            PRIO(1); EXP4(sc, 0); SBAR(); PRIO(2);
            EXP4(sc, 4); PACK4(p0, sc, 0); SBAR(); EXP4(sc, 8); EXP4(sc, 12); PACK4(p1, sc, 8); SBAR(); PRIO(3);
            sc = MFMA32(kf1[1], q1, zero16); SBAR();
            sc = MFMA32K8(kf2[1], q2, sc); SBAR();
            KLOAD(3);
            SBAR();
            o = MFMA32(vf[0][0], BF8(p0), zero16); SBAR();
            VLOAD(2);
            SBAR();
            o = MFMA32(vf[0][1], BF8(p1), o); SBAR();
            PRIO(1); EXP4(sc, 0); SBAR(); PRIO(2);
            EXP4(sc, 4); PACK4(p0, sc, 0); SBAR(); EXP4(sc, 8); EXP4(sc, 12); PACK4(p1, sc, 8); SBAR(); PRIO(3);
            sc = MFMA32(kf1[2], q1, zero16); SBAR();
            sc = MFMA32K8(kf2[2], q2, sc); SBAR();
            KLOAD(4);
            SBAR();
            o = MFMA32(vf[1][0], BF8(p0), o); SBAR();
            VLOAD(3);
            SBAR();
            o = MFMA32(vf[1][1], BF8(p1), o); SBAR();
            PRIO(1); EXP4(sc, 0); SBAR(); PRIO(2);
            EXP4(sc, 4); PACK4(p0, sc, 0); SBAR(); EXP4(sc, 8); EXP4(sc, 12); PACK4(p1, sc, 8); SBAR(); PRIO(3);
            sc = MFMA32(kf1[3], q1, zero16); SBAR();
            sc = MFMA32K8(kf2[3], q2, sc); SBAR();
            KLOAD(5);
            SBAR();
            o = MFMA32(vf[2][0], BF8(p0), o); SBAR();
            VLOAD(4);
            SBAR();
            o = MFMA32(vf[2][1], BF8(p1), o); SBAR();
            PRIO(1); EXP4(sc, 0); SBAR(); PRIO(2);
            EXP4(sc, 4); PACK4(p0, sc, 0); SBAR(); EXP4(sc, 8); EXP4(sc, 12); PACK4(p1, sc, 8); SBAR(); PRIO(3);
            sc = MFMA32(kf1[4], q1, zero16); SBAR();
            sc = MFMA32K8(kf2[4], q2, sc); SBAR();
            KLOAD(6);
            SBAR();
            o = MFMA32(vf[3][0], BF8(p0), o); SBAR();
            VLOAD(5);
            SBAR();
            o = MFMA32(vf[3][1], BF8(p1), o); SBAR();
            PRIO(1); EXP4(sc, 0); SBAR(); PRIO(2);
            EXP4(sc, 4); PACK4(p0, sc, 0); SBAR(); EXP4(sc, 8); EXP4(sc, 12); PACK4(p1, sc, 8); SBAR(); PRIO(3);
            sc = MFMA32(kf1[5], q1, zero16); SBAR();
            sc = MFMA32K8(kf2[5], q2, sc); SBAR();
            KLOAD(7);
            SBAR();
            o = MFMA32(vf[4][0], BF8(p0), o); SBAR();
            VLOAD(6);
            SBAR();
            o = MFMA32(vf[4][1], BF8(p1), o); SBAR();
            PRIO(1); EXP4(sc, 0); SBAR(); PRIO(2);
            EXP4(sc, 4); PACK4(p0, sc, 0); SBAR(); EXP4(sc, 8); EXP4(sc, 12); PACK4(p1, sc, 8); SBAR(); PRIO(3);
            sc = MFMA32(kf1[6], q1, zero16); SBAR();
            sc = MFMA32K8(kf2[6], q2, sc); SBAR();
            o = MFMA32(vf[5][0], BF8(p0), o); SBAR();
            VLOAD(7);
            SBAR();
            o = MFMA32(vf[5][1], BF8(p1), o); SBAR();
            PRIO(1); EXP4(sc, 0); SBAR(); PRIO(2);
            EXP4(sc, 4); PACK4(p0, sc, 0); SBAR(); EXP4(sc, 8); EXP4(sc, 12); PACK4(p1, sc, 8); SBAR(); PRIO(3);
            sc = MFMA32(kf1[7], q1, zero16); SBAR();
            sc = MFMA32K8(kf2[7], q2, sc); SBAR();
            o = MFMA32(vf[6][0], BF8(p0), o); SBAR();
            q1n = *(const bf16x8*)(smem + K2_QL + gn * 1536 + lane16); q2n = *(const bf16x4*)(q2v + gn * 1536);
            SBAR();
            o = MFMA32(vf[6][1], BF8(p1), o); SBAR();
            PRIO(1); EXP4(sc, 0); SBAR(); PRIO(2);
            EXP4(sc, 4); PACK4(p0, sc, 0); SBAR(); EXP4(sc, 8); EXP4(sc, 12); PACK4(p1, sc, 8); SBAR(); PRIO(3);
            sc = MFMA32(kf1[0], q1n, zero16); SBAR();
            sc = MFMA32K8(kf2[0], q2n, sc); SBAR();
            o = MFMA32(vf[7][0], BF8(p0), o); SBAR();
            o = MFMA32(vf[7][1], BF8(p1), o); SBAR();
            q1 = q1n; q2 = q2n;
            LSTAMP(1, 2);
        }
        {
            const int gn = 2;
            PHASE_LOCALS
            FRESH_IDS
            PRIO(1); EXP4(sc, 0); SBAR(); PRIO(2); WP_ADDR(0); WP_ST(0); SBAR();
            EXP4(sc, 4); SBAR(); WP_ST(1); SBAR(); PACK4(p0, sc, 0); EXP4(sc, 8); SBAR(); WP_ST(2); SBAR(); EXP4(sc, 12); PACK4(p1, sc, 8); SBAR(); PRIO(3);
            asm volatile("" : "+v"(p0), "+v"(p1)); if (w >= 4) LOOPBAR();
            sc = MFMA32(kf1[1], q1, zero16); SBAR();
            WARMXW();
            SBAR();
            sc = MFMA32K8(kf2[1], q2, sc); SBAR();
            OPJ(WARMDMA(0);)
            SBAR();
            o = MFMA32(vf[0][0], BF8(p0), zero16); SBAR();
            OPJ(WARMDMA(1);)
            SBAR();
            o = MFMA32(vf[0][1], BF8(p1), o); SBAR();
            asm volatile("" : "+v"(o), "+v"(sc)); if (w < 4) LOOPBAR();
            PRIO(1); EXP4(sc, 0); SBAR(); PRIO(2);
            EXP4(sc, 4); PACK4(p0, sc, 0); SBAR(); EXP4(sc, 8); EXP4(sc, 12); PACK4(p1, sc, 8); SBAR(); PRIO(3);
            sc = MFMA32(kf1[2], q1, zero16); SBAR();
            S2(STEP2A(0);)
            SBAR();
            sc = MFMA32K8(kf2[2], q2, sc); SBAR();
            OPJ(WARMDMA(2);)
            OPJ(WARMDMA(3);)
            SBAR();
            o = MFMA32(vf[1][0], BF8(p0), o); SBAR();
            OPJ(TAILDMA(0);)
            SBAR();
            o = MFMA32(vf[1][1], BF8(p1), o); SBAR();
            PRIO(1); EXP4(sc, 0); SBAR(); PRIO(2);
            EXP4(sc, 4); PACK4(p0, sc, 0); SBAR(); EXP4(sc, 8); EXP4(sc, 12); PACK4(p1, sc, 8); SBAR(); PRIO(3);
            sc = MFMA32(kf1[3], q1, zero16); SBAR();
            S2(STEP2B();)
            SBAR();
            sc = MFMA32K8(kf2[3], q2, sc); SBAR();
            OPJ(TAILDMA(1);)
            SBAR();
            o = MFMA32(vf[2][0], BF8(p0), o); SBAR();
            OPJ(TAILDMA(2);)
            SBAR();
            o = MFMA32(vf[2][1], BF8(p1), o); SBAR();
            PRIO(1); EXP4(sc, 0); SBAR(); PRIO(2);
            EXP4(sc, 4); PACK4(p0, sc, 0); SBAR(); EXP4(sc, 8); EXP4(sc, 12); PACK4(p1, sc, 8); SBAR(); PRIO(3);
            sc = MFMA32(kf1[4], q1, zero16); SBAR();
            S2(STEP2C(gn - 2);)
            SBAR();
            sc = MFMA32K8(kf2[4], q2, sc); SBAR();
            OPJ(TAILDMA(3);)
            SBAR();
            o = MFMA32(vf[3][0], BF8(p0), o); SBAR();
            OPJ(TAILDMA(4);)
            SBAR();
            o = MFMA32(vf[3][1], BF8(p1), o); SBAR();
            PRIO(1); EXP4(sc, 0); SBAR(); PRIO(2);
            EXP4(sc, 4); PACK4(p0, sc, 0); SBAR(); EXP4(sc, 8); EXP4(sc, 12); PACK4(p1, sc, 8); SBAR(); PRIO(3);
            sc = MFMA32(kf1[5], q1, zero16); SBAR();
            OPJ(TAILDMA(5);)
            SBAR();
            sc = MFMA32K8(kf2[5], q2, sc); SBAR();
            OPJ(TAILDMA(6);)
            SBAR();
            o = MFMA32(vf[4][0], BF8(p0), o); SBAR();
            OPJ(TAILDMA(7);)
            SBAR();
            o = MFMA32(vf[4][1], BF8(p1), o); SBAR();
            PRIO(1); EXP4(sc, 0); SBAR(); PRIO(2);
            EXP4(sc, 4); PACK4(p0, sc, 0); SBAR(); EXP4(sc, 8); EXP4(sc, 12); PACK4(p1, sc, 8); SBAR(); PRIO(3);
            sc = MFMA32(kf1[6], q1, zero16); SBAR();
            OPJ(TAILDMA(8);)
            SBAR();
            sc = MFMA32K8(kf2[6], q2, sc); SBAR();
            OPJ(TAILDMA(9);)
            SBAR();
            o = MFMA32(vf[5][0], BF8(p0), o); SBAR();
            OPJ(wa[0] = WOLOAD(0);)
            SBAR();
            o = MFMA32(vf[5][1], BF8(p1), o); SBAR();
            PRIO(1); EXP4(sc, 0); SBAR(); PRIO(2);
            EXP4(sc, 4); PACK4(p0, sc, 0); SBAR(); EXP4(sc, 8); EXP4(sc, 12); PACK4(p1, sc, 8); SBAR(); PRIO(3);
            sc = MFMA32(kf1[7], q1, zero16); SBAR();
            OPJ(wa[1] = WOLOAD(1024);)
            SBAR();
            sc = MFMA32K8(kf2[7], q2, sc); SBAR();
            OPJ(wa[2] = WOLOAD(2048);)
            SBAR();
            o = MFMA32(vf[6][0], BF8(p0), o); SBAR();
            q1n = *(const bf16x8*)(smem + K2_QL + gn * 1536 + lane16); q2n = *(const bf16x4*)(q2v + gn * 1536);
            SBAR();
            o = MFMA32(vf[6][1], BF8(p1), o); SBAR();
            PRIO(1); EXP4(sc, 0); SBAR(); PRIO(2);
            EXP4(sc, 4); PACK4(p0, sc, 0); SBAR(); EXP4(sc, 8); EXP4(sc, 12); PACK4(p1, sc, 8); SBAR(); PRIO(3);
            sc = MFMA32(kf1[0], q1n, zero16); SBAR();
            OPJ(wa[3] = WOLOAD(3072);)
            SBAR();
            sc = MFMA32K8(kf2[0], q2n, sc); SBAR();
            OPJ(wa[4] = WOLOAD(4096);)
            SBAR();
            o = MFMA32(vf[7][0], BF8(p0), o); SBAR();
            o = MFMA32(vf[7][1], BF8(p1), o); SBAR();
            q1 = q1n; q2 = q2n;
            LSTAMP(1, 3);
        }
        {
            const int gn = 3;
            PHASE_LOCALS
            FRESH_IDS
            PRIO(1); EXP4(sc, 0); SBAR(); PRIO(2); WP_ADDR(1); WP_ST(0); SBAR();
            EXP4(sc, 4); SBAR(); WP_ST(1); SBAR(); PACK4(p0, sc, 0); EXP4(sc, 8); SBAR(); WP_ST(2); SBAR(); EXP4(sc, 12); PACK4(p1, sc, 8); SBAR(); PRIO(3);
            asm volatile("" : "+v"(p0), "+v"(p1)); if (w >= 4) LOOPBAR();
            sc = MFMA32(kf1[1], q1, zero16); SBAR();
            sc = MFMA32K8(kf2[1], q2, sc); SBAR();
            o = MFMA32(vf[0][0], BF8(p0), zero16); SBAR();
            o = MFMA32(vf[0][1], BF8(p1), o); SBAR();
            asm volatile("" : "+v"(o), "+v"(sc)); if (w < 4) LOOPBAR();
            PRIO(1); EXP4(sc, 0); SBAR(); PRIO(2);
            EXP4(sc, 4); PACK4(p0, sc, 0); SBAR(); EXP4(sc, 8); EXP4(sc, 12); PACK4(p1, sc, 8); SBAR(); PRIO(3);
            sc = MFMA32(kf1[2], q1, zero16); SBAR();
            OPJ(ob = *(const bf16x8*)(otp + 0);)
            S2(STEP2A(1);)
            SBAR();
            sc = MFMA32K8(kf2[2], q2, sc); SBAR();
            o = MFMA32(vf[1][0], BF8(p0), o); SBAR();
            OPJ(oacc[0] = MFMA16(wa[0], ob, oacc[0]);)
            OPJ(wa[0] = WOLOAD(5120);)
            SBAR();
            o = MFMA32(vf[1][1], BF8(p1), o); SBAR();
            PRIO(1); EXP4(sc, 0); SBAR(); PRIO(2);
            EXP4(sc, 4); PACK4(p0, sc, 0); SBAR(); EXP4(sc, 8); EXP4(sc, 12); PACK4(p1, sc, 8); SBAR(); PRIO(3);
            sc = MFMA32(kf1[3], q1, zero16); SBAR();
            S2(STEP2B();)
            SBAR();
            sc = MFMA32K8(kf2[3], q2, sc); SBAR();
            o = MFMA32(vf[2][0], BF8(p0), o); SBAR();
            OPJ(oacc[1] = MFMA16(wa[1], ob, oacc[1]);)
            OPJ(wa[1] = WOLOAD(6144);)
            SBAR();
            o = MFMA32(vf[2][1], BF8(p1), o); SBAR();
            PRIO(1); EXP4(sc, 0); SBAR(); PRIO(2);
            EXP4(sc, 4); PACK4(p0, sc, 0); SBAR(); EXP4(sc, 8); EXP4(sc, 12); PACK4(p1, sc, 8); SBAR(); PRIO(3);
            sc = MFMA32(kf1[4], q1, zero16); SBAR();
            S2(STEP2C(gn - 2);)
            SBAR();
            sc = MFMA32K8(kf2[4], q2, sc); SBAR();
            o = MFMA32(vf[3][0], BF8(p0), o); SBAR();
            OPJ(oacc[2] = MFMA16(wa[2], ob, oacc[2]);)
            OPJ(wa[2] = WOLOAD(7168);)
            SBAR();
            o = MFMA32(vf[3][1], BF8(p1), o); SBAR();
            PRIO(1); EXP4(sc, 0); SBAR(); PRIO(2);
            EXP4(sc, 4); PACK4(p0, sc, 0); SBAR(); EXP4(sc, 8); EXP4(sc, 12); PACK4(p1, sc, 8); SBAR(); PRIO(3);
            sc = MFMA32(kf1[5], q1, zero16); SBAR();
            sc = MFMA32K8(kf2[5], q2, sc); SBAR();
            OPJ(oacc[3] = MFMA16(wa[3], ob, oacc[3]);)
            OPJ(wa[3] = WOLOAD(8192);)
            SBAR();
            o = MFMA32(vf[4][0], BF8(p0), o); SBAR();
            OPJ(oacc[4] = MFMA16(wa[4], ob, oacc[4]);)
            OPJ(wa[4] = WOLOAD(9216);)
            OPJ(ob = *(const bf16x8*)(otp + 512);)
            SBAR();
            o = MFMA32(vf[4][1], BF8(p1), o); SBAR();
            PRIO(1); EXP4(sc, 0); SBAR(); PRIO(2);
            EXP4(sc, 4); PACK4(p0, sc, 0); SBAR(); EXP4(sc, 8); EXP4(sc, 12); PACK4(p1, sc, 8); SBAR(); PRIO(3);
            sc = MFMA32(kf1[6], q1, zero16); SBAR();
            sc = MFMA32K8(kf2[6], q2, sc); SBAR();
            OPJ(oacc[0] = MFMA16(wa[0], ob, oacc[0]);)
            OPJ(wa[0] = WOLOAD(10240);)
            SBAR();
            o = MFMA32(vf[5][0], BF8(p0), o); SBAR();
            OPJ(oacc[1] = MFMA16(wa[1], ob, oacc[1]);)
            OPJ(wa[1] = WOLOAD(11264);)
            SBAR();
            o = MFMA32(vf[5][1], BF8(p1), o); SBAR();
            PRIO(1); EXP4(sc, 0); SBAR(); PRIO(2);
            EXP4(sc, 4); PACK4(p0, sc, 0); SBAR(); EXP4(sc, 8); EXP4(sc, 12); PACK4(p1, sc, 8); SBAR(); PRIO(3);
            sc = MFMA32(kf1[7], q1, zero16); SBAR();
            sc = MFMA32K8(kf2[7], q2, sc); SBAR();
            OPJ(oacc[2] = MFMA16(wa[2], ob, oacc[2]);)
            OPJ(wa[2] = WOLOAD(12288);)
            SBAR();
            o = MFMA32(vf[6][0], BF8(p0), o); SBAR();
            q1n = *(const bf16x8*)(smem + K2_QL + gn * 1536 + lane16); q2n = *(const bf16x4*)(q2v + gn * 1536);
            SBAR();
            o = MFMA32(vf[6][1], BF8(p1), o); SBAR();
            PRIO(1); EXP4(sc, 0); SBAR(); PRIO(2);
            EXP4(sc, 4); PACK4(p0, sc, 0); SBAR(); EXP4(sc, 8); EXP4(sc, 12); PACK4(p1, sc, 8); SBAR(); PRIO(3);
            sc = MFMA32(kf1[0], q1n, zero16); SBAR();
            sc = MFMA32K8(kf2[0], q2n, sc); SBAR();
            OPJ(oacc[3] = MFMA16(wa[3], ob, oacc[3]);)
            OPJ(wa[3] = WOLOAD(13312);)
            SBAR();
            o = MFMA32(vf[7][0], BF8(p0), o); SBAR();
            OPJ(oacc[4] = MFMA16(wa[4], ob, oacc[4]);)
            OPJ(wa[4] = WOLOAD(14336);)
            SBAR();
            o = MFMA32(vf[7][1], BF8(p1), o); SBAR();
            q1 = q1n; q2 = q2n;
            LSTAMP(1, 4);
        }
        {
            const int gn = 4;
            PHASE_LOCALS
            FRESH_IDS
            PRIO(1); EXP4(sc, 0); SBAR(); PRIO(2); WP_ADDR(0); WP_ST(0); SBAR();
            EXP4(sc, 4); SBAR(); WP_ST(1); SBAR(); PACK4(p0, sc, 0); EXP4(sc, 8); SBAR(); WP_ST(2); SBAR(); EXP4(sc, 12); PACK4(p1, sc, 8); SBAR(); PRIO(3);
            asm volatile("" : "+v"(p0), "+v"(p1)); if (w >= 4) LOOPBAR();
            sc = MFMA32(kf1[1], q1, zero16); SBAR();
            sc = MFMA32K8(kf2[1], q2, sc); SBAR();
            o = MFMA32(vf[0][0], BF8(p0), zero16); SBAR();
            o = MFMA32(vf[0][1], BF8(p1), o); SBAR();
            asm volatile("" : "+v"(o), "+v"(sc)); if (w < 4) LOOPBAR();
            PRIO(1); EXP4(sc, 0); SBAR(); PRIO(2);
            EXP4(sc, 4); PACK4(p0, sc, 0); SBAR(); EXP4(sc, 8); EXP4(sc, 12); PACK4(p1, sc, 8); SBAR(); PRIO(3);
            sc = MFMA32(kf1[2], q1, zero16); SBAR();
            OPJ(ob = *(const bf16x8*)(otp + 1024);)
            S2(STEP2A(0);)
            SBAR();
            sc = MFMA32K8(kf2[2], q2, sc); SBAR();
            o = MFMA32(vf[1][0], BF8(p0), o); SBAR();
            OPJ(oacc[0] = MFMA16(wa[0], ob, oacc[0]);)
            OPJ(wa[0] = WOLOAD(15360);)
            SBAR();
            o = MFMA32(vf[1][1], BF8(p1), o); SBAR();
            PRIO(1); EXP4(sc, 0); SBAR(); PRIO(2);
            EXP4(sc, 4); PACK4(p0, sc, 0); SBAR(); EXP4(sc, 8); EXP4(sc, 12); PACK4(p1, sc, 8); SBAR(); PRIO(3);
            sc = MFMA32(kf1[3], q1, zero16); SBAR();
            S2(STEP2B();)
            SBAR();
            sc = MFMA32K8(kf2[3], q2, sc); SBAR();
            OPJ(oacc[1] = MFMA16(wa[1], ob, oacc[1]);)
            OPJ(wa[1] = WOLOAD(16384);)
            SBAR();
            o = MFMA32(vf[2][0], BF8(p0), o); SBAR();
            OPJ(oacc[2] = MFMA16(wa[2], ob, oacc[2]);)
            OPJ(wa[2] = WOLOAD(17408);)
            SBAR();
            o = MFMA32(vf[2][1], BF8(p1), o); SBAR();
            PRIO(1); EXP4(sc, 0); SBAR(); PRIO(2);
            EXP4(sc, 4); PACK4(p0, sc, 0); SBAR(); EXP4(sc, 8); EXP4(sc, 12); PACK4(p1, sc, 8); SBAR(); PRIO(3);
            sc = MFMA32(kf1[4], q1, zero16); SBAR();
            S2(STEP2C(gn - 2);)
            SBAR();
            sc = MFMA32K8(kf2[4], q2, sc); SBAR();
            OPJ(oacc[3] = MFMA16(wa[3], ob, oacc[3]);)
            OPJ(wa[3] = WOLOAD(18432);)
            SBAR();
            o = MFMA32(vf[3][0], BF8(p0), o); SBAR();
            OPJ(oacc[4] = MFMA16(wa[4], ob, oacc[4]);)
            OPJ(wa[4] = WOLOAD(19456);)
            OPJ(ob = *(const bf16x8*)(otp + 1536);)
            SBAR();
            o = MFMA32(vf[3][1], BF8(p1), o); SBAR();
            PRIO(1); EXP4(sc, 0); SBAR(); PRIO(2);
            EXP4(sc, 4); PACK4(p0, sc, 0); SBAR(); EXP4(sc, 8); EXP4(sc, 12); PACK4(p1, sc, 8); SBAR(); PRIO(3);
            sc = MFMA32(kf1[5], q1, zero16); SBAR();
            OPJ(oacc[0] = MFMA16(wa[0], ob, oacc[0]);)
            OPJ(wa[0] = WOLOAD(20480);)
            SBAR();
            sc = MFMA32K8(kf2[5], q2, sc); SBAR();
            OPJ(oacc[1] = MFMA16(wa[1], ob, oacc[1]);)
            OPJ(wa[1] = WOLOAD(21504);)
            SBAR();
            o = MFMA32(vf[4][0], BF8(p0), o); SBAR();
            o = MFMA32(vf[4][1], BF8(p1), o); SBAR();
            PRIO(1); EXP4(sc, 0); SBAR(); PRIO(2);
            EXP4(sc, 4); PACK4(p0, sc, 0); SBAR(); EXP4(sc, 8); EXP4(sc, 12); PACK4(p1, sc, 8); SBAR(); PRIO(3);
            sc = MFMA32(kf1[6], q1, zero16); SBAR();
            OPJ(oacc[2] = MFMA16(wa[2], ob, oacc[2]);)
            OPJ(wa[2] = WOLOAD(22528);)
            SBAR();
            sc = MFMA32K8(kf2[6], q2, sc); SBAR();
            OPJ(oacc[3] = MFMA16(wa[3], ob, oacc[3]);)
            OPJ(wa[3] = WOLOAD(23552);)
            SBAR();
            o = MFMA32(vf[5][0], BF8(p0), o); SBAR();
            OPJ(oacc[4] = MFMA16(wa[4], ob, oacc[4]);)
            OPJ(wa[4] = WOLOAD(24576);)
            OPJ(ob = *(const bf16x8*)(otp + 2048);)
            SBAR();
            o = MFMA32(vf[5][1], BF8(p1), o); SBAR();
            PRIO(1); EXP4(sc, 0); SBAR(); PRIO(2);
            EXP4(sc, 4); PACK4(p0, sc, 0); SBAR(); EXP4(sc, 8); EXP4(sc, 12); PACK4(p1, sc, 8); SBAR(); PRIO(3);
            sc = MFMA32(kf1[7], q1, zero16); SBAR();
            OPJ(oacc[0] = MFMA16(wa[0], ob, oacc[0]);)
            OPJ(wa[0] = WOLOAD(25600);)
            SBAR();
            sc = MFMA32K8(kf2[7], q2, sc); SBAR();
            OPJ(oacc[1] = MFMA16(wa[1], ob, oacc[1]);)
            OPJ(wa[1] = WOLOAD(26624);)
            SBAR();
            o = MFMA32(vf[6][0], BF8(p0), o); SBAR();
            q1n = *(const bf16x8*)(smem + K2_QL + gn * 1536 + lane16); q2n = *(const bf16x4*)(q2v + gn * 1536);
            SBAR();
            o = MFMA32(vf[6][1], BF8(p1), o); SBAR();
            PRIO(1); EXP4(sc, 0); SBAR(); PRIO(2);
            EXP4(sc, 4); PACK4(p0, sc, 0); SBAR(); EXP4(sc, 8); EXP4(sc, 12); PACK4(p1, sc, 8); SBAR(); PRIO(3);
            sc = MFMA32(kf1[0], q1n, zero16); SBAR();
            OPJ(oacc[2] = MFMA16(wa[2], ob, oacc[2]);)
            OPJ(wa[2] = WOLOAD(27648);)
            SBAR();
            sc = MFMA32K8(kf2[0], q2n, sc); SBAR();
            OPJ(oacc[3] = MFMA16(wa[3], ob, oacc[3]);)
            OPJ(wa[3] = WOLOAD(28672);)
            SBAR();
            o = MFMA32(vf[7][0], BF8(p0), o); SBAR();
            OPJ(oacc[4] = MFMA16(wa[4], ob, oacc[4]);)
            OPJ(wa[4] = WOLOAD(29696);)
            SBAR();
            o = MFMA32(vf[7][1], BF8(p1), o); SBAR();
            q1 = q1n; q2 = q2n;
            LSTAMP(1, 5);
        }
        {
            const int gn = 5;
            PHASE_LOCALS
            FRESH_IDS
            PRIO(1); EXP4(sc, 0); SBAR(); PRIO(2); WP_ADDR(1); WP_ST(0); SBAR();
            EXP4(sc, 4); SBAR(); WP_ST(1); SBAR(); PACK4(p0, sc, 0); EXP4(sc, 8); SBAR(); WP_ST(2); SBAR(); EXP4(sc, 12); PACK4(p1, sc, 8); SBAR(); PRIO(3);
            asm volatile("" : "+v"(p0), "+v"(p1)); if (w >= 4) LOOPBAR();
            sc = MFMA32(kf1[1], q1, zero16); SBAR();
            sc = MFMA32K8(kf2[1], q2, sc); SBAR();
            o = MFMA32(vf[0][0], BF8(p0), zero16); SBAR();
            o = MFMA32(vf[0][1], BF8(p1), o); SBAR();
            asm volatile("" : "+v"(o), "+v"(sc)); if (w < 4) LOOPBAR();
            PRIO(1); EXP4(sc, 0); SBAR(); PRIO(2);
            EXP4(sc, 4); PACK4(p0, sc, 0); SBAR(); EXP4(sc, 8); EXP4(sc, 12); PACK4(p1, sc, 8); SBAR(); PRIO(3);
            sc = MFMA32(kf1[2], q1, zero16); SBAR();
            OPJ(ob = *(const bf16x8*)(otp + 2560);)
            S2(STEP2A(1);)
            SBAR();
            sc = MFMA32K8(kf2[2], q2, sc); SBAR();
            o = MFMA32(vf[1][0], BF8(p0), o); SBAR();
            OPJ(oacc[0] = MFMA16(wa[0], ob, oacc[0]);)
            OPJ(wa[0] = WOLOAD(30720);)
            SBAR();
            o = MFMA32(vf[1][1], BF8(p1), o); SBAR();
            PRIO(1); EXP4(sc, 0); SBAR(); PRIO(2);
            EXP4(sc, 4); PACK4(p0, sc, 0); SBAR(); EXP4(sc, 8); EXP4(sc, 12); PACK4(p1, sc, 8); SBAR(); PRIO(3);
            sc = MFMA32(kf1[3], q1, zero16); SBAR();
            S2(STEP2B();)
            SBAR();
            sc = MFMA32K8(kf2[3], q2, sc); SBAR();
            o = MFMA32(vf[2][0], BF8(p0), o); SBAR();
            OPJ(oacc[1] = MFMA16(wa[1], ob, oacc[1]);)
            OPJ(wa[1] = WOLOAD(31744);)
            SBAR();
            o = MFMA32(vf[2][1], BF8(p1), o); SBAR();
            PRIO(1); EXP4(sc, 0); SBAR(); PRIO(2);
            EXP4(sc, 4); PACK4(p0, sc, 0); SBAR(); EXP4(sc, 8); EXP4(sc, 12); PACK4(p1, sc, 8); SBAR(); PRIO(3);
            sc = MFMA32(kf1[4], q1, zero16); SBAR();
            S2(STEP2C(gn - 2);)
            SBAR();
            sc = MFMA32K8(kf2[4], q2, sc); SBAR();
            o = MFMA32(vf[3][0], BF8(p0), o); SBAR();
            OPJ(oacc[2] = MFMA16(wa[2], ob, oacc[2]);)
            OPJ(wa[2] = WOLOAD(32768);)
            SBAR();
            o = MFMA32(vf[3][1], BF8(p1), o); SBAR();
            PRIO(1); EXP4(sc, 0); SBAR(); PRIO(2);
            EXP4(sc, 4); PACK4(p0, sc, 0); SBAR(); EXP4(sc, 8); EXP4(sc, 12); PACK4(p1, sc, 8); SBAR(); PRIO(3);
            sc = MFMA32(kf1[5], q1, zero16); SBAR();
            sc = MFMA32K8(kf2[5], q2, sc); SBAR();
            OPJ(oacc[3] = MFMA16(wa[3], ob, oacc[3]);)
            OPJ(wa[3] = WOLOAD(33792);)
            SBAR();
            o = MFMA32(vf[4][0], BF8(p0), o); SBAR();
            OPJ(oacc[4] = MFMA16(wa[4], ob, oacc[4]);)
            OPJ(wa[4] = WOLOAD(34816);)
            OPJ(ob = *(const bf16x8*)(otp + 3072);)
            SBAR();
            o = MFMA32(vf[4][1], BF8(p1), o); SBAR();
            PRIO(1); EXP4(sc, 0); SBAR(); PRIO(2);
            EXP4(sc, 4); PACK4(p0, sc, 0); SBAR(); EXP4(sc, 8); EXP4(sc, 12); PACK4(p1, sc, 8); SBAR(); PRIO(3);
            sc = MFMA32(kf1[6], q1, zero16); SBAR();
            sc = MFMA32K8(kf2[6], q2, sc); SBAR();
            OPJ(oacc[0] = MFMA16(wa[0], ob, oacc[0]);)
            OPJ(wa[0] = WOLOAD(35840);)
            SBAR();
            o = MFMA32(vf[5][0], BF8(p0), o); SBAR();
            OPJ(oacc[1] = MFMA16(wa[1], ob, oacc[1]);)
            OPJ(wa[1] = WOLOAD(36864);)
            SBAR();
            o = MFMA32(vf[5][1], BF8(p1), o); SBAR();
            PRIO(1); EXP4(sc, 0); SBAR(); PRIO(2);
            EXP4(sc, 4); PACK4(p0, sc, 0); SBAR(); EXP4(sc, 8); EXP4(sc, 12); PACK4(p1, sc, 8); SBAR(); PRIO(3);
            sc = MFMA32(kf1[7], q1, zero16); SBAR();
            sc = MFMA32K8(kf2[7], q2, sc); SBAR();
            OPJ(oacc[2] = MFMA16(wa[2], ob, oacc[2]);)
            OPJ(wa[2] = WOLOAD(37888);)
            SBAR();
            o = MFMA32(vf[6][0], BF8(p0), o); SBAR();
            q1n = *(const bf16x8*)(smem + K2_QL + gn * 1536 + lane16); q2n = *(const bf16x4*)(q2v + gn * 1536);
            SBAR();
            o = MFMA32(vf[6][1], BF8(p1), o); SBAR();
            PRIO(1); EXP4(sc, 0); SBAR(); PRIO(2);
            EXP4(sc, 4); PACK4(p0, sc, 0); SBAR(); EXP4(sc, 8); EXP4(sc, 12); PACK4(p1, sc, 8); SBAR(); PRIO(3);
            sc = MFMA32(kf1[0], q1n, zero16); SBAR();
            sc = MFMA32K8(kf2[0], q2n, sc); SBAR();
            OPJ(oacc[3] = MFMA16(wa[3], ob, oacc[3]);)
            OPJ(wa[3] = WOLOAD(38912);)
            SBAR();
            o = MFMA32(vf[7][0], BF8(p0), o); SBAR();
            OPJ(oacc[4] = MFMA16(wa[4], ob, oacc[4]);)
            OPJ(wa[4] = WOLOAD(39936);)
            SBAR();
            o = MFMA32(vf[7][1], BF8(p1), o); SBAR();
            q1 = q1n; q2 = q2n;
            LSTAMP(1, 6);
        }
        {
            const int gn = 6;
            PHASE_LOCALS
            FRESH_IDS
            PRIO(1); EXP4(sc, 0); SBAR(); PRIO(2); WP_ADDR(0); WP_ST(0); SBAR();
            EXP4(sc, 4); SBAR(); WP_ST(1); SBAR(); PACK4(p0, sc, 0); EXP4(sc, 8); SBAR(); WP_ST(2); SBAR(); EXP4(sc, 12); PACK4(p1, sc, 8); SBAR(); PRIO(3);
            asm volatile("" : "+v"(p0), "+v"(p1)); if (w >= 4) LOOPBAR();
            sc = MFMA32(kf1[1], q1, zero16); SBAR();
            sc = MFMA32K8(kf2[1], q2, sc); SBAR();
            o = MFMA32(vf[0][0], BF8(p0), zero16); SBAR();
            o = MFMA32(vf[0][1], BF8(p1), o); SBAR();
            asm volatile("" : "+v"(o), "+v"(sc)); if (w < 4) LOOPBAR();
            PRIO(1); EXP4(sc, 0); SBAR(); PRIO(2);
            EXP4(sc, 4); PACK4(p0, sc, 0); SBAR(); EXP4(sc, 8); EXP4(sc, 12); PACK4(p1, sc, 8); SBAR(); PRIO(3);
            sc = MFMA32(kf1[2], q1, zero16); SBAR();
            OPJ(ob = *(const bf16x8*)(otp + 3584);)
            S2(STEP2A(0);)
            SBAR();
            sc = MFMA32K8(kf2[2], q2, sc); SBAR();
            o = MFMA32(vf[1][0], BF8(p0), o); SBAR();
            OPJ(oacc[0] = MFMA16(wa[0], ob, oacc[0]);)
            OPJ(wa[0] = WOLOAD(40960);)
            SBAR();
            o = MFMA32(vf[1][1], BF8(p1), o); SBAR();
            PRIO(1); EXP4(sc, 0); SBAR(); PRIO(2);
            EXP4(sc, 4); PACK4(p0, sc, 0); SBAR(); EXP4(sc, 8); EXP4(sc, 12); PACK4(p1, sc, 8); SBAR(); PRIO(3);
            sc = MFMA32(kf1[3], q1, zero16); SBAR();
            S2(STEP2B();)
            SBAR();
            sc = MFMA32K8(kf2[3], q2, sc); SBAR();
            OPJ(oacc[1] = MFMA16(wa[1], ob, oacc[1]);)
            OPJ(wa[1] = WOLOAD(41984);)
            SBAR();
            o = MFMA32(vf[2][0], BF8(p0), o); SBAR();
            OPJ(oacc[2] = MFMA16(wa[2], ob, oacc[2]);)
            OPJ(wa[2] = WOLOAD(43008);)
            SBAR();
            o = MFMA32(vf[2][1], BF8(p1), o); SBAR();
            PRIO(1); EXP4(sc, 0); SBAR(); PRIO(2);
            EXP4(sc, 4); PACK4(p0, sc, 0); SBAR(); EXP4(sc, 8); EXP4(sc, 12); PACK4(p1, sc, 8); SBAR(); PRIO(3);
            sc = MFMA32(kf1[4], q1, zero16); SBAR();
            S2(STEP2C(gn - 2);)
            SBAR();
            sc = MFMA32K8(kf2[4], q2, sc); SBAR();
            OPJ(oacc[3] = MFMA16(wa[3], ob, oacc[3]);)
            OPJ(wa[3] = WOLOAD(44032);)
            SBAR();
            o = MFMA32(vf[3][0], BF8(p0), o); SBAR();
            OPJ(oacc[4] = MFMA16(wa[4], ob, oacc[4]);)
            OPJ(wa[4] = WOLOAD(45056);)
            OPJ(ob = *(const bf16x8*)(otp + 4096);)
            SBAR();
            o = MFMA32(vf[3][1], BF8(p1), o); SBAR();
            PRIO(1); EXP4(sc, 0); SBAR(); PRIO(2);
            EXP4(sc, 4); PACK4(p0, sc, 0); SBAR(); EXP4(sc, 8); EXP4(sc, 12); PACK4(p1, sc, 8); SBAR(); PRIO(3);
            sc = MFMA32(kf1[5], q1, zero16); SBAR();
            OPJ(oacc[0] = MFMA16(wa[0], ob, oacc[0]);)
            OPJ(wa[0] = WOLOAD(46080);)
            SBAR();
            sc = MFMA32K8(kf2[5], q2, sc); SBAR();
            OPJ(oacc[1] = MFMA16(wa[1], ob, oacc[1]);)
            OPJ(wa[1] = WOLOAD(47104);)
            SBAR();
            o = MFMA32(vf[4][0], BF8(p0), o); SBAR();
            o = MFMA32(vf[4][1], BF8(p1), o); SBAR();
            PRIO(1); EXP4(sc, 0); SBAR(); PRIO(2);
            EXP4(sc, 4); PACK4(p0, sc, 0); SBAR(); EXP4(sc, 8); EXP4(sc, 12); PACK4(p1, sc, 8); SBAR(); PRIO(3);
            sc = MFMA32(kf1[6], q1, zero16); SBAR();
            OPJ(oacc[2] = MFMA16(wa[2], ob, oacc[2]);)
            OPJ(wa[2] = WOLOAD(48128);)
            SBAR();
            sc = MFMA32K8(kf2[6], q2, sc); SBAR();
            OPJ(oacc[3] = MFMA16(wa[3], ob, oacc[3]);)
            OPJ(wa[3] = WOLOAD(49152);)
            SBAR();
            o = MFMA32(vf[5][0], BF8(p0), o); SBAR();
            OPJ(oacc[4] = MFMA16(wa[4], ob, oacc[4]);)
            OPJ(wa[4] = WOLOAD(50176);)
            OPJ(ob = *(const bf16x8*)(otp + 4608);)
            SBAR();
            o = MFMA32(vf[5][1], BF8(p1), o); SBAR();
            PRIO(1); EXP4(sc, 0); SBAR(); PRIO(2);
            EXP4(sc, 4); PACK4(p0, sc, 0); SBAR(); EXP4(sc, 8); EXP4(sc, 12); PACK4(p1, sc, 8); SBAR(); PRIO(3);
            sc = MFMA32(kf1[7], q1, zero16); SBAR();
            OPJ(oacc[0] = MFMA16(wa[0], ob, oacc[0]);)
            OPJ(wa[0] = WOLOAD(51200);)
            SBAR();
            sc = MFMA32K8(kf2[7], q2, sc); SBAR();
            OPJ(oacc[1] = MFMA16(wa[1], ob, oacc[1]);)
            OPJ(wa[1] = WOLOAD(52224);)
            SBAR();
            o = MFMA32(vf[6][0], BF8(p0), o); SBAR();
            q1n = *(const bf16x8*)(smem + K2_QL + gn * 1536 + lane16); q2n = *(const bf16x4*)(q2v + gn * 1536);
            SBAR();
            o = MFMA32(vf[6][1], BF8(p1), o); SBAR();
            PRIO(1); EXP4(sc, 0); SBAR(); PRIO(2);
            EXP4(sc, 4); PACK4(p0, sc, 0); SBAR(); EXP4(sc, 8); EXP4(sc, 12); PACK4(p1, sc, 8); SBAR(); PRIO(3);
            sc = MFMA32(kf1[0], q1n, zero16); SBAR();
            OPJ(oacc[2] = MFMA16(wa[2], ob, oacc[2]);)
            OPJ(wa[2] = WOLOAD(53248);)
            SBAR();
            sc = MFMA32K8(kf2[0], q2n, sc); SBAR();
            OPJ(oacc[3] = MFMA16(wa[3], ob, oacc[3]);)
            OPJ(wa[3] = WOLOAD(54272);)
            SBAR();
            o = MFMA32(vf[7][0], BF8(p0), o); SBAR();
            OPJ(oacc[4] = MFMA16(wa[4], ob, oacc[4]);)
            OPJ(wa[4] = WOLOAD(55296);)
            SBAR();
            o = MFMA32(vf[7][1], BF8(p1), o); SBAR();
            q1 = q1n; q2 = q2n;
            LSTAMP(1, 7);
        }
        {
            const int gn = 7;
            PHASE_LOCALS
            FRESH_IDS
            PRIO(1); EXP4(sc, 0); SBAR(); PRIO(2); WP_ADDR(1); WP_ST(0); SBAR();
            EXP4(sc, 4); SBAR(); WP_ST(1); SBAR(); PACK4(p0, sc, 0); EXP4(sc, 8); SBAR(); WP_ST(2); SBAR(); EXP4(sc, 12); PACK4(p1, sc, 8); SBAR(); PRIO(3);
            asm volatile("" : "+v"(p0), "+v"(p1)); if (w >= 4) LOOPBAR();
            sc = MFMA32(kf1[1], q1, zero16); SBAR();
            sc = MFMA32K8(kf2[1], q2, sc); SBAR();
            o = MFMA32(vf[0][0], BF8(p0), zero16); SBAR();
            o = MFMA32(vf[0][1], BF8(p1), o); SBAR();
            asm volatile("" : "+v"(o), "+v"(sc)); if (w < 4) LOOPBAR();
            PRIO(1); EXP4(sc, 0); SBAR(); PRIO(2);
            EXP4(sc, 4); PACK4(p0, sc, 0); SBAR(); EXP4(sc, 8); EXP4(sc, 12); PACK4(p1, sc, 8); SBAR(); PRIO(3);
            sc = MFMA32(kf1[2], q1, zero16); SBAR();
            OPJ(ob = *(const bf16x8*)(otp + 5120);)
            S2(STEP2A(1);)
            SBAR();
            sc = MFMA32K8(kf2[2], q2, sc); SBAR();
            o = MFMA32(vf[1][0], BF8(p0), o); SBAR();
            OPJ(oacc[0] = MFMA16(wa[0], ob, oacc[0]);)
            OPJ(wa[0] = WOLOAD(56320);)
            SBAR();
            o = MFMA32(vf[1][1], BF8(p1), o); SBAR();
            PRIO(1); EXP4(sc, 0); SBAR(); PRIO(2);
            EXP4(sc, 4); PACK4(p0, sc, 0); SBAR(); EXP4(sc, 8); EXP4(sc, 12); PACK4(p1, sc, 8); SBAR(); PRIO(3);
            sc = MFMA32(kf1[3], q1, zero16); SBAR();
            S2(STEP2B();)
            SBAR();
            sc = MFMA32K8(kf2[3], q2, sc); SBAR();
            o = MFMA32(vf[2][0], BF8(p0), o); SBAR();
            OPJ(oacc[1] = MFMA16(wa[1], ob, oacc[1]);)
            OPJ(wa[1] = WOLOAD(57344);)
            SBAR();
            o = MFMA32(vf[2][1], BF8(p1), o); SBAR();
            PRIO(1); EXP4(sc, 0); SBAR(); PRIO(2);
            EXP4(sc, 4); PACK4(p0, sc, 0); SBAR(); EXP4(sc, 8); EXP4(sc, 12); PACK4(p1, sc, 8); SBAR(); PRIO(3);
            sc = MFMA32(kf1[4], q1, zero16); SBAR();
            S2(STEP2C(gn - 2);)
            SBAR();
            sc = MFMA32K8(kf2[4], q2, sc); SBAR();
            o = MFMA32(vf[3][0], BF8(p0), o); SBAR();
            OPJ(oacc[2] = MFMA16(wa[2], ob, oacc[2]);)
            OPJ(wa[2] = WOLOAD(58368);)
            SBAR();
            o = MFMA32(vf[3][1], BF8(p1), o); SBAR();
            PRIO(1); EXP4(sc, 0); SBAR(); PRIO(2);
            EXP4(sc, 4); PACK4(p0, sc, 0); SBAR(); EXP4(sc, 8); EXP4(sc, 12); PACK4(p1, sc, 8); SBAR(); PRIO(3);
            sc = MFMA32(kf1[5], q1, zero16); SBAR();
            sc = MFMA32K8(kf2[5], q2, sc); SBAR();
            OPJ(oacc[3] = MFMA16(wa[3], ob, oacc[3]);)
            OPJ(wa[3] = WOLOAD(59392);)
            SBAR();
            o = MFMA32(vf[4][0], BF8(p0), o); SBAR();
            OPJ(oacc[4] = MFMA16(wa[4], ob, oacc[4]);)
            OPJ(wa[4] = WOLOAD(60416);)
            OPJ(ob = *(const bf16x8*)(otp + 5632);)
            SBAR();
            o = MFMA32(vf[4][1], BF8(p1), o); SBAR();
            PRIO(1); EXP4(sc, 0); SBAR(); PRIO(2);
            EXP4(sc, 4); PACK4(p0, sc, 0); SBAR(); EXP4(sc, 8); EXP4(sc, 12); PACK4(p1, sc, 8); SBAR(); PRIO(3);
            sc = MFMA32(kf1[6], q1, zero16); SBAR();
            sc = MFMA32K8(kf2[6], q2, sc); SBAR();
            OPJ(oacc[0] = MFMA16(wa[0], ob, oacc[0]);)
            OPJ(wa[0] = WOLOAD(61440);)
            SBAR();
            o = MFMA32(vf[5][0], BF8(p0), o); SBAR();
            OPJ(oacc[1] = MFMA16(wa[1], ob, oacc[1]);)
            OPJ(wa[1] = WOLOAD(62464);)
            SBAR();
            o = MFMA32(vf[5][1], BF8(p1), o); SBAR();
            PRIO(1); EXP4(sc, 0); SBAR(); PRIO(2);
            EXP4(sc, 4); PACK4(p0, sc, 0); SBAR(); EXP4(sc, 8); EXP4(sc, 12); PACK4(p1, sc, 8); SBAR(); PRIO(3);
            sc = MFMA32(kf1[7], q1, zero16); SBAR();
            sc = MFMA32K8(kf2[7], q2, sc); SBAR();
            OPJ(oacc[2] = MFMA16(wa[2], ob, oacc[2]);)
            OPJ(wa[2] = WOLOAD(63488);)
            SBAR();
            o = MFMA32(vf[6][0], BF8(p0), o); SBAR();
            q1n = *(const bf16x8*)(smem + K2_QL + gn * 1536 + lane16); q2n = *(const bf16x4*)(q2v + gn * 1536);
            SBAR();
            o = MFMA32(vf[6][1], BF8(p1), o); SBAR();
            PRIO(1); EXP4(sc, 0); SBAR(); PRIO(2);
            EXP4(sc, 4); PACK4(p0, sc, 0); SBAR(); EXP4(sc, 8); EXP4(sc, 12); PACK4(p1, sc, 8); SBAR(); PRIO(3);
            sc = MFMA32(kf1[0], q1n, zero16); SBAR();
            sc = MFMA32K8(kf2[0], q2n, sc); SBAR();
            OPJ(oacc[3] = MFMA16(wa[3], ob, oacc[3]);)
            OPJ(wa[3] = WOLOAD(64512);)
            SBAR();
            o = MFMA32(vf[7][0], BF8(p0), o); SBAR();
            OPJ(oacc[4] = MFMA16(wa[4], ob, oacc[4]);)
            OPJ(wa[4] = WOLOAD(65536);)
            SBAR();
            o = MFMA32(vf[7][1], BF8(p1), o); SBAR();
            q1 = q1n; q2 = q2n;
            LSTAMP(1, 8);
        }

        bf16x8 wl[10];
        {
            const int gn = 8;
            PHASE_LOCALS
            PRIO(1); EXP4(sc, 0); SBAR(); PRIO(2); WP_ADDR(0); WP_ST(0); SBAR();
            EXP4(sc, 4); SBAR(); WP_ST(1); SBAR(); PACK4(p0, sc, 0); EXP4(sc, 8); SBAR(); WP_ST(2); SBAR(); EXP4(sc, 12); PACK4(p1, sc, 8); SBAR(); PRIO(3);
            asm volatile("" : "+v"(p0), "+v"(p1)); if (w >= 4) LOOPBAR();
            sc = MFMA32(kf1[1], q1, zero16); SBAR();
            sc = MFMA32K8(kf2[1], q2, sc); SBAR();
            o = MFMA32(vf[0][0], BF8(p0), zero16); SBAR();
            o = MFMA32(vf[0][1], BF8(p1), o); SBAR();
            asm volatile("" : "+v"(o), "+v"(sc)); if (w < 4) LOOPBAR();
            PRIO(1); EXP4(sc, 0); SBAR(); PRIO(2);
            EXP4(sc, 4); PACK4(p0, sc, 0); SBAR(); EXP4(sc, 8); EXP4(sc, 12); PACK4(p1, sc, 8); SBAR(); PRIO(3);
            sc = MFMA32(kf1[2], q1, zero16); SBAR();
            OPJ(ob = *(const bf16x8*)(otp + 6144);)
            S2(STEP2A(0);)
            SBAR();
            sc = MFMA32K8(kf2[2], q2, sc); SBAR();
            o = MFMA32(vf[1][0], BF8(p0), o); SBAR();
            OPJ(oacc[0] = MFMA16(wa[0], ob, oacc[0]);)
            OPJ(wa[0] = WOLOAD(66560);)
            SBAR();
            o = MFMA32(vf[1][1], BF8(p1), o); SBAR();
            PRIO(1); EXP4(sc, 0); SBAR(); PRIO(2);
            EXP4(sc, 4); PACK4(p0, sc, 0); SBAR(); EXP4(sc, 8); EXP4(sc, 12); PACK4(p1, sc, 8); SBAR(); PRIO(3);
            sc = MFMA32(kf1[3], q1, zero16); SBAR();
            S2(STEP2B();)
            wl[0] = WOLOAD(81920);
            SBAR();
            sc = MFMA32K8(kf2[3], q2, sc); SBAR();
            OPJ(oacc[1] = MFMA16(wa[1], ob, oacc[1]);)
            OPJ(wa[1] = WOLOAD(67584);)
            SBAR();
            o = MFMA32(vf[2][0], BF8(p0), o); SBAR();
            OPJ(oacc[2] = MFMA16(wa[2], ob, oacc[2]);)
            OPJ(wa[2] = WOLOAD(68608);)
            SBAR();
            o = MFMA32(vf[2][1], BF8(p1), o); SBAR();
            PRIO(1); EXP4(sc, 0); SBAR(); PRIO(2);
            EXP4(sc, 4); PACK4(p0, sc, 0); SBAR(); EXP4(sc, 8); EXP4(sc, 12); PACK4(p1, sc, 8); SBAR(); PRIO(3);
            sc = MFMA32(kf1[4], q1, zero16); SBAR();
            S2(STEP2C(gn - 2);)
            wl[1] = WOLOAD(82944);
            SBAR();
            sc = MFMA32K8(kf2[4], q2, sc); SBAR();
            OPJ(oacc[3] = MFMA16(wa[3], ob, oacc[3]);)
            OPJ(wa[3] = WOLOAD(69632);)
            SBAR();
            o = MFMA32(vf[3][0], BF8(p0), o); SBAR();
            OPJ(oacc[4] = MFMA16(wa[4], ob, oacc[4]);)
            OPJ(wa[4] = WOLOAD(70656);)
            OPJ(ob = *(const bf16x8*)(otp + 6656);)
            wl[2] = WOLOAD(83968);
            SBAR();
            o = MFMA32(vf[3][1], BF8(p1), o); SBAR();
            PRIO(1); EXP4(sc, 0); SBAR(); PRIO(2);
            EXP4(sc, 4); PACK4(p0, sc, 0); SBAR(); EXP4(sc, 8); EXP4(sc, 12); PACK4(p1, sc, 8); SBAR(); PRIO(3);
            sc = MFMA32(kf1[5], q1, zero16); SBAR();
            OPJ(oacc[0] = MFMA16(wa[0], ob, oacc[0]);)
            OPJ(wa[0] = WOLOAD(71680);)
            wl[3] = WOLOAD(84992);
            SBAR();
            sc = MFMA32K8(kf2[5], q2, sc); SBAR();
            OPJ(oacc[1] = MFMA16(wa[1], ob, oacc[1]);)
            OPJ(wa[1] = WOLOAD(72704);)
            SBAR();
            o = MFMA32(vf[4][0], BF8(p0), o); SBAR();
            wl[4] = WOLOAD(86016);
            SBAR();
            o = MFMA32(vf[4][1], BF8(p1), o); SBAR();
            PRIO(1); EXP4(sc, 0); SBAR(); PRIO(2);
            EXP4(sc, 4); PACK4(p0, sc, 0); SBAR(); EXP4(sc, 8); EXP4(sc, 12); PACK4(p1, sc, 8); SBAR(); PRIO(3);
            sc = MFMA32(kf1[6], q1, zero16); SBAR();
            OPJ(oacc[2] = MFMA16(wa[2], ob, oacc[2]);)
            OPJ(wa[2] = WOLOAD(73728);)
            wl[5] = WOLOAD(87040);
            SBAR();
            sc = MFMA32K8(kf2[6], q2, sc); SBAR();
            OPJ(oacc[3] = MFMA16(wa[3], ob, oacc[3]);)
            OPJ(wa[3] = WOLOAD(74752);)
            SBAR();
            o = MFMA32(vf[5][0], BF8(p0), o); SBAR();
            OPJ(oacc[4] = MFMA16(wa[4], ob, oacc[4]);)
            OPJ(wa[4] = WOLOAD(75776);)
            OPJ(ob = *(const bf16x8*)(otp + 7168);)
            wl[6] = WOLOAD(88064);
            SBAR();
            o = MFMA32(vf[5][1], BF8(p1), o); SBAR();
            PRIO(1); EXP4(sc, 0); SBAR(); PRIO(2);
            EXP4(sc, 4); PACK4(p0, sc, 0); SBAR(); EXP4(sc, 8); EXP4(sc, 12); PACK4(p1, sc, 8); SBAR(); PRIO(3);
            sc = MFMA32(kf1[7], q1, zero16); SBAR();
            OPJ(oacc[0] = MFMA16(wa[0], ob, oacc[0]);)
            OPJ(wa[0] = WOLOAD(76800);)
            wl[7] = WOLOAD(89088);
            SBAR();
            sc = MFMA32K8(kf2[7], q2, sc); SBAR();
            OPJ(oacc[1] = MFMA16(wa[1], ob, oacc[1]);)
            OPJ(wa[1] = WOLOAD(77824);)
            SBAR();
            o = MFMA32(vf[6][0], BF8(p0), o); SBAR();
            wl[8] = WOLOAD(90112);
            SBAR();
            o = MFMA32(vf[6][1], BF8(p1), o); SBAR();
            PRIO(1); EXP4(sc, 0); SBAR(); PRIO(2);
            EXP4(sc, 4); PACK4(p0, sc, 0); SBAR(); EXP4(sc, 8); EXP4(sc, 12); PACK4(p1, sc, 8); SBAR(); PRIO(3);
            OPJ(oacc[2] = MFMA16(wa[2], ob, oacc[2]);)
            OPJ(wa[2] = WOLOAD(78848);)
            wl[9] = WOLOAD(91136);
            SBAR();
            OPJ(oacc[3] = MFMA16(wa[3], ob, oacc[3]);)
            OPJ(wa[3] = WOLOAD(79872);)
            SBAR();
            o = MFMA32(vf[7][0], BF8(p0), o); SBAR();
            OPJ(oacc[4] = MFMA16(wa[4], ob, oacc[4]);)
            OPJ(wa[4] = WOLOAD(80896);)
            SBAR();
            o = MFMA32(vf[7][1], BF8(p1), o); SBAR();

            STAMP(1, 9); RSTAMP(29);
            otp += 7680;
        }
        PRIO(0);
        XWLOAD();
        WRITE_PARTIAL(1);
        bf16x8 wt[10];
        {
            const char* tp = smem + K2_TAIL + w * 10240 + OPQ16();
#pragma unroll
            for (int f = 0; f < 10; ++f) wt[f] = *(const bf16x8*)(tp + f * 1024);
        }
        BAR_LDS();
        STAMP(1, 12);
        {
            PHASE_LOCALS
            STEP2A(1); SBAR();
            const bf16x8 ob0 = *(const bf16x8*)(otp), ob1 = *(const bf16x8*)(otp + 512);
#pragma unroll
            for (int t = 0; t < 5; ++t) oacc[t] = MFMA16(wa[t], ob0, oacc[t]);
            STEP2B(); STEP2C(7);
            if (badm != 0ull) *(unsigned*)(smem + K2_MISC) = 1u;
#pragma unroll
            for (int t = 0; t < 5; ++t) oacc[t] = MFMA16(wl[t], ob1, oacc[t]);
            asm volatile("" : "+v"(oacc[0]), "+v"(oacc[1]), "+v"(oacc[2]), "+v"(oacc[3]), "+v"(oacc[4]));
        }
        BAR_LDS();
        STAMP(1, 13);
        {
            const bf16x8 ob2 = *(const bf16x8*)(otp + 1024), ob3 = *(const bf16x8*)(otp + 1536), ob4 = *(const bf16x8*)(otp + 2048);
#pragma unroll
            for (int t = 0; t < 5; ++t) oacc[t] = MFMA16(wl[5 + t], ob2, oacc[t]);
#pragma unroll
            for (int t = 0; t < 5; ++t) oacc[t] = MFMA16(wt[t], ob3, oacc[t]);
#pragma unroll
            for (int t = 0; t < 5; ++t) oacc[t] = MFMA16(wt[5 + t], ob4, oacc[t]);
        }
        const unsigned bad = *(const unsigned*)(smem + K2_MISC);
        asm volatile("" : "+v"(xa), "+v"(xb1), "+v"(wa4), "+v"(wb1));
        if (__builtin_amdgcn_readfirstlane(bad) == 0u) break;

        {
            FRESH_IDS
            const unsigned l16 = OPQ16();
            const unsigned lane = l16 >> 4, r32 = lane & 31, hi = lane >> 5;
            for (int g = 0; g < 8; ++g) {
                const bf16x8 qa = *(const bf16x8*)(smem + K2_QL + g * 1536 + l16);
                bf16x4 qb = *(const bf16x4*)(smem + K2_QL + g * 1536 + 1024 + (l16 >> 1));
                if (hi == 1) qb = (bf16x4){0, 0, 0, 0};
                float m = -INFINITY;
                for (int t = 0; t < 8; ++t) {
                    const bf16x8 ka = *(const bf16x8*)(ws + OFF_KF1 + (size_t)(w * 8 + t) * 1024 + l16);
                    const bf16x4 kb = *(const bf16x4*)(ws + OFF_KF2 + (size_t)((w * 8 + t) * 32 + r32) * 8);
                    f32x16 s = MFMA32(ka, qa, zero16);
                    s = MFMA32K8(kb, qb, s);
                    m = fmaxf(m, max16(s));
                }
                m = max32(m);
                if (hi == 0) *(float*)(smem + K2_PBUF + ((w * 8 + g) * 32 + r32) * 4) = m;
            }
            BAR_LDS();
            if (hi == 1) {
                float m = -INFINITY;
#pragma unroll
                for (int u = 0; u < 8; ++u) m = fmaxf(m, *(const float*)(smem + K2_PBUF + ((u * 8 + w) * 32 + r32) * 4));
                ushort_t a, b2, c; split3(-m, a, b2, c);
                *(bf16x4*)(smem + K2_QL + w * 1536 + 1024 + (l16 >> 1)) = (bf16x4){(short)a, (short)b2, (short)c, 0};
            }
        }
        asm volatile("" : "=v"(q1w), "=v"(q2w), "=v"(k1w), "=v"(k2w));
    }

    STAMP(1, 10); RSTAMP(24);
    FRESH_IDS
    const int lane = OPQ16() >> 4, l15 = lane & 15, lg = lane >> 4;
    if (l15 < 8) {
#pragma unroll
        for (int t = 0; t < 5; ++t) *(f32x4*)(smem + K2_STG + l15 * 2576 + (w * 80 + t * 16 + lg * 4) * 4) = oacc[t];
    }
    BAR_LDS();
    STAMP(1, 15);
    unsigned hp[5], hnp[5], op[5];
    f32x2 hf[5];
    f32x2 sum2 = {0.f, 0.f};
    {
        const f32x4 a0 = *(const f32x4*)(smem + K2_STG + w * 2576 + lane * 32), a1 = *(const f32x4*)(smem + K2_STG + w * 2576 + lane * 32 + 16);
        const f32x2 a2 = *(const f32x2*)(smem + K2_STG + w * 2576 + 2048 + lane * 8);
        const f32x2 av[5] = {{a0[0], a0[1]}, {a0[2], a0[3]}, {a1[0], a1[1]}, {a1[2], a1[3]}, a2};
#pragma unroll
        for (int i = 0; i < 5; ++i) {
            hp[i] = cvtpk2(unpk(i < 4 ? xa[i] : xb1) + unpk(cvtpk2(av[i])));
            hf[i] = unpk(hp[i]);
        }
    }
    const float c0 = __builtin_bit_cast(float, __builtin_amdgcn_readfirstlane(__builtin_bit_cast(int, hf[0][0])));
    f32x2 sq2 = {0.f, 0.f};
#pragma unroll
    for (int i = 0; i < 5; ++i) { const f32x2 d = hf[i] - c0; sum2 += d; sq2 += d * d; }
    float sd = sum2[0] + sum2[1], sq = sq2[0] + sq2[1];
    wave_sum2(sd, sq);
    sd *= (1.0f / DM);
    const float mean = c0 + sd;
    const float var = fmaxf(sq * (1.0f / DM) - sd * sd, 0.f);
    const float mb = rbf(mean);
    const float sig = rbf(__builtin_amdgcn_sqrtf(rbf(rbf(var) + 1e-5f)));
    const float rsig = __builtin_amdgcn_rcpf(sig);
    f32x2 rp2 = {0.f, 0.f};
    const int token = wg * 8 + w;
#pragma unroll
    for (int i = 0; i < 5; ++i) {
        hnp[i] = cvtpk2(unpk(cvtpk2(hf[i] - mb)) * rsig);
        const f32x2 hn2 = unpk(hnp[i]);
        rp2 += hn2 * unpk(i < 4 ? wa4[i] : wb1);
        op[i] = cvtpk2(hf[i] + hn2);
    }
    const float rsc = wave_sum(rp2[0] + rp2[1]);
    const unsigned rb = f2bf(rsc);
    const unsigned key = (((rb & 0x8000u) ? (~rb & 0xffffu) : (rb | 0x8000u)) << 16) | (unsigned)(2047 - token);
    if (lane == 0) *(unsigned*)(smem + K2_MISC + 16 + w * 4) = key;
    STAMP(1, 19);
    BAR_LDS();
    STAMP(1, 20);
    bool cand;
    {
        const u32x4 ka = *(const u32x4*)(smem + K2_MISC + 16), kb = *(const u32x4*)(smem + K2_MISC + 32);
        const unsigned best = max(max(max(ka[0], ka[1]), max(ka[2], ka[3])), max(max(kb[0], kb[1]), max(kb[2], kb[3])));
        cand = __builtin_amdgcn_readfirstlane(best) == __builtin_amdgcn_readfirstlane(key);
    }
    char* orow = (char*)(out + (size_t)token * DM);
#define OUT_ROW() do { *(u32x4*)(orow + lane * 16) = (u32x4){op[0], op[1], op[2], op[3]}; *(unsigned*)(orow + 1024 + lane * 4) = op[4]; } while (0)
    if (cand) {
        if (lane == 0) __hip_atomic_store((GAS unsigned long long*)(ws + OFF_KEYG) + wg, ((unsigned long long)GTAG << 32) | key, __ATOMIC_RELAXED, __HIP_MEMORY_SCOPE_AGENT);
        const int ro = (int)OFF_CROW + wg * 5120;
        __builtin_amdgcn_raw_buffer_store_b128((u32x4){hnp[0], GTAG, hnp[1], GTAG}, wsr, lane * 32, ro, 16);
        __builtin_amdgcn_raw_buffer_store_b128((u32x4){hnp[2], GTAG, hnp[3], GTAG}, wsr, lane * 32 + 16, ro, 16);
        __builtin_amdgcn_raw_buffer_store_b64((u32x2){hnp[4], GTAG}, wsr, lane * 8, ro + 2048, 16);
        __builtin_amdgcn_raw_buffer_store_b128((u32x4){hp[0], GTAG, hp[1], GTAG}, wsr, lane * 32, ro + 2560, 16);
        __builtin_amdgcn_raw_buffer_store_b128((u32x4){hp[2], GTAG, hp[3], GTAG}, wsr, lane * 32 + 16, ro + 2560, 16);
        __builtin_amdgcn_raw_buffer_store_b64((u32x2){hp[4], GTAG}, wsr, lane * 8, ro + 4608, 16);
    }
    if (!cand) OUT_ROW();
    if (w < 7) {
        if (bi < 216) {
            const char* gp = (const char*)((lane & 1) ? w_up : w_gate) + (size_t)(lane >> 1) * 3456 + gcb * 16;
#pragma unroll
            for (int i = 0; i < 3; ++i) { const int in = min(w + 7 * i, 19); glds16(gp + (size_t)in * 110592, lds0 + K2_GU + in * 1024); }
        } else {
            const unsigned vo2 = (lane >> 1) * 1280 + (lane & 1) * 16;
#pragma unroll
            for (int i = 0; i < 8; ++i) { const int in = min(w + 7 * i, 53); glds16s((const char*)w_down + dcb * 32 + (size_t)in * 40960, vo2, lds0 + K2_TAIL + in * 1024); }
        }
    }
    STAMP(1, 11); RSTAMP(27);
    const int rep = (int)OFF_REPL + (bi & 7) * 256;
    if (w == 7) {
        if (bi < 8) {
            unsigned best = 0;
            for (unsigned spins = 0; spins < (1u << 16); ++spins) {
                bool ok = true; best = 0;
#pragma unroll
                for (int i = 0; i < 2; ++i) {
                    const u32x4 g = __builtin_amdgcn_raw_buffer_load_b128(wsr, lane * 16, (int)OFF_KEYG + 1024 * i, 16);
                    ok = ok && g[1] == GTAG && g[3] == GTAG;
                    best = max(best, max(g[0], g[2]));
                }
                if (__all(ok)) break;
                __builtin_amdgcn_s_sleep(1);
            }
            best = wave_umax(best);
            if (lane == 0) { __builtin_amdgcn_raw_buffer_store_b64((u32x2){best, GTAG}, wsr, 0, rep, 16); *(unsigned*)(smem + K2_MISC + 64) = best; }
        } else {
            u32x2 gk;
            for (unsigned spins = 0; spins < (1u << 16); ++spins) {
                gk = __builtin_amdgcn_raw_buffer_load_b64(wsr, 0, rep, 16);
                if (__builtin_amdgcn_readfirstlane(gk[1]) == GTAG) break;
                __builtin_amdgcn_s_sleep(1);
            }
            if (lane == 0) *(unsigned*)(smem + K2_MISC + 64) = gk[0];
        }
    }
    asm volatile("s_waitcnt vmcnt(0) lgkmcnt(0)\n\ts_barrier" ::: "memory");
    const unsigned wkey = __builtin_amdgcn_readfirstlane(*(const unsigned*)(smem + K2_MISC + 64));
    const int wtok = 2047 - (int)(wkey & 0xffffu);
    const int wro = (int)OFF_CROW + (wtok >> 3) * 5120;
    u32x4 xb[3];
    if (bi < 216) {
        for (unsigned spins = 0; spins < (1u << 16); ++spins) {
            bool ok = true;
#pragma unroll
            for (int i = 0; i < 3; ++i) {
                const int so = wro + min(w + 8 * i, 19) * 128;
                const u32x4 g0 = __builtin_amdgcn_raw_buffer_load_b128(wsr, lg * 32, so, 16);
                const u32x4 g1 = __builtin_amdgcn_raw_buffer_load_b128(wsr, lg * 32 + 16, so, 16);
                ok = ok && g0[1] == GTAG && g0[3] == GTAG && g1[1] == GTAG && g1[3] == GTAG;
                xb[i] = (u32x4){g0[0], g0[2], g1[0], g1[2]};
            }
            if (__all(ok)) break;
            __builtin_amdgcn_s_sleep(1);
        }
    }
    STAMP(1, 16); RSTAMP(26);
    if (bi < 216) {
        f32x4 acc = {0, 0, 0, 0};
        const unsigned ga = lds0 + K2_GU + (8 * lg + (l15 >> 2)) * 32 + (l15 & 3) * 8;
#pragma unroll
        for (int i = 0; i < 3; ++i) {
            if (w + 8 * i < 20) {
                const bf16x4 lo = trread(ga + (w + 8 * i) * 1024), hi = trread(ga + (w + 8 * i) * 1024 + 128);
                acc = MFMA16(__builtin_shufflevector(lo, hi, 0, 1, 2, 3, 4, 5, 6, 7), __builtin_bit_cast(bf16x8, xb[i]), acc);
            }
        }
        if (l15 == 0) *(f32x4*)(smem + K2_MISC + 128 + w * 64 + lg * 16) = acc;
    }
    if (cand && token != wtok) OUT_ROW();
    if (bi < 216) {
        BAR_LDS();
        if (w == 0) {
            float g = 0.f, u = 0.f;
#pragma unroll
            for (int v = 0; v < 8; ++v) { g += *(const float*)(smem + K2_MISC + 128 + v * 64 + (lane & 7) * 4); u += *(const float*)(smem + K2_MISC + 128 + v * 64 + 32 + (lane & 7) * 4); }
            g = rbf(g); u = rbf(u);
            const float sg = rbf(g * __builtin_amdgcn_rcpf(1.0f + __expf(-g)));
            const unsigned a = f2bf(sg * u);
            const unsigned an = (unsigned)__builtin_amdgcn_update_dpp(0, (int)a, 0xB1, 0xf, 0xf, true);
            if (lane < 8 && (lane & 1) == 0)
                __hip_atomic_store((GAS unsigned long long*)(ws + OFF_ACTG) + gcb * 4 + (lane >> 1), ((unsigned long long)GTAG << 32) | a | (an << 16), __ATOMIC_RELAXED, __HIP_MEMORY_SCOPE_AGENT);
        }
    }
    STAMP(1, 17); RSTAMP(25);
    if (bi >= 216) {
        bf16x8 af[7];
        const unsigned da = lds0 + K2_TAIL + (8 * lg + (l15 >> 2)) * 32 + (l15 & 3) * 8;
#pragma unroll
        for (int i = 0; i < 7; ++i) {
            const int ks = min(w + 8 * i, 53);
            const bf16x4 lo = trread(da + ks * 1024), hi = trread(da + ks * 1024 + 128);
            af[i] = __builtin_shufflevector(lo, hi, 0, 1, 2, 3, 4, 5, 6, 7);
        }
        unsigned hh = 0, hnn = 0;
        if (w == 1) {
            u32x4 g[7];
            for (unsigned spins = 0; spins < (1u << 16); ++spins) {
                bool ok = true;
#pragma unroll
                for (int i = 0; i < 7; ++i) {
                    g[i] = __builtin_amdgcn_raw_buffer_load_b128(wsr, min(lane * 16 + i * 1024, 6896), (int)OFF_ACTG, 16);
                    ok = ok && g[i][1] == GTAG && g[i][3] == GTAG;
                }
                if (__all(ok)) break;
                __builtin_amdgcn_s_sleep(1);
            }
#pragma unroll
            for (int i = 0; i < 7; ++i) *(u32x2*)(smem + K2_ACT + min(lane * 8 + i * 512, 3448)) = (u32x2){g[i][0], g[i][2]};
        }
        if (w == 0) {
            for (unsigned spins = 0; spins < (1u << 16); ++spins) {
                const u32x2 g0 = __builtin_amdgcn_raw_buffer_load_b64(wsr, (lane & 7) * 8, wro + dcb * 64, 16);
                const u32x2 g1 = __builtin_amdgcn_raw_buffer_load_b64(wsr, (lane & 7) * 8, wro + 2560 + dcb * 64, 16);
                hnn = g0[0]; hh = g1[0];
                if (__all(g0[1] == GTAG && g1[1] == GTAG)) break;
                __builtin_amdgcn_s_sleep(1);
            }
        }
        asm volatile("s_waitcnt vmcnt(0) lgkmcnt(0)\n\ts_barrier" ::: "memory");
        RSTAMP(23);
        f32x4 acc = {0, 0, 0, 0};
#pragma unroll
        for (int i = 0; i < 7; ++i) {
            if (w + 8 * i < 54) {
                const bf16x8 ab = *(const bf16x8*)(smem + K2_ACT + (w + 8 * i) * 64 + lg * 16);
                acc = MFMA16(af[i], ab, acc);
            }
        }
        if (l15 == 0) *(f32x4*)(smem + K2_MISC + 128 + w * 64 + lg * 16) = acc;
        BAR_LDS();
        if (w == 0 && lane < 8) {
            f32x2 y = {0.f, 0.f};
#pragma unroll
            for (int v = 0; v < 8; ++v) y += *(const f32x2*)(smem + K2_MISC + 128 + v * 64 + lane * 8);
            const unsigned ord = wkey >> 16;
            const float r = bf2f((ushort_t)((ord & 0x8000u) ? (ord & 0x7fffu) : (~ord & 0xffffu)));
            const float sg = rbf(__builtin_amdgcn_rcpf(1.0f + __expf(-r)));
            const f32x2 yy = unpk(cvtpk2(unpk(cvtpk2(y)) * sg));
            *(unsigned*)(out + (size_t)wtok * DM + dcb * 16 + lane * 2) = cvtpk2(unpk(hh) + unpk(cvtpk2(unpk(hnn) + yy)));
        }
    }
    asm volatile(".globl k2_code_end_mark\n\t.protected k2_code_end_mark\nk2_code_end_mark:");
    STAMP(1, 18); RSTAMP(31);
    STAMP_FLUSH(1);
#undef DIAG_LDS
}

extern "C" void kernel_launch(void* const* d_in, const int* in_sizes, int n_in,
                              void* d_out, int out_size, void* d_ws, size_t ws_size,
                              hipStream_t stream) {
    const ushort_t* x = (const ushort_t*)d_in[0];
    const ushort_t* wq = (const ushort_t*)d_in[1];
    const ushort_t* wk = (const ushort_t*)d_in[2];
    const ushort_t* wv = (const ushort_t*)d_in[3];
    const ushort_t* wo = (const ushort_t*)d_in[4];
    const ushort_t* w_r = (const ushort_t*)d_in[6];
    const ushort_t* w_gate = (const ushort_t*)d_in[7];
    const ushort_t* w_up = (const ushort_t*)d_in[8];
    const ushort_t* w_down = (const ushort_t*)d_in[9];
    char* ws = (char*)d_ws;
    ushort_t* out = (ushort_t*)d_out;
    (void)hipFuncSetAttribute((const void*)k1_qkv, hipFuncAttributeMaxDynamicSharedMemorySize, K1_LDS_BYTES);
    (void)hipFuncSetAttribute((const void*)k2_attn, hipFuncAttributeMaxDynamicSharedMemorySize, K2_LDS_BYTES);
    hipLaunchKernelGGL(k1_qkv, dim3(256), dim3(768), K1_LDS_BYTES, stream, x, wq, wk, wv, wo, w_gate, w_up, w_down, ws);
    hipLaunchKernelGGL(k2_attn, dim3(256), dim3(512), K2_LDS_BYTES, stream, x, w_r, w_gate, w_up, w_down, ws, out);
}
```

```cpp
#include <hip/hip_runtime.h>
#include <hip/hip_bf16.h>
#include <stdint.h>

typedef short bf16x8 __attribute__((ext_vector_type(8)));
typedef short bf16x4 __attribute__((ext_vector_type(4)));
typedef float f32x2 __attribute__((ext_vector_type(2)));
typedef float f32x4 __attribute__((ext_vector_type(4)));
typedef float f32x16 __attribute__((ext_vector_type(16)));
typedef unsigned u32x4 __attribute__((ext_vector_type(4)));
typedef unsigned u32x2 __attribute__((ext_vector_type(2)));
typedef unsigned short ushort_t;
#define LAS __attribute__((address_space(3)))
#define GAS __attribute__((address_space(1)))
#define DI __device__ __forceinline__

constexpr int NTOK = 2048, DM = 640, NHEAD = 32, DH = 20, FF = 1728;

constexpr size_t OFF_QF1  = 0;
constexpr size_t OFF_QF2  = OFF_QF1 + 2097152;
constexpr size_t OFF_KF1  = OFF_QF2 + 524288;
constexpr size_t OFF_KF2  = OFF_KF1 + 65536;
constexpr size_t OFF_VF   = OFF_KF2 + 16384;
constexpr size_t OFF_WOF  = OFF_VF + 131072;
constexpr size_t OFF_KEYG = OFF_WOF + 819200;
constexpr size_t OFF_ACTG = OFF_KEYG + 2048;
constexpr size_t OFF_CROW = OFF_KEYG + 9216;
constexpr size_t OFF_REPL = OFF_CROW + 1310720;
constexpr size_t OFF_CONST = OFF_REPL + 2048;
constexpr unsigned GTAG = 0x600DF00Du;
constexpr size_t OFF_DIAG = 64u << 20;

#define STAMP(kid, idx) do {} while (0)
#define STAMP_INIT() do {} while (0)
#define RSTAMP(idx) do {} while (0)
#define STAMP_FLUSH(kid) do {} while (0)
#define DIAG_BYTES 0
DI float bf2f(ushort_t b) { return __uint_as_float(((unsigned)b) << 16); }
typedef __bf16 bf16x2_t __attribute__((ext_vector_type(2)));
DI unsigned cvtpk(float lo, float hi) { return __builtin_bit_cast(unsigned, __builtin_convertvector((f32x2){lo, hi}, bf16x2_t)); }
DI unsigned cvtpk2(f32x2 v) { return __builtin_bit_cast(unsigned, __builtin_convertvector(v, bf16x2_t)); }
DI f32x2 unpk(unsigned p) { return (f32x2){__uint_as_float(p << 16), __uint_as_float(p & 0xffff0000u)}; }
DI float rbf(float x) { return __uint_as_float(cvtpk(x, x) << 16); }
DI ushort_t f2bf(float x) { return (ushort_t)(cvtpk(x, x) & 0xffffu); }
DI float dot2acc(unsigned a, unsigned b, float c) { asm("v_dot2c_f32_bf16 %0, %1, %2" : "+v"(c) : "v"(a), "v"(b)); return c; }
DI void glds16(const void* g, unsigned lds_base) {
    unsigned sv;
    asm volatile("s_mov_b32 %0, m0\n\ts_mov_b32 m0, %2\n\ts_nop 0\n\tglobal_load_lds_dwordx4 %1, off\n\ts_mov_b32 m0, %0"
                 : "=&s"(sv) : "v"(g), "s"(lds_base) : "memory");
}
DI void glds16s(const char* sbase, unsigned voff, unsigned lds_base) {
    unsigned sv;
    asm volatile("s_mov_b32 %0, m0\n\ts_mov_b32 m0, %3\n\ts_nop 0\n\tglobal_load_lds_dwordx4 %1, %2\n\ts_mov_b32 m0, %0"
                 : "=&s"(sv) : "v"(voff), "s"(sbase), "s"(lds_base) : "memory");
}
#define SBAR() __builtin_amdgcn_sched_barrier(0)
#define BAR_LDS() asm volatile("s_waitcnt lgkmcnt(0)\n\ts_barrier" ::: "memory")
#define MFMA32(a, b, c) __builtin_amdgcn_mfma_f32_32x32x16_bf16((a), (b), (c), 0, 0, 0)
#define MFMA32K8(a, b, c) __builtin_amdgcn_mfma_f32_32x32x8bf16_1k((a), (b), (c), 0, 0, 0)
#define MFMA16(a, b, c) __builtin_amdgcn_mfma_f32_16x16x32_bf16((a), (b), (c), 0, 0, 0)
typedef short v4i16_t __attribute__((ext_vector_type(4)));
DI bf16x4 trread(unsigned lds_addr) {
    return __builtin_amdgcn_ds_read_tr16_b64_v4i16((LAS v4i16_t*)(uintptr_t)lds_addr);
}
#define DPP_ADD(v, ctrl, rmask, bc) v += __builtin_bit_cast(float, __builtin_amdgcn_update_dpp(0, __builtin_bit_cast(int, v), ctrl, rmask, 0xf, bc))
DI float wave_sum(float v) {
    DPP_ADD(v, 0x111, 0xf, true); DPP_ADD(v, 0x112, 0xf, true); DPP_ADD(v, 0x114, 0xf, true); DPP_ADD(v, 0x118, 0xf, true);
    DPP_ADD(v, 0x142, 0xa, false); DPP_ADD(v, 0x143, 0xc, false);
    return __builtin_bit_cast(float, __builtin_amdgcn_readlane(__builtin_bit_cast(int, v), 63));
}
DI void wave_sum2(float& a, float& b) {
    DPP_ADD(a, 0x111, 0xf, true); DPP_ADD(b, 0x111, 0xf, true); DPP_ADD(a, 0x112, 0xf, true); DPP_ADD(b, 0x112, 0xf, true);
    DPP_ADD(a, 0x114, 0xf, true); DPP_ADD(b, 0x114, 0xf, true); DPP_ADD(a, 0x118, 0xf, true); DPP_ADD(b, 0x118, 0xf, true);
    DPP_ADD(a, 0x142, 0xa, false); DPP_ADD(b, 0x142, 0xa, false); DPP_ADD(a, 0x143, 0xc, false); DPP_ADD(b, 0x143, 0xc, false);
    a = __builtin_bit_cast(float, __builtin_amdgcn_readlane(__builtin_bit_cast(int, a), 63));
    b = __builtin_bit_cast(float, __builtin_amdgcn_readlane(__builtin_bit_cast(int, b), 63));
}
DI unsigned wave_umax(unsigned v) {
#define DPP_MAX(ctrl, rmask) v = max(v, (unsigned)__builtin_amdgcn_update_dpp(0, (int)v, ctrl, rmask, 0xf, true))
    DPP_MAX(0x111, 0xf); DPP_MAX(0x112, 0xf); DPP_MAX(0x114, 0xf); DPP_MAX(0x118, 0xf); DPP_MAX(0x142, 0xa); DPP_MAX(0x143, 0xc);
    return (unsigned)__builtin_amdgcn_readlane((int)v, 63);
}
DI float xor16(float v) { auto r = __builtin_amdgcn_permlane16_swap(__float_as_uint(v), __float_as_uint(v), false, false);
                          return __uint_as_float(r[0]) + __uint_as_float(r[1]); }
DI float xor32(float v) { auto r = __builtin_amdgcn_permlane32_swap(__float_as_uint(v), __float_as_uint(v), false, false);
                          return __uint_as_float(r[0]) + __uint_as_float(r[1]); }
DI float max32(float v) { auto r = __builtin_amdgcn_permlane32_swap(__float_as_uint(v), __float_as_uint(v), false, false);
                          return __builtin_fmaxf(__uint_as_float(r[0]), __uint_as_float(r[1])); }

constexpr int K1_XB = 0;
constexpr int K1_W = 40960;
constexpr int K1_KV = 143360;
constexpr int K1_WOT = 153664;
constexpr int K1_ST = 158784;
constexpr int K1_CS = K1_ST + 1024;
constexpr int K1_JUNK = K1_CS + 768;
constexpr int K1_END = K1_JUNK + 256;
constexpr int K1_LDS_BYTES = K1_END + DIAG_BYTES;
constexpr int K1_OQ = K1_XB;
constexpr int K1_OKV = K1_XB + 10240;

DI int swap23(int r) { return (r & ~12) | ((r & 4) << 1) | ((r & 8) >> 1); }
DI void glds5(const char* sbase, const unsigned (&v)[5], unsigned lds_base) {
    unsigned sv;
    asm volatile("s_mov_b32 %0, m0\n\ts_mov_b32 m0, %7\n\ts_nop 0\n\tglobal_load_lds_dwordx4 %1, %6\n\t"
                 "s_add_u32 m0, m0, 0x400\n\ts_nop 0\n\tglobal_load_lds_dwordx4 %2, %6\n\t"
                 "s_add_u32 m0, m0, 0x400\n\ts_nop 0\n\tglobal_load_lds_dwordx4 %3, %6\n\t"
                 "s_add_u32 m0, m0, 0x400\n\ts_nop 0\n\tglobal_load_lds_dwordx4 %4, %6\n\t"
                 "s_add_u32 m0, m0, 0x400\n\ts_nop 0\n\tglobal_load_lds_dwordx4 %5, %6\n\t"
                 "s_mov_b32 m0, %0"
                 : "=&s"(sv) : "v"(v[0]), "v"(v[1]), "v"(v[2]), "v"(v[3]), "v"(v[4]), "s"(sbase), "s"(lds_base) : "memory", "scc");
}
#define VMBAR(n) asm volatile("s_waitcnt vmcnt(" #n ") lgkmcnt(0)\n\ts_barrier" ::: "memory")

#define RAWBAR() asm volatile("s_barrier" ::: "memory")
#define K1_SYM "_Z6k1_qkvPKtS0_S0_S0_S0_S0_S0_S0_Pc"
#define K2_SYM "_Z7k2_attnPKtS0_S0_S0_S0_PcPt"
#define CODE_ADDR(dst, sym) asm volatile("s_getpc_b64 s[40:41]\n\ts_add_u32 s40, s40, " sym "@rel32@lo+4\n\ts_addc_u32 s41, s41, " sym "@rel32@hi+12\n\t" \
                                         "s_mov_b64 %0, s[40:41]" : "=s"(dst) :: "scc", "s40", "s41")
DI void k1_loader(char* smem, unsigned lds0, const ushort_t* x, const ushort_t* wq, const ushort_t* wk, const ushort_t* wv,
                  const ushort_t* wo, const ushort_t* w_gate, const ushort_t* w_up, const ushort_t* w_down,
                  char* ws, int d, int lane, int b, int panel, int j) {
#define DIAG_LDS K1_END
    const int rot = (5 * (panel & 3) + 2 * j + (b & 7)) % 20;
    unsigned xo[5], xo2[5], wo5[5];
    const char* xb = (const char*)(x + (size_t)(panel * 64 + d * 16) * DM);
#pragma unroll
    for (int k = 0; k < 5; ++k) {
        const int p = k * 64 + lane;
        const int r = p / 40, c = p - r * 40;
        const int cb = (((c >> 2) ^ ((r >> 1) & 1)) * 64) + (c & 3) * 16 + rot * 64;
        xo[k] = r * 1280 + (cb >= 1280 ? cb - 1280 : cb);
        glds16s(xb, xo[k], lds0 + K1_XB + d * 10240 + k * 1024);
        glds16s(xb + 10240, xo[k], lds0 + K1_XB + d * 10240 + 5120 + k * 1024);
        const int cb2 = cb + 640;
        xo2[k] = r * 1280 + (cb2 >= 1280 ? cb2 - 1280 : cb2);
    }
#pragma unroll
    for (int k = 0; k < 5; ++k) {
        const int p = k * 64 + lane;
        const int rw = p / 10, c8 = p - rw * 10;
        wo5[k] = swap23(rw) * 1280 + c8 * 16;
    }
    const char* wqb = (const char*)(wq + j * 80);
    const unsigned wl = lds0 + K1_W + d * 5120;
#define K1_WSLOT(s4) glds5(wqb + (size_t)((d + (s4) + rot) % 20) * 40960, wo5, wl + (s4) * 5120)
    K1_WSLOT(0);
    VMBAR(5);
    K1_WSLOT(4);
    VMBAR(5);
    glds5(xb, xo2, lds0 + K1_XB + d * 10240);
    glds5(xb + 10240, xo2, lds0 + K1_XB + d * 10240 + 5120);
    K1_WSLOT(8);
    VMBAR(15);
    K1_WSLOT(12);
    K1_WSLOT(16);
    VMBAR(10);
    {
        const int bo = min(b, 159);
        const char* wob = (const char*)(wo + (size_t)(bo >> 3) * 32 * DM + (bo & 7) * 80);
        const unsigned oa = (d == 0) ? wo5[0] : (d == 1) ? wo5[1] : (d == 2) ? wo5[2] : wo5[3];
        glds16s(wob, oa, lds0 + K1_WOT + d * 1024);
        glds16s(wob, wo5[4], lds0 + K1_WOT + 4096);
    }
    {
        const unsigned sl = (b >> 3) * 4 + d;
#pragma unroll
        for (int i = 0; i < 2; ++i) {
            const unsigned l = min(i * 64u + lane, 76u);
            const char* p;
            if (l < 50u) { const unsigned n = sl * 50 + l;
                p = (const char*)((n >= 3200u) ? w_up : w_gate) + (size_t)((n % 3200u) / 5u) * 3456 + min(((b & 7) * 432u & ~127u) + (n % 5u) * 128u, 3328u); }
            else { const unsigned n = sl * 27 + (l - 50u);
                p = (const char*)w_down + (size_t)(n >> 1) * 1280 + min(((b & 7) * 160u & ~127u) + (n & 1u) * 128u, 1152u); }
            unsigned sv;
            asm volatile("s_mov_b32 %0, m0\n\ts_mov_b32 m0, %2\n\ts_nop 0\n\tglobal_load_lds_dword %1, off\n\ts_mov_b32 m0, %0"
                         : "=&s"(sv) : "v"(p), "s"(lds0 + K1_JUNK) : "memory");
        }
        if (d == 1) {
            unsigned long long c0, c1;
            CODE_ADDR(c0, K2_SYM); CODE_ADDR(c1, "k2_code_end_mark");
            const unsigned off = min(((b >> 3) * 12u + min((unsigned)lane, 11u)) * 128u, (unsigned)(c1 - c0) & ~3u);
            unsigned sv;
            asm volatile("s_mov_b32 %0, m0\n\ts_mov_b32 m0, %3\n\ts_nop 0\n\tglobal_load_lds_dword %1, %2\n\ts_mov_b32 m0, %0"
                         : "=&s"(sv) : "v"(off), "s"(c0), "s"(lds0 + K1_JUNK) : "memory");
        }
        const u32x4 z = {0, 0, 0, 0};
        char* zp = ws + OFF_CROW + (size_t)b * 5120 + d * 1024 + lane * 16;
        asm volatile("global_store_dwordx4 %0, %1, off\n\ts_nop 1" :: "v"(zp), "v"(z) : "memory");
        if (d == 0) zp += 4096; else if (b < 9) zp = ws + OFF_KEYG + b * 1024 + lane * 16; else if (b < 11) zp = ws + OFF_REPL + (b - 9) * 1024 + lane * 16;
        asm volatile("global_store_dwordx4 %0, %1, off\n\ts_nop 1" :: "v"(zp), "v"(z) : "memory");
    }
    VMBAR(6);
    if (d == 2 && b == 0 && lane < 3) {
        const unsigned o2 = 0x3F803F80u;
        *(u32x4*)(ws + OFF_CONST + lane * 16) = (lane == 0) ? (u32x4){o2, o2, o2, o2} : (lane == 1) ? (u32x4){0, 0, 0, 0} : (u32x4){o2, 0x3F80u, 0, 0};
    }
    VMBAR(4);
    if (d == 3 && b < 160) {
        const int l15 = lane & 15, lg = lane >> 4;
        const unsigned a = lds0 + K1_WOT + (16 * (lg >> 1) + 4 * (lg & 1) + (l15 >> 2)) * 160 + (l15 & 3) * 8;
        char* dst = ws + OFF_WOF + (size_t)((b & 7) * 100 + (b >> 3) * 5) * 1024 + lane * 16;
#pragma unroll
        for (int t = 0; t < 5; ++t) {
            const bf16x4 lo = trread(a + t * 32), hi = trread(a + t * 32 + 1280);
            *(bf16x8*)(dst + t * 1024) = __builtin_shufflevector(lo, hi, 0, 1, 2, 3, 4, 5, 6, 7);
        }
    }
    if (d == 1 && j < 6) {
        const u32x4 kv = *(const u32x4*)(smem + K1_OKV + lane * 16);
        const int tile0 = panel * 2;
        if (j < 2) {
            *(u32x4*)(ws + OFF_KF1 + (size_t)((tile0 + (lane >> 5)) * 64 + 32 * j + (lane & 31)) * 16) = kv;
        } else if (j == 2) {
            if (lane < 32) *(u32x4*)(ws + OFF_KF2 + (size_t)tile0 * 256 + lane * 16) = kv;
        } else {
            const int dd = (j == 5) ? 12 + (lane & 7) : 8 * (j - 3) + (lane & 7);
            if (j < 5 || (lane & 7) >= 4)
                *(u32x4*)(ws + OFF_VF + (size_t)((tile0 * 2 + (lane >> 4)) * 64 + 32 * ((lane >> 3) & 1) + dd) * 16) = kv;
        }
    }
    asm volatile("s_waitcnt vmcnt(0)\n\t.globl k1_code_end_mark\n\t.protected k1_code_end_mark\nk1_code_end_mark:" ::: "memory");
#undef DIAG_LDS
}

template <int NH, int LOW>
DI void k1_body(char* smem, unsigned lds0, char* ws, int u, int lane, int j) {
#define DIAG_LDS K1_END
    const int l15 = lane & 15, lg = lane >> 4;
    const int m = u >> 1, rho = (m ^ NH) & 1;
    bf16x8 xf[10];
    const char* xr0 = smem + K1_XB + (m * 16 + l15) * 640 + lg * 16;
    const int e = rho ^ ((l15 >> 1) & 1);
    const char* xe = xr0 + e * 64;
    const char* xod = xr0 - e * 64;
    float s1 = 0.f, s2 = 0.f;
    const unsigned ones2 = 0x3F803F80u;
    f32x4 acc[3], cs[3];
#pragma unroll
    for (int t = 0; t < 3; ++t) { acc[t] = (f32x4){0, 0, 0, 0}; cs[t] = (f32x4){0, 0, 0, 0}; }
    bf16x8 onesf;
#pragma unroll
    for (int i = 0; i < 8; ++i) onesf[i] = (short)0x3F80;
    asm volatile("" : "+v"(onesf));
    const int rrow = 16 * (lg >> 1) + 4 * (lg & 1) + (l15 >> 2);
    const unsigned aW = lds0 + K1_W + rrow * 160 + (l15 & 3) * 8 + NH * 96;
    const unsigned aW4[4] = {aW + rho * 5120, aW - rho * 5120, aW + rho * 5120 + 51200, aW - rho * 5120 + 51200};
    const unsigned aK = lds0 + K1_KV + (8 * lg + (l15 >> 2)) * 16 + (l15 & 3) * 8;
    const unsigned aKe = aK + rho * 512, aKo = aK - rho * 512;
    bf16x8 wf[4][3];
#define K1_STATS() do { _Pragma("unroll") for (int i_ = 0; i_ < 10; i_ += 2) { const u32x4 v_ = __builtin_bit_cast(u32x4, xf[i_]); \
        _Pragma("unroll") for (int q_ = 0; q_ < 4; ++q_) { s1 = dot2acc(v_[q_], ones2, s1); s2 = dot2acc(v_[q_], v_[q_], s2); } } SBAR(); } while (0)
#define K1_READ(i) do { \
        const unsigned ab_ = aW4[((i) >= 10 ? 2 : 0) + ((i) & 1)] + ((i) % 10) * 5120; \
        _Pragma("unroll") for (int t_ = 0; t_ < (NH ? 2 : 3); ++t_) { \
            const bf16x4 lo_ = trread(ab_ + t_ * 32), hi_ = trread(ab_ + t_ * 32 + 1280); \
            wf[(i) & 3][t_] = __builtin_shufflevector(lo_, hi_, 0, 1, 2, 3, 4, 5, 6, 7); } \
        if (NH) { const unsigned ak_ = (((i) & 1) ? aKo : aKe) + (i) * 512; \
            const bf16x4 lo_ = trread(ak_), hi_ = trread(ak_ + 64); \
            wf[(i) & 3][2] = __builtin_shufflevector(lo_, hi_, 0, 1, 2, 3, 4, 5, 6, 7); } \
    } while (0)
#define K1_MMA(i, XF) do { \
        _Pragma("unroll") for (int t_ = 0; t_ < 3; ++t_) { \
            acc[t_] = MFMA16(wf[(i) & 3][t_], XF[(i) % 10], acc[t_]); \
            if ((i) >= 12 || (LOW && ((i) & 1) == 0)) cs[t_] = MFMA16(wf[(i) & 3][t_], onesf, cs[t_]); } \
    } while (0)
#define K1_CHUNK(i0, XA, XB) do { K1_READ(i0); K1_READ((i0) + 1); K1_READ((i0) + 2); K1_READ((i0) + 3); SBAR(); \
        K1_MMA(i0, XA); K1_MMA((i0) + 1, XA); K1_MMA((i0) + 2, XB); K1_MMA((i0) + 3, XB); SBAR(); } while (0)
    RAWBAR();
    STAMP(0, 11);
#pragma unroll
    for (int i = 0; i < 10; ++i) xf[i] = *(const bf16x8*)(((i & 1) ? xod : xe) + i * 64);
    VMBAR(0);
    K1_CHUNK(0, xf, xf);
    K1_STATS();
    STAMP(0, 1);
    RAWBAR();
    K1_CHUNK(4, xf, xf);
    RAWBAR();
    bf16x8 xg[10];
#pragma unroll
    for (int i = 0; i < 10; ++i) xg[i] = *(const bf16x8*)(((i & 1) ? xod : xe) + i * 64);
    K1_CHUNK(8, xf, xg);
#pragma unroll
    for (int i = 0; i < 10; ++i) xf[i] = xg[i];
    K1_STATS();
    s1 = xor32(xor16(s1));
    s2 = xor32(xor16(s2));
    if (lg == 0) *(f32x2*)(smem + K1_ST + (NH * 64 + m * 16 + l15) * 8) = (f32x2){s1, s2};
    if (LOW && l15 == 0) {
#pragma unroll
        for (int t = 0; t < 3; ++t) *(f32x4*)(smem + K1_CS + (m * 96 + NH * 48 + t * 16 + lg * 4) * 4) = cs[t];
    }
    STAMP(0, 2);
    BAR_LDS();
    const int tl = m * 16 + l15;
    K1_READ(12); K1_READ(13);
    const f32x2 sa = *(const f32x2*)(smem + K1_ST + tl * 8);
    const f32x2 sb = *(const f32x2*)(smem + K1_ST + (64 + tl) * 8);
#pragma unroll
    for (int t = 0; t < 3; ++t)
        cs[t] = *(const f32x4*)(smem + K1_CS + (NH * 48 + t * 16 + lg * 4) * 4) +
                *(const f32x4*)(smem + K1_CS + (96 + NH * 48 + t * 16 + lg * 4) * 4);
    SBAR();
#pragma unroll
    for (int i = 12; i < 20; ++i) { if (i + 2 < 20) K1_READ(i + 2); SBAR(); K1_MMA(i, xf); SBAR(); }
    const float mean = (sa[0] + sb[0]) * (1.0f / DM);
    const float var = fmaxf((sa[1] + sb[1]) * (1.0f / DM) - mean * mean, 0.f);
    const float mb = rbf(mean);
    const float sig = rbf(__builtin_amdgcn_sqrtf(rbf(rbf(var) + 1e-5f)));
    const float rs = __builtin_amdgcn_rcpf(sig);
    STAMP(0, 4);
    const float QSCALE = 0.22360679774997896f * 1.4426950408889634f;
    const int tok = tl & 7;
#pragma unroll
    for (int t = 0; t < 3; ++t) {
        const f32x4 csum = cs[t];
        float v[4];
#pragma unroll
        for (int r = 0; r < 4; ++r) v[r] = (acc[t][r] - mb * csum[r]) * rs;
        if (t == 2 && NH == 1) {
            if (lg < 2) {
                const int kk = tl & 31;
                if (j < 3) {
                    u32x2 pk; pk[0] = cvtpk(v[0], v[1]); pk[1] = cvtpk(v[2], v[3]);
                    if (j < 2) *(u32x2*)(smem + K1_OKV + tl * 16 + lg * 8) = pk;
                    else if (lg == 1) *(u32x2*)(smem + K1_OKV + tl * 8) = pk;
                } else {
                    const int s = kk >> 4, h = (kk >> 2) & 1, jj = ((kk >> 3) & 1) * 4 + (kk & 3);
                    char* d = smem + K1_OKV + (((((tl >> 5) * 2 + s) * 2 + h) * 8 + lg * 4) * 16) + jj * 2;
#pragma unroll
                    for (int r = 0; r < 4; ++r) *(ushort_t*)(d + r * 16) = f2bf(v[r]);
                }
            }
        } else {
            const int nl = (NH * 3 + t) * 16 + lg * 4;
            const int hl = nl / 20, dq = (nl - hl * 20) >> 2;
            u32x2 pk;
            pk[0] = cvtpk(v[0] * QSCALE, v[1] * QSCALE);
            pk[1] = cvtpk(v[2] * QSCALE, v[3] * QSCALE);
            const int off = (dq < 4) ? ((hl * 8 + tok + 32 * (dq >> 1)) * 16 + (dq & 1) * 8) : (1024 + (hl * 8 + tok) * 8);
            *(u32x2*)(smem + K1_OQ + (tl >> 3) * 1280 + off) = pk;
        }
    }
    BAR_LDS();
    STAMP(0, 5);
#undef DIAG_LDS
}

__global__ __launch_bounds__(768, 3) void k1_qkv(const ushort_t* __restrict__ x, const ushort_t* __restrict__ wq,
                                                 const ushort_t* __restrict__ wk, const ushort_t* __restrict__ wv,
                                                 const ushort_t* __restrict__ wo, const ushort_t* __restrict__ w_gate,
                                                 const ushort_t* __restrict__ w_up, const ushort_t* __restrict__ w_down,
                                                 char* __restrict__ ws) {
    extern __shared__ __attribute__((aligned(16))) char smem[];
    const unsigned lds0 = (unsigned)(uintptr_t)smem;
    const int lane = threadIdx.x & 63;
    const int u = __builtin_amdgcn_readfirstlane(threadIdx.x >> 6);
    if (u < 3) {
        unsigned long long c0, c1;
        CODE_ADDR(c0, K1_SYM); CODE_ADDR(c1, "k1_code_end_mark");
        const unsigned off = min(threadIdx.x * 128u, (unsigned)(c1 - c0) & ~3u);
        unsigned sv;
        asm volatile("s_mov_b32 %0, m0\n\ts_mov_b32 m0, %3\n\ts_nop 0\n\tglobal_load_lds_dword %1, %2\n\ts_mov_b32 m0, %0"
                     : "=&s"(sv) : "v"(off), "s"(c0), "s"(lds0 + K1_JUNK) : "memory");
    }
    const int b = blockIdx.x;
    const int panel = (b & 7) * 4 + ((b >> 3) >> 3), j = (b >> 3) & 7;
    if (u >= 8) { k1_loader(smem, lds0, x, wq, wk, wv, wo, w_gate, w_up, w_down, ws, u - 8, lane, b, panel, j); return; }
#define DIAG_LDS K1_END
    STAMP_INIT();
    STAMP(0, 0); RSTAMP(30);
    {
        const int rot = (5 * (panel & 3) + 2 * j + (b & 7)) % 20;
        const int jq = (j >= 3) ? ((j >= 6) ? 0 : j - 3) : j;
        const char* kvb = (const char*)((j >= 3 && j < 6) ? wv : wk) + (jq == 2 ? 24 : jq * 16);
        int row = u * 64 + lane + rot * 32;
        glds16s(kvb, (unsigned)((row >= 640 ? row - 640 : row) * 40), lds0 + K1_KV + u * 1024);
        if (u < 2) {
            row += 512;
            glds16s(kvb, (unsigned)((row >= 640 ? row - 640 : row) * 40), lds0 + K1_KV + (8 + u) * 1024);
        }
    }
    if (u < 4) {
        if (u & 1) k1_body<1, 1>(smem, lds0, ws, u, lane, j);
        else k1_body<0, 1>(smem, lds0, ws, u, lane, j);
    } else {
        if (u & 1) k1_body<1, 0>(smem, lds0, ws, u, lane, j);
        else k1_body<0, 0>(smem, lds0, ws, u, lane, j);
    }
    {
        const int wg2 = panel * 8 + u;
        const u32x4 v = *(const u32x4*)(smem + K1_OQ + u * 1280 + lane * 16);
        *(u32x4*)(ws + OFF_QF1 + (size_t)(wg2 * 8 + j) * 1024 + lane * 16) = v;
        if (lane < 16) {
            const u32x4 v2 = *(const u32x4*)(smem + K1_OQ + u * 1280 + 1024 + lane * 16);
            *(u32x4*)(ws + OFF_QF2 + (size_t)(wg2 * 8 + j) * 256 + lane * 16) = v2;
        }
    }
    STAMP(0, 6); RSTAMP(31);
    STAMP_FLUSH(0);
#undef DIAG_LDS
}

constexpr int K2_PBUF = 0;
constexpr int K2_QL = 49152;
constexpr int K2_OT = 61440;
constexpr int K2_MISC = 71680;
constexpr int K2_TAIL = K2_MISC + 1152;
constexpr int K2_GU = K2_TAIL;
constexpr int K2_ACT = K2_TAIL + 57344;
constexpr int K2_STG = K2_PBUF;
constexpr int K2_JUNK = K2_TAIL + 81920;
constexpr int K2_END = K2_JUNK + 1024;
constexpr int K2_LDS_BYTES = K2_END + DIAG_BYTES;

DI void split3(float v, ushort_t& a, ushort_t& b, ushort_t& c) {
    a = f2bf(v); const float r1 = v - bf2f(a);
    b = f2bf(r1); const float r2 = r1 - bf2f(b);
    c = f2bf(r2);
}
#define MX3(a, b, c) __builtin_fmaxf(__builtin_fmaxf((a), (b)), (c))
DI float max16(const f32x16& s) {
    float a = MX3(s[0], s[1], s[2]), b = MX3(s[3], s[4], s[5]);
    a = MX3(a, s[6], s[7]); b = MX3(b, s[8], s[9]);
    a = MX3(a, s[10], s[11]); b = MX3(b, s[12], s[13]);
    a = MX3(a, s[14], s[15]);
    return __builtin_fmaxf(a, b);
}

__global__ __launch_bounds__(512, 2) void k2_attn(const ushort_t* __restrict__ x, const ushort_t* __restrict__ w_r,
                                                   const ushort_t* __restrict__ w_gate, const ushort_t* __restrict__ w_up,
                                                   const ushort_t* __restrict__ w_down,
                                                   char* __restrict__ ws, ushort_t* __restrict__ out) {
    extern __shared__ __attribute__((aligned(16))) char smem[];
    const unsigned lds0 = (unsigned)(uintptr_t)smem;
    const int w_0 = __builtin_amdgcn_readfirstlane(threadIdx.x >> 6);
    const int wg_0 = (blockIdx.x & 7) * 32 + (blockIdx.x >> 3);
    const unsigned lane16 = (threadIdx.x & 63) * 16;
#define FRESH_IDS int w = w_0, wg = wg_0; asm volatile("" : "+s"(w), "+s"(wg));
#define DIAG_LDS K2_END
    STAMP_INIT();
    STAMP(1, 0); RSTAMP(30);
    bf16x8 q1w = *(const bf16x8*)(ws + OFF_QF1 + (size_t)(wg_0 * 8 + w_0) * 1024 + lane16);
    bf16x4 q2w = *(const bf16x4*)(ws + (lane16 < 512u ? (unsigned)OFF_QF2 + (unsigned)(wg_0 * 8 + w_0) * 256u + (lane16 >> 1) : (unsigned)OFF_CONST + 16u));
    bf16x8 k1w = *(const bf16x8*)(ws + OFF_KF1 + (size_t)w_0 * 8192 + lane16);
    bf16x4 k2w = *(const bf16x4*)(ws + (lane16 < 512u ? (unsigned)OFF_KF2 + (unsigned)w_0 * 2048u + (lane16 >> 1) : (unsigned)OFF_CONST + 32u));
    SBAR();
    const __amdgpu_buffer_rsrc_t wrs = __builtin_amdgcn_make_buffer_rsrc((void*)(ws + OFF_WOF + (size_t)w_0 * 102400), 0, 102400 + 5120, 0x00020000);
    const __amdgpu_buffer_rsrc_t wsr = __builtin_amdgcn_make_buffer_rsrc((void*)ws, 0, 8 << 20, 0x00020000);
    unsigned lo4[4] = {lane16, lane16 + 1024, lane16 + 2048, lane16 + 3072};
    asm volatile("" : "+v"(lo4[1]), "+v"(lo4[2]), "+v"(lo4[3]));
#define WOLOAD(off) __builtin_bit_cast(bf16x8, __builtin_amdgcn_raw_buffer_load_b128(wrs, lo4[((off) >> 10) & 3], (off) & ~4095, 0))
#define OPQ16() ({ unsigned l_ = lane16; asm volatile("" : "+v"(l_)); l_; })

    const int bi = blockIdx.x;
    const int gcb = (bi & 7) * 27 + min(bi >> 3, 26);
    const int dcb = (bi & 7) * 5 + max((bi >> 3) - 27, 0);
    u32x4 xa, wa4; unsigned xb1, wb1;
    f32x4 oacc[5];
    const f32x16 zero16 = {};

    for (int attempt = 0; attempt < 2; ++attempt) {
        FRESH_IDS
        bf16x8 kf1[8]; bf16x4 kf2[8]; bf16x8 vf[8][2];
        unsigned k2o, k2s, vo, vs;
        STAMP(1, 14);
        {
            const unsigned l16 = OPQ16();
            const unsigned lane = l16 >> 4, r32 = lane & 31, hi = lane >> 5;
            k2o = hi == 0 ? (unsigned)OFF_KF2 + (unsigned)(w * 256 + r32) * 8u : (unsigned)OFF_CONST + 32u;
            k2s = hi == 0 ? 256u : 0u;
            vo = r32 < DH ? (unsigned)OFF_VF + (unsigned)(w * 1024) * 16u + l16 : (unsigned)OFF_CONST + (r32 == DH ? 0u : 16u);
            vs = r32 < DH ? 1024u : 0u;
#define KLOAD(t) do { kf1[t] = __builtin_bit_cast(bf16x8, __builtin_amdgcn_raw_buffer_load_b128(wsr, lane16, (int)OFF_KF1 + w * 8192 + (t) * 1024, 0)); \
            kf2[t] = __builtin_bit_cast(bf16x4, __builtin_amdgcn_raw_buffer_load_b64(wsr, k2o + (t) * k2s, 0, 0)); } while (0)
#define VLOAD(t) do { vf[t][0] = __builtin_bit_cast(bf16x8, __builtin_amdgcn_raw_buffer_load_b128(wsr, vo + (2 * (t)) * vs, 0, 0)); \
            vf[t][1] = __builtin_bit_cast(bf16x8, __builtin_amdgcn_raw_buffer_load_b128(wsr, vo + (2 * (t) + 1) * vs, 0, 0)); } while (0)
            KLOAD(0); SBAR(); KLOAD(1); SBAR(); VLOAD(0); SBAR(); KLOAD(2); SBAR(); VLOAD(1); SBAR();
            if (attempt == 0) {
                f32x16 s = MFMA32(k1w, q1w, zero16);
                s = MFMA32K8(k2w, q2w, s);
                const float m = max32(max16(s));
                if (hi == 1) { ushort_t a, b2, c; split3(-m, a, b2, c); q2w = (bf16x4){(short)a, (short)b2, (short)c, 0}; }
                *(bf16x8*)(smem + K2_QL + w * 1536 + l16) = q1w;
                *(bf16x4*)(smem + K2_QL + w * 1536 + 1024 + (l16 >> 1)) = q2w;
            }
            if (w == 0) *(unsigned*)(smem + K2_MISC) = 0u;
        }
#pragma unroll
        for (int t = 0; t < 5; ++t) oacc[t] = (f32x4){0, 0, 0, 0};
        BAR_LDS();
        STAMP(1, 1); RSTAMP(28);

        unsigned long long badm = 0ull;
        f32x16 sc, o;
        u32x4 p0, p1;
        bf16x8 wa[5];
        bf16x8 q1 = *(const bf16x8*)(smem + K2_QL + OPQ16());
        bf16x4 q2 = *(const bf16x4*)(smem + K2_QL + 1024 + (OPQ16() >> 1));
        const char* otp; { const unsigned l_ = OPQ16(); otp = smem + K2_OT + ((l_ >> 1) & 0x180) + (l_ & 0x70); }
        char* pbv = smem + K2_PBUF + w * 3072 + OPQ16();
        const char* q2v = smem + K2_QL + 1024 + (OPQ16() >> 1);
        const char *s2pa, *s2pl; char* s2dst;
        {
            const unsigned l_ = OPQ16() >> 4;
            const unsigned it_ = w * 20 + min(l_ >> 1, 19u);
            s2pa = smem + K2_PBUF + (l_ & 1) * 12288 + it_ * 16;
            s2pl = smem + K2_PBUF + (l_ & 1) * 12288 + (160 + (it_ & 31)) * 16;
            const unsigned kr_ = 20 * ((it_ & 31) >> 3) + 4 * (it_ >> 5);
            s2dst = smem + K2_OT + ((kr_ >> 3) * 8 + (it_ & 7)) * 16 + (kr_ & 7) * 2;
        }
#define PHASE_LOCALS bf16x8 q1n; bf16x4 q2n; bf16x8 ob; f32x4 sa0, sa1, sa2, sa3; float sl0, sl1, sl2, sl3;
        sc = MFMA32(kf1[0], q1, zero16);
        sc = MFMA32K8(kf2[0], q2, sc);
#define PRIO(n) __builtin_amdgcn_s_setprio(n)
#define LSTAMP(a, b) STAMP(a, b)

#define EXP4(S, i) do { S[i] = __builtin_amdgcn_exp2f(S[i]); S[i + 1] = __builtin_amdgcn_exp2f(S[i + 1]); \
                        S[i + 2] = __builtin_amdgcn_exp2f(S[i + 2]); S[i + 3] = __builtin_amdgcn_exp2f(S[i + 3]); } while (0)
#define PACK4(P, S, i) do { P[0] = cvtpk(S[i], S[i + 1]); P[1] = cvtpk(S[i + 2], S[i + 3]); \
                            P[2] = cvtpk(S[i + 4], S[i + 5]); P[3] = cvtpk(S[i + 6], S[i + 7]); } while (0)
#define BF8(P) __builtin_bit_cast(bf16x8, P)
#define WP_ADDR(PAR) char* pb_ = pbv + (PAR) * 24576
#define WP_ST(i) *(f32x4*)(pb_ + (i) * 1024) = (f32x4){o[4 * (i)], o[4 * (i) + 1], o[4 * (i) + 2], o[4 * (i) + 3]}
#define WRITE_PARTIAL(PAR) do { WP_ADDR(PAR); asm volatile("" : "+v"(o)); WP_ST(0); WP_ST(1); WP_ST(2); } while (0)
#define STEP2A(PAR) do { const char* a_ = s2pa + (PAR) * 24576; const char* l_ = s2pl + (PAR) * 24576; \
            sa0 = *(const f32x4*)(a_); sa1 = *(const f32x4*)(a_ + 3072); sa2 = *(const f32x4*)(a_ + 6144); sa3 = *(const f32x4*)(a_ + 9216); \
            sl0 = *(const float*)(l_); sl1 = *(const float*)(l_ + 3072); sl2 = *(const float*)(l_ + 6144); sl3 = *(const float*)(l_ + 9216); \
        } while (0)
#define STEP2B() do { \
            sa0 = (sa0 + sa1) + (sa2 + sa3); sl0 += sl1; asm volatile("" : "+v"(sl0)); sl2 += sl3; asm volatile("" : "+v"(sl2)); sl0 += sl2; \
            DPP_ADD(sl0, 0xB1, 0xf, true); \
            badm |= __ballot(!(sl0 < 1.8446744e19f)); \
            sl0 = __builtin_amdgcn_rcpf(sl0); \
            asm volatile("" : "+v"(sa0), "+v"(sl0)); \
        } while (0)
#define STEP2C(G) do { \
            float a0_ = sa0[0], a1_ = sa0[1], a2_ = sa0[2], a3_ = sa0[3]; \
            DPP_ADD(a0_, 0xB1, 0xf, true); DPP_ADD(a1_, 0xB1, 0xf, true); DPP_ADD(a2_, 0xB1, 0xf, true); DPP_ADD(a3_, 0xB1, 0xf, true); \
            u32x2 pk_; pk_[0] = cvtpk(a0_ * sl0, a1_ * sl0); pk_[1] = cvtpk(a2_ * sl0, a3_ * sl0); \
            *(u32x2*)(s2dst + (G) * 1280) = pk_; \
        } while (0)
#define TAILDMA(i) glds16s(ws + OFF_WOF + (size_t)w * 102400 + 92160 + (i) * 1024, lane16, lds0 + K2_TAIL + w * 10240 + (i) * 1024)
#define WARMDMA(i) glds16s(ws + OFF_WOF + (size_t)((blockIdx.x >> 3) * 25 + min(w + 8 * (i), 24)) * 1024, lane16, lds0 + K2_JUNK)
#define WARMXW() do { const unsigned l_ = OPQ16() >> 4; \
            const char* p_ = (l_ & 32) ? (const char*)w_r + min(l_ & 31u, 9u) * 128 : (const char*)(x + (size_t)(wg * 8 + w) * DM) + min(l_, 9u) * 128; \
            unsigned sv_; asm volatile("s_mov_b32 %0, m0\n\ts_mov_b32 m0, %2\n\ts_nop 0\n\tglobal_load_lds_dword %1, off\n\ts_mov_b32 m0, %0" \
                                       : "=&s"(sv_) : "v"(p_), "s"(lds0 + K2_JUNK) : "memory"); } while (0)
#define XWLOAD() do { const unsigned l_ = OPQ16(); const char* xrow_ = (const char*)(x + (size_t)(wg * 8 + w) * DM); \
            xa = *(const u32x4*)(xrow_ + l_); xb1 = *(const unsigned*)(xrow_ + 1024 + (l_ >> 2)); \
            wa4 = *(const u32x4*)((const char*)w_r + l_); wb1 = *(const unsigned*)((const char*)w_r + 1024 + (l_ >> 2)); } while (0)
#define OPJ(...) __VA_ARGS__
#define LOOPBAR() BAR_LDS()
#define S2(...) __VA_ARGS__

        {
            const int gn = 1;
            PHASE_LOCALS
            FRESH_IDS
            PRIO(1); EXP4(sc, 0); SBAR(); PRIO(2);
            EXP4(sc, 4); PACK4(p0, sc, 0); SBAR(); EXP4(sc, 8); EXP4(sc, 12); PACK4(p1, sc, 8); SBAR(); PRIO(3);
            sc = MFMA32(kf1[1], q1, zero16); SBAR();
            sc = MFMA32K8(kf2[1], q2, sc); SBAR();
            KLOAD(3);
            SBAR();
            o = MFMA32(vf[0][0], BF8(p0), zero16); SBAR();
            VLOAD(2);
            SBAR();
            o = MFMA32(vf[0][1], BF8(p1), o); SBAR();
            PRIO(1); EXP4(sc, 0); SBAR(); PRIO(2);
            EXP4(sc, 4); PACK4(p0, sc, 0); SBAR(); EXP4(sc, 8); EXP4(sc, 12); PACK4(p1, sc, 8); SBAR(); PRIO(3);
            sc = MFMA32(kf1[2], q1, zero16); SBAR();
            sc = MFMA32K8(kf2[2], q2, sc); SBAR();
            KLOAD(4);
            SBAR();
            o = MFMA32(vf[1][0], BF8(p0), o); SBAR();
            VLOAD(3);
            SBAR();
            o = MFMA32(vf[1][1], BF8(p1), o); SBAR();
            PRIO(1); EXP4(sc, 0); SBAR(); PRIO(2);
            EXP4(sc, 4); PACK4(p0, sc, 0); SBAR(); EXP4(sc, 8); EXP4(sc, 12); PACK4(p1, sc, 8); SBAR(); PRIO(3);
            sc = MFMA32(kf1[3], q1, zero16); SBAR();
            sc = MFMA32K8(kf2[3], q2, sc); SBAR();
            KLOAD(5);
            SBAR();
            o = MFMA32(vf[2][0], BF8(p0), o); SBAR();
            VLOAD(4);
            SBAR();
            o = MFMA32(vf[2][1], BF8(p1), o); SBAR();
            PRIO(1); EXP4(sc, 0); SBAR(); PRIO(2);
            EXP4(sc, 4); PACK4(p0, sc, 0); SBAR(); EXP4(sc, 8); EXP4(sc, 12); PACK4(p1, sc, 8); SBAR(); PRIO(3);
            sc = MFMA32(kf1[4], q1, zero16); SBAR();
            sc = MFMA32K8(kf2[4], q2, sc); SBAR();
            KLOAD(6);
            SBAR();
            o = MFMA32(vf[3][0], BF8(p0), o); SBAR();
            VLOAD(5);
            SBAR();
            o = MFMA32(vf[3][1], BF8(p1), o); SBAR();
            PRIO(1); EXP4(sc, 0); SBAR(); PRIO(2);
            EXP4(sc, 4); PACK4(p0, sc, 0); SBAR(); EXP4(sc, 8); EXP4(sc, 12); PACK4(p1, sc, 8); SBAR(); PRIO(3);
            sc = MFMA32(kf1[5], q1, zero16); SBAR();
            sc = MFMA32K8(kf2[5], q2, sc); SBAR();
            KLOAD(7);
            SBAR();
            o = MFMA32(vf[4][0], BF8(p0), o); SBAR();
            VLOAD(6);
            SBAR();
            o = MFMA32(vf[4][1], BF8(p1), o); SBAR();
            PRIO(1); EXP4(sc, 0); SBAR(); PRIO(2);
            EXP4(sc, 4); PACK4(p0, sc, 0); SBAR(); EXP4(sc, 8); EXP4(sc, 12); PACK4(p1, sc, 8); SBAR(); PRIO(3);
            sc = MFMA32(kf1[6], q1, zero16); SBAR();
            sc = MFMA32K8(kf2[6], q2, sc); SBAR();
            o = MFMA32(vf[5][0], BF8(p0), o); SBAR();
            VLOAD(7);
            SBAR();
            o = MFMA32(vf[5][1], BF8(p1), o); SBAR();
            PRIO(1); EXP4(sc, 0); SBAR(); PRIO(2);
            EXP4(sc, 4); PACK4(p0, sc, 0); SBAR(); EXP4(sc, 8); EXP4(sc, 12); PACK4(p1, sc, 8); SBAR(); PRIO(3);
            sc = MFMA32(kf1[7], q1, zero16); SBAR();
            sc = MFMA32K8(kf2[7], q2, sc); SBAR();
            o = MFMA32(vf[6][0], BF8(p0), o); SBAR();
            q1n = *(const bf16x8*)(smem + K2_QL + gn * 1536 + lane16); q2n = *(const bf16x4*)(q2v + gn * 1536);
            SBAR();
            o = MFMA32(vf[6][1], BF8(p1), o); SBAR();
            PRIO(1); EXP4(sc, 0); SBAR(); PRIO(2);
            EXP4(sc, 4); PACK4(p0, sc, 0); SBAR(); EXP4(sc, 8); EXP4(sc, 12); PACK4(p1, sc, 8); SBAR(); PRIO(3);
            sc = MFMA32(kf1[0], q1n, zero16); SBAR();
            sc = MFMA32K8(kf2[0], q2n, sc); SBAR();
            o = MFMA32(vf[7][0], BF8(p0), o); SBAR();
            o = MFMA32(vf[7][1], BF8(p1), o); SBAR();
            q1 = q1n; q2 = q2n;
            LSTAMP(1, 2);
        }
        {
            const int gn = 2;
            PHASE_LOCALS
            FRESH_IDS
            PRIO(1); EXP4(sc, 0); SBAR(); PRIO(2); WP_ADDR(0); WP_ST(0); SBAR();
            EXP4(sc, 4); SBAR(); WP_ST(1); SBAR(); PACK4(p0, sc, 0); EXP4(sc, 8); SBAR(); WP_ST(2); SBAR(); EXP4(sc, 12); PACK4(p1, sc, 8); SBAR(); PRIO(3);
            asm volatile("" : "+v"(p0), "+v"(p1)); if (w >= 4) LOOPBAR();
            sc = MFMA32(kf1[1], q1, zero16); SBAR();
            WARMXW();
            SBAR();
            sc = MFMA32K8(kf2[1], q2, sc); SBAR();
            OPJ(WARMDMA(0);)
            SBAR();
            o = MFMA32(vf[0][0], BF8(p0), zero16); SBAR();
            OPJ(WARMDMA(1);)
            SBAR();
            o = MFMA32(vf[0][1], BF8(p1), o); SBAR();
            asm volatile("" : "+v"(o), "+v"(sc)); if (w < 4) LOOPBAR();
            PRIO(1); EXP4(sc, 0); SBAR(); PRIO(2);
            EXP4(sc, 4); PACK4(p0, sc, 0); SBAR(); EXP4(sc, 8); EXP4(sc, 12); PACK4(p1, sc, 8); SBAR(); PRIO(3);
            sc = MFMA32(kf1[2], q1, zero16); SBAR();
            S2(STEP2A(0);)
            SBAR();
            sc = MFMA32K8(kf2[2], q2, sc); SBAR();
            OPJ(WARMDMA(2);)
            OPJ(WARMDMA(3);)
            SBAR();
            o = MFMA32(vf[1][0], BF8(p0), o); SBAR();
            OPJ(TAILDMA(0);)
            SBAR();
            o = MFMA32(vf[1][1], BF8(p1), o); SBAR();
            PRIO(1); EXP4(sc, 0); SBAR(); PRIO(2);
            EXP4(sc, 4); PACK4(p0, sc, 0); SBAR(); EXP4(sc, 8); EXP4(sc, 12); PACK4(p1, sc, 8); SBAR(); PRIO(3);
            sc = MFMA32(kf1[3], q1, zero16); SBAR();
            S2(STEP2B();)
            SBAR();
            sc = MFMA32K8(kf2[3], q2, sc); SBAR();
            OPJ(TAILDMA(1);)
            SBAR();
            o = MFMA32(vf[2][0], BF8(p0), o); SBAR();
            OPJ(TAILDMA(2);)
            SBAR();
            o = MFMA32(vf[2][1], BF8(p1), o); SBAR();
            PRIO(1); EXP4(sc, 0); SBAR(); PRIO(2);
            EXP4(sc, 4); PACK4(p0, sc, 0); SBAR(); EXP4(sc, 8); EXP4(sc, 12); PACK4(p1, sc, 8); SBAR(); PRIO(3);
            sc = MFMA32(kf1[4], q1, zero16); SBAR();
            S2(STEP2C(gn - 2);)
            SBAR();
            sc = MFMA32K8(kf2[4], q2, sc); SBAR();
            OPJ(TAILDMA(3);)
            SBAR();
            o = MFMA32(vf[3][0], BF8(p0), o); SBAR();
            OPJ(TAILDMA(4);)
            SBAR();
            o = MFMA32(vf[3][1], BF8(p1), o); SBAR();
            PRIO(1); EXP4(sc, 0); SBAR(); PRIO(2);
            EXP4(sc, 4); PACK4(p0, sc, 0); SBAR(); EXP4(sc, 8); EXP4(sc, 12); PACK4(p1, sc, 8); SBAR(); PRIO(3);
            sc = MFMA32(kf1[5], q1, zero16); SBAR();
            OPJ(TAILDMA(5);)
            SBAR();
            sc = MFMA32K8(kf2[5], q2, sc); SBAR();
            OPJ(TAILDMA(6);)
            SBAR();
            o = MFMA32(vf[4][0], BF8(p0), o); SBAR();
            OPJ(TAILDMA(7);)
            SBAR();
            o = MFMA32(vf[4][1], BF8(p1), o); SBAR();
            PRIO(1); EXP4(sc, 0); SBAR(); PRIO(2);
            EXP4(sc, 4); PACK4(p0, sc, 0); SBAR(); EXP4(sc, 8); EXP4(sc, 12); PACK4(p1, sc, 8); SBAR(); PRIO(3);
            sc = MFMA32(kf1[6], q1, zero16); SBAR();
            OPJ(TAILDMA(8);)
            SBAR();
            sc = MFMA32K8(kf2[6], q2, sc); SBAR();
            OPJ(TAILDMA(9);)
            SBAR();
            o = MFMA32(vf[5][0], BF8(p0), o); SBAR();
            OPJ(wa[0] = WOLOAD(0);)
            SBAR();
            o = MFMA32(vf[5][1], BF8(p1), o); SBAR();
            PRIO(1); EXP4(sc, 0); SBAR(); PRIO(2);
            EXP4(sc, 4); PACK4(p0, sc, 0); SBAR(); EXP4(sc, 8); EXP4(sc, 12); PACK4(p1, sc, 8); SBAR(); PRIO(3);
            sc = MFMA32(kf1[7], q1, zero16); SBAR();
            OPJ(wa[1] = WOLOAD(1024);)
            SBAR();
            sc = MFMA32K8(kf2[7], q2, sc); SBAR();
            OPJ(wa[2] = WOLOAD(2048);)
            SBAR();
            o = MFMA32(vf[6][0], BF8(p0), o); SBAR();
            q1n = *(const bf16x8*)(smem + K2_QL + gn * 1536 + lane16); q2n = *(const bf16x4*)(q2v + gn * 1536);
            SBAR();
            o = MFMA32(vf[6][1], BF8(p1), o); SBAR();
            PRIO(1); EXP4(sc, 0); SBAR(); PRIO(2);
            EXP4(sc, 4); PACK4(p0, sc, 0); SBAR(); EXP4(sc, 8); EXP4(sc, 12); PACK4(p1, sc, 8); SBAR(); PRIO(3);
            sc = MFMA32(kf1[0], q1n, zero16); SBAR();
            OPJ(wa[3] = WOLOAD(3072);)
            SBAR();
            sc = MFMA32K8(kf2[0], q2n, sc); SBAR();
            OPJ(wa[4] = WOLOAD(4096);)
            SBAR();
            o = MFMA32(vf[7][0], BF8(p0), o); SBAR();
            o = MFMA32(vf[7][1], BF8(p1), o); SBAR();
            q1 = q1n; q2 = q2n;
            LSTAMP(1, 3);
        }
        {
            const int gn = 3;
            PHASE_LOCALS
            FRESH_IDS
            PRIO(1); EXP4(sc, 0); SBAR(); PRIO(2); WP_ADDR(1); WP_ST(0); SBAR();
            EXP4(sc, 4); SBAR(); WP_ST(1); SBAR(); PACK4(p0, sc, 0); EXP4(sc, 8); SBAR(); WP_ST(2); SBAR(); EXP4(sc, 12); PACK4(p1, sc, 8); SBAR(); PRIO(3);
            asm volatile("" : "+v"(p0), "+v"(p1)); if (w >= 4) LOOPBAR();
            sc = MFMA32(kf1[1], q1, zero16); SBAR();
            sc = MFMA32K8(kf2[1], q2, sc); SBAR();
            o = MFMA32(vf[0][0], BF8(p0), zero16); SBAR();
            o = MFMA32(vf[0][1], BF8(p1), o); SBAR();
            asm volatile("" : "+v"(o), "+v"(sc)); if (w < 4) LOOPBAR();
            PRIO(1); EXP4(sc, 0); SBAR(); PRIO(2);
            EXP4(sc, 4); PACK4(p0, sc, 0); SBAR(); EXP4(sc, 8); EXP4(sc, 12); PACK4(p1, sc, 8); SBAR(); PRIO(3);
            sc = MFMA32(kf1[2], q1, zero16); SBAR();
            OPJ(ob = *(const bf16x8*)(otp + 0);)
            S2(STEP2A(1);)
            SBAR();
            sc = MFMA32K8(kf2[2], q2, sc); SBAR();
            o = MFMA32(vf[1][0], BF8(p0), o); SBAR();
            OPJ(oacc[0] = MFMA16(wa[0], ob, oacc[0]);)
            OPJ(wa[0] = WOLOAD(5120);)
            SBAR();
            o = MFMA32(vf[1][1], BF8(p1), o); SBAR();
            PRIO(1); EXP4(sc, 0); SBAR(); PRIO(2);
            EXP4(sc, 4); PACK4(p0, sc, 0); SBAR(); EXP4(sc, 8); EXP4(sc, 12); PACK4(p1, sc, 8); SBAR(); PRIO(3);
            sc = MFMA32(kf1[3], q1, zero16); SBAR();
            S2(STEP2B();)
            SBAR();
            sc = MFMA32K8(kf2[3], q2, sc); SBAR();
            o = MFMA32(vf[2][0], BF8(p0), o); SBAR();
            OPJ(oacc[1] = MFMA16(wa[1], ob, oacc[1]);)
            OPJ(wa[1] = WOLOAD(6144);)
            SBAR();
            o = MFMA32(vf[2][1], BF8(p1), o); SBAR();
            PRIO(1); EXP4(sc, 0); SBAR(); PRIO(2);
            EXP4(sc, 4); PACK4(p0, sc, 0); SBAR(); EXP4(sc, 8); EXP4(sc, 12); PACK4(p1, sc, 8); SBAR(); PRIO(3);
            sc = MFMA32(kf1[4], q1, zero16); SBAR();
            S2(STEP2C(gn - 2);)
            SBAR();
            sc = MFMA32K8(kf2[4], q2, sc); SBAR();
            o = MFMA32(vf[3][0], BF8(p0), o); SBAR();
            OPJ(oacc[2] = MFMA16(wa[2], ob, oacc[2]);)
            OPJ(wa[2] = WOLOAD(7168);)
            SBAR();
            o = MFMA32(vf[3][1], BF8(p1), o); SBAR();
            PRIO(1); EXP4(sc, 0); SBAR(); PRIO(2);
            EXP4(sc, 4); PACK4(p0, sc, 0); SBAR(); EXP4(sc, 8); EXP4(sc, 12); PACK4(p1, sc, 8); SBAR(); PRIO(3);
            sc = MFMA32(kf1[5], q1, zero16); SBAR();
            sc = MFMA32K8(kf2[5], q2, sc); SBAR();
            OPJ(oacc[3] = MFMA16(wa[3], ob, oacc[3]);)
            OPJ(wa[3] = WOLOAD(8192);)
            SBAR();
            o = MFMA32(vf[4][0], BF8(p0), o); SBAR();
            OPJ(oacc[4] = MFMA16(wa[4], ob, oacc[4]);)
            OPJ(wa[4] = WOLOAD(9216);)
            OPJ(ob = *(const bf16x8*)(otp + 512);)
            SBAR();
            o = MFMA32(vf[4][1], BF8(p1), o); SBAR();
            PRIO(1); EXP4(sc, 0); SBAR(); PRIO(2);
            EXP4(sc, 4); PACK4(p0, sc, 0); SBAR(); EXP4(sc, 8); EXP4(sc, 12); PACK4(p1, sc, 8); SBAR(); PRIO(3);
            sc = MFMA32(kf1[6], q1, zero16); SBAR();
            sc = MFMA32K8(kf2[6], q2, sc); SBAR();
            OPJ(oacc[0] = MFMA16(wa[0], ob, oacc[0]);)
            OPJ(wa[0] = WOLOAD(10240);)
            SBAR();
            o = MFMA32(vf[5][0], BF8(p0), o); SBAR();
            OPJ(oacc[1] = MFMA16(wa[1], ob, oacc[1]);)
            OPJ(wa[1] = WOLOAD(11264);)
            SBAR();
            o = MFMA32(vf[5][1], BF8(p1), o); SBAR();
            PRIO(1); EXP4(sc, 0); SBAR(); PRIO(2);
            EXP4(sc, 4); PACK4(p0, sc, 0); SBAR(); EXP4(sc, 8); EXP4(sc, 12); PACK4(p1, sc, 8); SBAR(); PRIO(3);
            sc = MFMA32(kf1[7], q1, zero16); SBAR();
            sc = MFMA32K8(kf2[7], q2, sc); SBAR();
            OPJ(oacc[2] = MFMA16(wa[2], ob, oacc[2]);)
            OPJ(wa[2] = WOLOAD(12288);)
            SBAR();
            o = MFMA32(vf[6][0], BF8(p0), o); SBAR();
            q1n = *(const bf16x8*)(smem + K2_QL + gn * 1536 + lane16); q2n = *(const bf16x4*)(q2v + gn * 1536);
            SBAR();
            o = MFMA32(vf[6][1], BF8(p1), o); SBAR();
            PRIO(1); EXP4(sc, 0); SBAR(); PRIO(2);
            EXP4(sc, 4); PACK4(p0, sc, 0); SBAR(); EXP4(sc, 8); EXP4(sc, 12); PACK4(p1, sc, 8); SBAR(); PRIO(3);
            sc = MFMA32(kf1[0], q1n, zero16); SBAR();
            sc = MFMA32K8(kf2[0], q2n, sc); SBAR();
            OPJ(oacc[3] = MFMA16(wa[3], ob, oacc[3]);)
            OPJ(wa[3] = WOLOAD(13312);)
            SBAR();
            o = MFMA32(vf[7][0], BF8(p0), o); SBAR();
            OPJ(oacc[4] = MFMA16(wa[4], ob, oacc[4]);)
            OPJ(wa[4] = WOLOAD(14336);)
            SBAR();
            o = MFMA32(vf[7][1], BF8(p1), o); SBAR();
            q1 = q1n; q2 = q2n;
            LSTAMP(1, 4);
        }
        {
            const int gn = 4;
            PHASE_LOCALS
            FRESH_IDS
            PRIO(1); EXP4(sc, 0); SBAR(); PRIO(2); WP_ADDR(0); WP_ST(0); SBAR();
            EXP4(sc, 4); SBAR(); WP_ST(1); SBAR(); PACK4(p0, sc, 0); EXP4(sc, 8); SBAR(); WP_ST(2); SBAR(); EXP4(sc, 12); PACK4(p1, sc, 8); SBAR(); PRIO(3);
            asm volatile("" : "+v"(p0), "+v"(p1)); if (w >= 4) LOOPBAR();
            sc = MFMA32(kf1[1], q1, zero16); SBAR();
            sc = MFMA32K8(kf2[1], q2, sc); SBAR();
            o = MFMA32(vf[0][0], BF8(p0), zero16); SBAR();
            o = MFMA32(vf[0][1], BF8(p1), o); SBAR();
            asm volatile("" : "+v"(o), "+v"(sc)); if (w < 4) LOOPBAR();
            PRIO(1); EXP4(sc, 0); SBAR(); PRIO(2);
            EXP4(sc, 4); PACK4(p0, sc, 0); SBAR(); EXP4(sc, 8); EXP4(sc, 12); PACK4(p1, sc, 8); SBAR(); PRIO(3);
            sc = MFMA32(kf1[2], q1, zero16); SBAR();
            OPJ(ob = *(const bf16x8*)(otp + 1024);)
            S2(STEP2A(0);)
            SBAR();
            sc = MFMA32K8(kf2[2], q2, sc); SBAR();
            o = MFMA32(vf[1][0], BF8(p0), o); SBAR();
            OPJ(oacc[0] = MFMA16(wa[0], ob, oacc[0]);)
            OPJ(wa[0] = WOLOAD(15360);)
            SBAR();
            o = MFMA32(vf[1][1], BF8(p1), o); SBAR();
            PRIO(1); EXP4(sc, 0); SBAR(); PRIO(2);
            EXP4(sc, 4); PACK4(p0, sc, 0); SBAR(); EXP4(sc, 8); EXP4(sc, 12); PACK4(p1, sc, 8); SBAR(); PRIO(3);
            sc = MFMA32(kf1[3], q1, zero16); SBAR();
            S2(STEP2B();)
            SBAR();
            sc = MFMA32K8(kf2[3], q2, sc); SBAR();
            OPJ(oacc[1] = MFMA16(wa[1], ob, oacc[1]);)
            OPJ(wa[1] = WOLOAD(16384);)
            SBAR();
            o = MFMA32(vf[2][0], BF8(p0), o); SBAR();
            OPJ(oacc[2] = MFMA16(wa[2], ob, oacc[2]);)
            OPJ(wa[2] = WOLOAD(17408);)
            SBAR();
            o = MFMA32(vf[2][1], BF8(p1), o); SBAR();
            PRIO(1); EXP4(sc, 0); SBAR(); PRIO(2);
            EXP4(sc, 4); PACK4(p0, sc, 0); SBAR(); EXP4(sc, 8); EXP4(sc, 12); PACK4(p1, sc, 8); SBAR(); PRIO(3);
            sc = MFMA32(kf1[4], q1, zero16); SBAR();
            S2(STEP2C(gn - 2);)
            SBAR();
            sc = MFMA32K8(kf2[4], q2, sc); SBAR();
            OPJ(oacc[3] = MFMA16(wa[3], ob, oacc[3]);)
            OPJ(wa[3] = WOLOAD(18432);)
            SBAR();
            o = MFMA32(vf[3][0], BF8(p0), o); SBAR();
            OPJ(oacc[4] = MFMA16(wa[4], ob, oacc[4]);)
            OPJ(wa[4] = WOLOAD(19456);)
            OPJ(ob = *(const bf16x8*)(otp + 1536);)
            SBAR();
            o = MFMA32(vf[3][1], BF8(p1), o); SBAR();
            PRIO(1); EXP4(sc, 0); SBAR(); PRIO(2);
            EXP4(sc, 4); PACK4(p0, sc, 0); SBAR(); EXP4(sc, 8); EXP4(sc, 12); PACK4(p1, sc, 8); SBAR(); PRIO(3);
            sc = MFMA32(kf1[5], q1, zero16); SBAR();
            OPJ(oacc[0] = MFMA16(wa[0], ob, oacc[0]);)
            OPJ(wa[0] = WOLOAD(20480);)
            SBAR();
            sc = MFMA32K8(kf2[5], q2, sc); SBAR();
            OPJ(oacc[1] = MFMA16(wa[1], ob, oacc[1]);)
            OPJ(wa[1] = WOLOAD(21504);)
            SBAR();
            o = MFMA32(vf[4][0], BF8(p0), o); SBAR();
            o = MFMA32(vf[4][1], BF8(p1), o); SBAR();
            PRIO(1); EXP4(sc, 0); SBAR(); PRIO(2);
            EXP4(sc, 4); PACK4(p0, sc, 0); SBAR(); EXP4(sc, 8); EXP4(sc, 12); PACK4(p1, sc, 8); SBAR(); PRIO(3);
            sc = MFMA32(kf1[6], q1, zero16); SBAR();
            OPJ(oacc[2] = MFMA16(wa[2], ob, oacc[2]);)
            OPJ(wa[2] = WOLOAD(22528);)
            SBAR();
            sc = MFMA32K8(kf2[6], q2, sc); SBAR();
            OPJ(oacc[3] = MFMA16(wa[3], ob, oacc[3]);)
            OPJ(wa[3] = WOLOAD(23552);)
            SBAR();
            o = MFMA32(vf[5][0], BF8(p0), o); SBAR();
            OPJ(oacc[4] = MFMA16(wa[4], ob, oacc[4]);)
            OPJ(wa[4] = WOLOAD(24576);)
            OPJ(ob = *(const bf16x8*)(otp + 2048);)
            SBAR();
            o = MFMA32(vf[5][1], BF8(p1), o); SBAR();
            PRIO(1); EXP4(sc, 0); SBAR(); PRIO(2);
            EXP4(sc, 4); PACK4(p0, sc, 0); SBAR(); EXP4(sc, 8); EXP4(sc, 12); PACK4(p1, sc, 8); SBAR(); PRIO(3);
            sc = MFMA32(kf1[7], q1, zero16); SBAR();
            OPJ(oacc[0] = MFMA16(wa[0], ob, oacc[0]);)
            OPJ(wa[0] = WOLOAD(25600);)
            SBAR();
            sc = MFMA32K8(kf2[7], q2, sc); SBAR();
            OPJ(oacc[1] = MFMA16(wa[1], ob, oacc[1]);)
            OPJ(wa[1] = WOLOAD(26624);)
            SBAR();
            o = MFMA32(vf[6][0], BF8(p0), o); SBAR();
            q1n = *(const bf16x8*)(smem + K2_QL + gn * 1536 + lane16); q2n = *(const bf16x4*)(q2v + gn * 1536);
            SBAR();
            o = MFMA32(vf[6][1], BF8(p1), o); SBAR();
            PRIO(1); EXP4(sc, 0); SBAR(); PRIO(2);
            EXP4(sc, 4); PACK4(p0, sc, 0); SBAR(); EXP4(sc, 8); EXP4(sc, 12); PACK4(p1, sc, 8); SBAR(); PRIO(3);
            sc = MFMA32(kf1[0], q1n, zero16); SBAR();
            OPJ(oacc[2] = MFMA16(wa[2], ob, oacc[2]);)
            OPJ(wa[2] = WOLOAD(27648);)
            SBAR();
            sc = MFMA32K8(kf2[0], q2n, sc); SBAR();
            OPJ(oacc[3] = MFMA16(wa[3], ob, oacc[3]);)
            OPJ(wa[3] = WOLOAD(28672);)
            SBAR();
            o = MFMA32(vf[7][0], BF8(p0), o); SBAR();
            OPJ(oacc[4] = MFMA16(wa[4], ob, oacc[4]);)
            OPJ(wa[4] = WOLOAD(29696);)
            SBAR();
            o = MFMA32(vf[7][1], BF8(p1), o); SBAR();
            q1 = q1n; q2 = q2n;
            LSTAMP(1, 5);
        }
        {
            const int gn = 5;
            PHASE_LOCALS
            FRESH_IDS
            PRIO(1); EXP4(sc, 0); SBAR(); PRIO(2); WP_ADDR(1); WP_ST(0); SBAR();
            EXP4(sc, 4); SBAR(); WP_ST(1); SBAR(); PACK4(p0, sc, 0); EXP4(sc, 8); SBAR(); WP_ST(2); SBAR(); EXP4(sc, 12); PACK4(p1, sc, 8); SBAR(); PRIO(3);
            asm volatile("" : "+v"(p0), "+v"(p1)); if (w >= 4) LOOPBAR();
            sc = MFMA32(kf1[1], q1, zero16); SBAR();
            sc = MFMA32K8(kf2[1], q2, sc); SBAR();
            o = MFMA32(vf[0][0], BF8(p0), zero16); SBAR();
            o = MFMA32(vf[0][1], BF8(p1), o); SBAR();
            asm volatile("" : "+v"(o), "+v"(sc)); if (w < 4) LOOPBAR();
            PRIO(1); EXP4(sc, 0); SBAR(); PRIO(2);
            EXP4(sc, 4); PACK4(p0, sc, 0); SBAR(); EXP4(sc, 8); EXP4(sc, 12); PACK4(p1, sc, 8); SBAR(); PRIO(3);
            sc = MFMA32(kf1[2], q1, zero16); SBAR();
            OPJ(ob = *(const bf16x8*)(otp + 2560);)
            S2(STEP2A(1);)
            SBAR();
            sc = MFMA32K8(kf2[2], q2, sc); SBAR();
            o = MFMA32(vf[1][0], BF8(p0), o); SBAR();
            OPJ(oacc[0] = MFMA16(wa[0], ob, oacc[0]);)
            OPJ(wa[0] = WOLOAD(30720);)
            SBAR();
            o = MFMA32(vf[1][1], BF8(p1), o); SBAR();
            PRIO(1); EXP4(sc, 0); SBAR(); PRIO(2);
            EXP4(sc, 4); PACK4(p0, sc, 0); SBAR(); EXP4(sc, 8); EXP4(sc, 12); PACK4(p1, sc, 8); SBAR(); PRIO(3);
            sc = MFMA32(kf1[3], q1, zero16); SBAR();
            S2(STEP2B();)
            SBAR();
            sc = MFMA32K8(kf2[3], q2, sc); SBAR();
            o = MFMA32(vf[2][0], BF8(p0), o); SBAR();
            OPJ(oacc[1] = MFMA16(wa[1], ob, oacc[1]);)
            OPJ(wa[1] = WOLOAD(31744);)
            SBAR();
            o = MFMA32(vf[2][1], BF8(p1), o); SBAR();
            PRIO(1); EXP4(sc, 0); SBAR(); PRIO(2);
            EXP4(sc, 4); PACK4(p0, sc, 0); SBAR(); EXP4(sc, 8); EXP4(sc, 12); PACK4(p1, sc, 8); SBAR(); PRIO(3);
            sc = MFMA32(kf1[4], q1, zero16); SBAR();
            S2(STEP2C(gn - 2);)
            SBAR();
            sc = MFMA32K8(kf2[4], q2, sc); SBAR();
            o = MFMA32(vf[3][0], BF8(p0), o); SBAR();
            OPJ(oacc[2] = MFMA16(wa[2], ob, oacc[2]);)
            OPJ(wa[2] = WOLOAD(32768);)
            SBAR();
            o = MFMA32(vf[3][1], BF8(p1), o); SBAR();
            PRIO(1); EXP4(sc, 0); SBAR(); PRIO(2);
            EXP4(sc, 4); PACK4(p0, sc, 0); SBAR(); EXP4(sc, 8); EXP4(sc, 12); PACK4(p1, sc, 8); SBAR(); PRIO(3);
            sc = MFMA32(kf1[5], q1, zero16); SBAR();
            sc = MFMA32K8(kf2[5], q2, sc); SBAR();
            OPJ(oacc[3] = MFMA16(wa[3], ob, oacc[3]);)
            OPJ(wa[3] = WOLOAD(33792);)
            SBAR();
            o = MFMA32(vf[4][0], BF8(p0), o); SBAR();
            OPJ(oacc[4] = MFMA16(wa[4], ob, oacc[4]);)
            OPJ(wa[4] = WOLOAD(34816);)
            OPJ(ob = *(const bf16x8*)(otp + 3072);)
            SBAR();
            o = MFMA32(vf[4][1], BF8(p1), o); SBAR();
            PRIO(1); EXP4(sc, 0); SBAR(); PRIO(2);
            EXP4(sc, 4); PACK4(p0, sc, 0); SBAR(); EXP4(sc, 8); EXP4(sc, 12); PACK4(p1, sc, 8); SBAR(); PRIO(3);
            sc = MFMA32(kf1[6], q1, zero16); SBAR();
            sc = MFMA32K8(kf2[6], q2, sc); SBAR();
            OPJ(oacc[0] = MFMA16(wa[0], ob, oacc[0]);)
            OPJ(wa[0] = WOLOAD(35840);)
            SBAR();
            o = MFMA32(vf[5][0], BF8(p0), o); SBAR();
            OPJ(oacc[1] = MFMA16(wa[1], ob, oacc[1]);)
            OPJ(wa[1] = WOLOAD(36864);)
            SBAR();
            o = MFMA32(vf[5][1], BF8(p1), o); SBAR();
            PRIO(1); EXP4(sc, 0); SBAR(); PRIO(2);
            EXP4(sc, 4); PACK4(p0, sc, 0); SBAR(); EXP4(sc, 8); EXP4(sc, 12); PACK4(p1, sc, 8); SBAR(); PRIO(3);
            sc = MFMA32(kf1[7], q1, zero16); SBAR();
            sc = MFMA32K8(kf2[7], q2, sc); SBAR();
            OPJ(oacc[2] = MFMA16(wa[2], ob, oacc[2]);)
            OPJ(wa[2] = WOLOAD(37888);)
            SBAR();
            o = MFMA32(vf[6][0], BF8(p0), o); SBAR();
            q1n = *(const bf16x8*)(smem + K2_QL + gn * 1536 + lane16); q2n = *(const bf16x4*)(q2v + gn * 1536);
            SBAR();
            o = MFMA32(vf[6][1], BF8(p1), o); SBAR();
            PRIO(1); EXP4(sc, 0); SBAR(); PRIO(2);
            EXP4(sc, 4); PACK4(p0, sc, 0); SBAR(); EXP4(sc, 8); EXP4(sc, 12); PACK4(p1, sc, 8); SBAR(); PRIO(3);
            sc = MFMA32(kf1[0], q1n, zero16); SBAR();
            sc = MFMA32K8(kf2[0], q2n, sc); SBAR();
            OPJ(oacc[3] = MFMA16(wa[3], ob, oacc[3]);)
            OPJ(wa[3] = WOLOAD(38912);)
            SBAR();
            o = MFMA32(vf[7][0], BF8(p0), o); SBAR();
            OPJ(oacc[4] = MFMA16(wa[4], ob, oacc[4]);)
            OPJ(wa[4] = WOLOAD(39936);)
            SBAR();
            o = MFMA32(vf[7][1], BF8(p1), o); SBAR();
            q1 = q1n; q2 = q2n;
            LSTAMP(1, 6);
        }
        {
            const int gn = 6;
            PHASE_LOCALS
            FRESH_IDS
            PRIO(1); EXP4(sc, 0); SBAR(); PRIO(2); WP_ADDR(0); WP_ST(0); SBAR();
            EXP4(sc, 4); SBAR(); WP_ST(1); SBAR(); PACK4(p0, sc, 0); EXP4(sc, 8); SBAR(); WP_ST(2); SBAR(); EXP4(sc, 12); PACK4(p1, sc, 8); SBAR(); PRIO(3);
            asm volatile("" : "+v"(p0), "+v"(p1)); if (w >= 4) LOOPBAR();
            sc = MFMA32(kf1[1], q1, zero16); SBAR();
            sc = MFMA32K8(kf2[1], q2, sc); SBAR();
            o = MFMA32(vf[0][0], BF8(p0), zero16); SBAR();
            o = MFMA32(vf[0][1], BF8(p1), o); SBAR();
            asm volatile("" : "+v"(o), "+v"(sc)); if (w < 4) LOOPBAR();
            PRIO(1); EXP4(sc, 0); SBAR(); PRIO(2);
            EXP4(sc, 4); PACK4(p0, sc, 0); SBAR(); EXP4(sc, 8); EXP4(sc, 12); PACK4(p1, sc, 8); SBAR(); PRIO(3);
            sc = MFMA32(kf1[2], q1, zero16); SBAR();
            OPJ(ob = *(const bf16x8*)(otp + 3584);)
            S2(STEP2A(0);)
            SBAR();
            sc = MFMA32K8(kf2[2], q2, sc); SBAR();
            o = MFMA32(vf[1][0], BF8(p0), o); SBAR();
            OPJ(oacc[0] = MFMA16(wa[0], ob, oacc[0]);)
            OPJ(wa[0] = WOLOAD(40960);)
            SBAR();
            o = MFMA32(vf[1][1], BF8(p1), o); SBAR();
            PRIO(1); EXP4(sc, 0); SBAR(); PRIO(2);
            EXP4(sc, 4); PACK4(p0, sc, 0); SBAR(); EXP4(sc, 8); EXP4(sc, 12); PACK4(p1, sc, 8); SBAR(); PRIO(3);
            sc = MFMA32(kf1[3], q1, zero16); SBAR();
            S2(STEP2B();)
            SBAR();
            sc = MFMA32K8(kf2[3], q2, sc); SBAR();
            OPJ(oacc[1] = MFMA16(wa[1], ob, oacc[1]);)
            OPJ(wa[1] = WOLOAD(41984);)
            SBAR();
            o = MFMA32(vf[2][0], BF8(p0), o); SBAR();
            OPJ(oacc[2] = MFMA16(wa[2], ob, oacc[2]);)
            OPJ(wa[2] = WOLOAD(43008);)
            SBAR();
            o = MFMA32(vf[2][1], BF8(p1), o); SBAR();
            PRIO(1); EXP4(sc, 0); SBAR(); PRIO(2);
            EXP4(sc, 4); PACK4(p0, sc, 0); SBAR(); EXP4(sc, 8); EXP4(sc, 12); PACK4(p1, sc, 8); SBAR(); PRIO(3);
            sc = MFMA32(kf1[4], q1, zero16); SBAR();
            S2(STEP2C(gn - 2);)
            SBAR();
            sc = MFMA32K8(kf2[4], q2, sc); SBAR();
            OPJ(oacc[3] = MFMA16(wa[3], ob, oacc[3]);)
            OPJ(wa[3] = WOLOAD(44032);)
            SBAR();
            o = MFMA32(vf[3][0], BF8(p0), o); SBAR();
            OPJ(oacc[4] = MFMA16(wa[4], ob, oacc[4]);)
            OPJ(wa[4] = WOLOAD(45056);)
            OPJ(ob = *(const bf16x8*)(otp + 4096);)
            SBAR();
            o = MFMA32(vf[3][1], BF8(p1), o); SBAR();
            PRIO(1); EXP4(sc, 0); SBAR(); PRIO(2);
            EXP4(sc, 4); PACK4(p0, sc, 0); SBAR(); EXP4(sc, 8); EXP4(sc, 12); PACK4(p1, sc, 8); SBAR(); PRIO(3);
            sc = MFMA32(kf1[5], q1, zero16); SBAR();
            OPJ(oacc[0] = MFMA16(wa[0], ob, oacc[0]);)
            OPJ(wa[0] = WOLOAD(46080);)
            SBAR();
            sc = MFMA32K8(kf2[5], q2, sc); SBAR();
            OPJ(oacc[1] = MFMA16(wa[1], ob, oacc[1]);)
            OPJ(wa[1] = WOLOAD(47104);)
            SBAR();
            o = MFMA32(vf[4][0], BF8(p0), o); SBAR();
            o = MFMA32(vf[4][1], BF8(p1), o); SBAR();
            PRIO(1); EXP4(sc, 0); SBAR(); PRIO(2);
            EXP4(sc, 4); PACK4(p0, sc, 0); SBAR(); EXP4(sc, 8); EXP4(sc, 12); PACK4(p1, sc, 8); SBAR(); PRIO(3);
            sc = MFMA32(kf1[6], q1, zero16); SBAR();
            OPJ(oacc[2] = MFMA16(wa[2], ob, oacc[2]);)
            OPJ(wa[2] = WOLOAD(48128);)
            SBAR();
            sc = MFMA32K8(kf2[6], q2, sc); SBAR();
            OPJ(oacc[3] = MFMA16(wa[3], ob, oacc[3]);)
            OPJ(wa[3] = WOLOAD(49152);)
            SBAR();
            o = MFMA32(vf[5][0], BF8(p0), o); SBAR();
            OPJ(oacc[4] = MFMA16(wa[4], ob, oacc[4]);)
            OPJ(wa[4] = WOLOAD(50176);)
            OPJ(ob = *(const bf16x8*)(otp + 4608);)
            SBAR();
            o = MFMA32(vf[5][1], BF8(p1), o); SBAR();
            PRIO(1); EXP4(sc, 0); SBAR(); PRIO(2);
            EXP4(sc, 4); PACK4(p0, sc, 0); SBAR(); EXP4(sc, 8); EXP4(sc, 12); PACK4(p1, sc, 8); SBAR(); PRIO(3);
            sc = MFMA32(kf1[7], q1, zero16); SBAR();
            OPJ(oacc[0] = MFMA16(wa[0], ob, oacc[0]);)
            OPJ(wa[0] = WOLOAD(51200);)
            SBAR();
            sc = MFMA32K8(kf2[7], q2, sc); SBAR();
            OPJ(oacc[1] = MFMA16(wa[1], ob, oacc[1]);)
            OPJ(wa[1] = WOLOAD(52224);)
            SBAR();
            o = MFMA32(vf[6][0], BF8(p0), o); SBAR();
            q1n = *(const bf16x8*)(smem + K2_QL + gn * 1536 + lane16); q2n = *(const bf16x4*)(q2v + gn * 1536);
            SBAR();
            o = MFMA32(vf[6][1], BF8(p1), o); SBAR();
            PRIO(1); EXP4(sc, 0); SBAR(); PRIO(2);
            EXP4(sc, 4); PACK4(p0, sc, 0); SBAR(); EXP4(sc, 8); EXP4(sc, 12); PACK4(p1, sc, 8); SBAR(); PRIO(3);
            sc = MFMA32(kf1[0], q1n, zero16); SBAR();
            OPJ(oacc[2] = MFMA16(wa[2], ob, oacc[2]);)
            OPJ(wa[2] = WOLOAD(53248);)
            SBAR();
            sc = MFMA32K8(kf2[0], q2n, sc); SBAR();
            OPJ(oacc[3] = MFMA16(wa[3], ob, oacc[3]);)
            OPJ(wa[3] = WOLOAD(54272);)
            SBAR();
            o = MFMA32(vf[7][0], BF8(p0), o); SBAR();
            OPJ(oacc[4] = MFMA16(wa[4], ob, oacc[4]);)
            OPJ(wa[4] = WOLOAD(55296);)
            SBAR();
            o = MFMA32(vf[7][1], BF8(p1), o); SBAR();
            q1 = q1n; q2 = q2n;
            LSTAMP(1, 7);
        }
        {
            const int gn = 7;
            PHASE_LOCALS
            FRESH_IDS
            PRIO(1); EXP4(sc, 0); SBAR(); PRIO(2); WP_ADDR(1); WP_ST(0); SBAR();
            EXP4(sc, 4); SBAR(); WP_ST(1); SBAR(); PACK4(p0, sc, 0); EXP4(sc, 8); SBAR(); WP_ST(2); SBAR(); EXP4(sc, 12); PACK4(p1, sc, 8); SBAR(); PRIO(3);
            asm volatile("" : "+v"(p0), "+v"(p1)); if (w >= 4) LOOPBAR();
            sc = MFMA32(kf1[1], q1, zero16); SBAR();
            sc = MFMA32K8(kf2[1], q2, sc); SBAR();
            o = MFMA32(vf[0][0], BF8(p0), zero16); SBAR();
            o = MFMA32(vf[0][1], BF8(p1), o); SBAR();
            asm volatile("" : "+v"(o), "+v"(sc)); if (w < 4) LOOPBAR();
            PRIO(1); EXP4(sc, 0); SBAR(); PRIO(2);
            EXP4(sc, 4); PACK4(p0, sc, 0); SBAR(); EXP4(sc, 8); EXP4(sc, 12); PACK4(p1, sc, 8); SBAR(); PRIO(3);
            sc = MFMA32(kf1[2], q1, zero16); SBAR();
            OPJ(ob = *(const bf16x8*)(otp + 5120);)
            S2(STEP2A(1);)
            SBAR();
            sc = MFMA32K8(kf2[2], q2, sc); SBAR();
            o = MFMA32(vf[1][0], BF8(p0), o); SBAR();
            OPJ(oacc[0] = MFMA16(wa[0], ob, oacc[0]);)
            OPJ(wa[0] = WOLOAD(56320);)
            SBAR();
            o = MFMA32(vf[1][1], BF8(p1), o); SBAR();
            PRIO(1); EXP4(sc, 0); SBAR(); PRIO(2);
            EXP4(sc, 4); PACK4(p0, sc, 0); SBAR(); EXP4(sc, 8); EXP4(sc, 12); PACK4(p1, sc, 8); SBAR(); PRIO(3);
            sc = MFMA32(kf1[3], q1, zero16); SBAR();
            S2(STEP2B();)
            SBAR();
            sc = MFMA32K8(kf2[3], q2, sc); SBAR();
            o = MFMA32(vf[2][0], BF8(p0), o); SBAR();
            OPJ(oacc[1] = MFMA16(wa[1], ob, oacc[1]);)
            OPJ(wa[1] = WOLOAD(57344);)
            SBAR();
            o = MFMA32(vf[2][1], BF8(p1), o); SBAR();
            PRIO(1); EXP4(sc, 0); SBAR(); PRIO(2);
            EXP4(sc, 4); PACK4(p0, sc, 0); SBAR(); EXP4(sc, 8); EXP4(sc, 12); PACK4(p1, sc, 8); SBAR(); PRIO(3);
            sc = MFMA32(kf1[4], q1, zero16); SBAR();
            S2(STEP2C(gn - 2);)
            SBAR();
            sc = MFMA32K8(kf2[4], q2, sc); SBAR();
            o = MFMA32(vf[3][0], BF8(p0), o); SBAR();
            OPJ(oacc[2] = MFMA16(wa[2], ob, oacc[2]);)
            OPJ(wa[2] = WOLOAD(58368);)
            SBAR();
            o = MFMA32(vf[3][1], BF8(p1), o); SBAR();
            PRIO(1); EXP4(sc, 0); SBAR(); PRIO(2);
            EXP4(sc, 4); PACK4(p0, sc, 0); SBAR(); EXP4(sc, 8); EXP4(sc, 12); PACK4(p1, sc, 8); SBAR(); PRIO(3);
            sc = MFMA32(kf1[5], q1, zero16); SBAR();
            sc = MFMA32K8(kf2[5], q2, sc); SBAR();
            OPJ(oacc[3] = MFMA16(wa[3], ob, oacc[3]);)
            OPJ(wa[3] = WOLOAD(59392);)
            SBAR();
            o = MFMA32(vf[4][0], BF8(p0), o); SBAR();
            OPJ(oacc[4] = MFMA16(wa[4], ob, oacc[4]);)
            OPJ(wa[4] = WOLOAD(60416);)
            OPJ(ob = *(const bf16x8*)(otp + 5632);)
            SBAR();
            o = MFMA32(vf[4][1], BF8(p1), o); SBAR();
            PRIO(1); EXP4(sc, 0); SBAR(); PRIO(2);
            EXP4(sc, 4); PACK4(p0, sc, 0); SBAR(); EXP4(sc, 8); EXP4(sc, 12); PACK4(p1, sc, 8); SBAR(); PRIO(3);
            sc = MFMA32(kf1[6], q1, zero16); SBAR();
            sc = MFMA32K8(kf2[6], q2, sc); SBAR();
            OPJ(oacc[0] = MFMA16(wa[0], ob, oacc[0]);)
            OPJ(wa[0] = WOLOAD(61440);)
            SBAR();
            o = MFMA32(vf[5][0], BF8(p0), o); SBAR();
            OPJ(oacc[1] = MFMA16(wa[1], ob, oacc[1]);)
            OPJ(wa[1] = WOLOAD(62464);)
            SBAR();
            o = MFMA32(vf[5][1], BF8(p1), o); SBAR();
            PRIO(1); EXP4(sc, 0); SBAR(); PRIO(2);
            EXP4(sc, 4); PACK4(p0, sc, 0); SBAR(); EXP4(sc, 8); EXP4(sc, 12); PACK4(p1, sc, 8); SBAR(); PRIO(3);
            sc = MFMA32(kf1[7], q1, zero16); SBAR();
            sc = MFMA32K8(kf2[7], q2, sc); SBAR();
            OPJ(oacc[2] = MFMA16(wa[2], ob, oacc[2]);)
            OPJ(wa[2] = WOLOAD(63488);)
            SBAR();
            o = MFMA32(vf[6][0], BF8(p0), o); SBAR();
            q1n = *(const bf16x8*)(smem + K2_QL + gn * 1536 + lane16); q2n = *(const bf16x4*)(q2v + gn * 1536);
            SBAR();
            o = MFMA32(vf[6][1], BF8(p1), o); SBAR();
            PRIO(1); EXP4(sc, 0); SBAR(); PRIO(2);
            EXP4(sc, 4); PACK4(p0, sc, 0); SBAR(); EXP4(sc, 8); EXP4(sc, 12); PACK4(p1, sc, 8); SBAR(); PRIO(3);
            sc = MFMA32(kf1[0], q1n, zero16); SBAR();
            sc = MFMA32K8(kf2[0], q2n, sc); SBAR();
            OPJ(oacc[3] = MFMA16(wa[3], ob, oacc[3]);)
            OPJ(wa[3] = WOLOAD(64512);)
            SBAR();
            o = MFMA32(vf[7][0], BF8(p0), o); SBAR();
            OPJ(oacc[4] = MFMA16(wa[4], ob, oacc[4]);)
            OPJ(wa[4] = WOLOAD(65536);)
            SBAR();
            o = MFMA32(vf[7][1], BF8(p1), o); SBAR();
            q1 = q1n; q2 = q2n;
            LSTAMP(1, 8);
        }

        bf16x8 wl[10];
        {
            const int gn = 8;
            PHASE_LOCALS
            PRIO(1); EXP4(sc, 0); SBAR(); PRIO(2); WP_ADDR(0); WP_ST(0); SBAR();
            EXP4(sc, 4); SBAR(); WP_ST(1); SBAR(); PACK4(p0, sc, 0); EXP4(sc, 8); SBAR(); WP_ST(2); SBAR(); EXP4(sc, 12); PACK4(p1, sc, 8); SBAR(); PRIO(3);
            asm volatile("" : "+v"(p0), "+v"(p1)); if (w >= 4) LOOPBAR();
            sc = MFMA32(kf1[1], q1, zero16); SBAR();
            sc = MFMA32K8(kf2[1], q2, sc); SBAR();
            o = MFMA32(vf[0][0], BF8(p0), zero16); SBAR();
            o = MFMA32(vf[0][1], BF8(p1), o); SBAR();
            asm volatile("" : "+v"(o), "+v"(sc)); if (w < 4) LOOPBAR();
            PRIO(1); EXP4(sc, 0); SBAR(); PRIO(2);
            EXP4(sc, 4); PACK4(p0, sc, 0); SBAR(); EXP4(sc, 8); EXP4(sc, 12); PACK4(p1, sc, 8); SBAR(); PRIO(3);
            sc = MFMA32(kf1[2], q1, zero16); SBAR();
            OPJ(ob = *(const bf16x8*)(otp + 6144);)
            S2(STEP2A(0);)
            SBAR();
            sc = MFMA32K8(kf2[2], q2, sc); SBAR();
            o = MFMA32(vf[1][0], BF8(p0), o); SBAR();
            OPJ(oacc[0] = MFMA16(wa[0], ob, oacc[0]);)
            OPJ(wa[0] = WOLOAD(66560);)
            SBAR();
            o = MFMA32(vf[1][1], BF8(p1), o); SBAR();
            PRIO(1); EXP4(sc, 0); SBAR(); PRIO(2);
            EXP4(sc, 4); PACK4(p0, sc, 0); SBAR(); EXP4(sc, 8); EXP4(sc, 12); PACK4(p1, sc, 8); SBAR(); PRIO(3);
            sc = MFMA32(kf1[3], q1, zero16); SBAR();
            S2(STEP2B();)
            wl[0] = WOLOAD(81920);
            SBAR();
            sc = MFMA32K8(kf2[3], q2, sc); SBAR();
            OPJ(oacc[1] = MFMA16(wa[1], ob, oacc[1]);)
            OPJ(wa[1] = WOLOAD(67584);)
            SBAR();
            o = MFMA32(vf[2][0], BF8(p0), o); SBAR();
            OPJ(oacc[2] = MFMA16(wa[2], ob, oacc[2]);)
            OPJ(wa[2] = WOLOAD(68608);)
            SBAR();
            o = MFMA32(vf[2][1], BF8(p1), o); SBAR();
            PRIO(1); EXP4(sc, 0); SBAR(); PRIO(2);
            EXP4(sc, 4); PACK4(p0, sc, 0); SBAR(); EXP4(sc, 8); EXP4(sc, 12); PACK4(p1, sc, 8); SBAR(); PRIO(3);
            sc = MFMA32(kf1[4], q1, zero16); SBAR();
            S2(STEP2C(gn - 2);)
            wl[1] = WOLOAD(82944);
            SBAR();
            sc = MFMA32K8(kf2[4], q2, sc); SBAR();
            OPJ(oacc[3] = MFMA16(wa[3], ob, oacc[3]);)
            OPJ(wa[3] = WOLOAD(69632);)
            SBAR();
            o = MFMA32(vf[3][0], BF8(p0), o); SBAR();
            OPJ(oacc[4] = MFMA16(wa[4], ob, oacc[4]);)
            OPJ(wa[4] = WOLOAD(70656);)
            OPJ(ob = *(const bf16x8*)(otp + 6656);)
            wl[2] = WOLOAD(83968);
            SBAR();
            o = MFMA32(vf[3][1], BF8(p1), o); SBAR();
            PRIO(1); EXP4(sc, 0); SBAR(); PRIO(2);
            EXP4(sc, 4); PACK4(p0, sc, 0); SBAR(); EXP4(sc, 8); EXP4(sc, 12); PACK4(p1, sc, 8); SBAR(); PRIO(3);
            sc = MFMA32(kf1[5], q1, zero16); SBAR();
            OPJ(oacc[0] = MFMA16(wa[0], ob, oacc[0]);)
            OPJ(wa[0] = WOLOAD(71680);)
            wl[3] = WOLOAD(84992);
            SBAR();
            sc = MFMA32K8(kf2[5], q2, sc); SBAR();
            OPJ(oacc[1] = MFMA16(wa[1], ob, oacc[1]);)
            OPJ(wa[1] = WOLOAD(72704);)
            SBAR();
            o = MFMA32(vf[4][0], BF8(p0), o); SBAR();
            wl[4] = WOLOAD(86016);
            SBAR();
            o = MFMA32(vf[4][1], BF8(p1), o); SBAR();
            PRIO(1); EXP4(sc, 0); SBAR(); PRIO(2);
            EXP4(sc, 4); PACK4(p0, sc, 0); SBAR(); EXP4(sc, 8); EXP4(sc, 12); PACK4(p1, sc, 8); SBAR(); PRIO(3);
            sc = MFMA32(kf1[6], q1, zero16); SBAR();
            OPJ(oacc[2] = MFMA16(wa[2], ob, oacc[2]);)
            OPJ(wa[2] = WOLOAD(73728);)
            wl[5] = WOLOAD(87040);
            SBAR();
            sc = MFMA32K8(kf2[6], q2, sc); SBAR();
            OPJ(oacc[3] = MFMA16(wa[3], ob, oacc[3]);)
            OPJ(wa[3] = WOLOAD(74752);)
            SBAR();
            o = MFMA32(vf[5][0], BF8(p0), o); SBAR();
            OPJ(oacc[4] = MFMA16(wa[4], ob, oacc[4]);)
            OPJ(wa[4] = WOLOAD(75776);)
            OPJ(ob = *(const bf16x8*)(otp + 7168);)
            wl[6] = WOLOAD(88064);
            SBAR();
            o = MFMA32(vf[5][1], BF8(p1), o); SBAR();
            PRIO(1); EXP4(sc, 0); SBAR(); PRIO(2);
            EXP4(sc, 4); PACK4(p0, sc, 0); SBAR(); EXP4(sc, 8); EXP4(sc, 12); PACK4(p1, sc, 8); SBAR(); PRIO(3);
            sc = MFMA32(kf1[7], q1, zero16); SBAR();
            OPJ(oacc[0] = MFMA16(wa[0], ob, oacc[0]);)
            OPJ(wa[0] = WOLOAD(76800);)
            wl[7] = WOLOAD(89088);
            SBAR();
            sc = MFMA32K8(kf2[7], q2, sc); SBAR();
            OPJ(oacc[1] = MFMA16(wa[1], ob, oacc[1]);)
            OPJ(wa[1] = WOLOAD(77824);)
            SBAR();
            o = MFMA32(vf[6][0], BF8(p0), o); SBAR();
            wl[8] = WOLOAD(90112);
            SBAR();
            o = MFMA32(vf[6][1], BF8(p1), o); SBAR();
            PRIO(1); EXP4(sc, 0); SBAR(); PRIO(2);
            EXP4(sc, 4); PACK4(p0, sc, 0); SBAR(); EXP4(sc, 8); EXP4(sc, 12); PACK4(p1, sc, 8); SBAR(); PRIO(3);
            OPJ(oacc[2] = MFMA16(wa[2], ob, oacc[2]);)
            OPJ(wa[2] = WOLOAD(78848);)
            wl[9] = WOLOAD(91136);
            SBAR();
            OPJ(oacc[3] = MFMA16(wa[3], ob, oacc[3]);)
            OPJ(wa[3] = WOLOAD(79872);)
            SBAR();
            o = MFMA32(vf[7][0], BF8(p0), o); SBAR();
            OPJ(oacc[4] = MFMA16(wa[4], ob, oacc[4]);)
            OPJ(wa[4] = WOLOAD(80896);)
            SBAR();
            o = MFMA32(vf[7][1], BF8(p1), o); SBAR();

            STAMP(1, 9); RSTAMP(29);
            otp += 7680;
        }
        PRIO(0);
        XWLOAD();
        WRITE_PARTIAL(1);
        bf16x8 wt[10];
        {
            const char* tp = smem + K2_TAIL + w * 10240 + OPQ16();
#pragma unroll
            for (int f = 0; f < 10; ++f) wt[f] = *(const bf16x8*)(tp + f * 1024);
        }
        BAR_LDS();
        STAMP(1, 12);
        {
            PHASE_LOCALS
            STEP2A(1); SBAR();
            const bf16x8 ob0 = *(const bf16x8*)(otp), ob1 = *(const bf16x8*)(otp + 512);
#pragma unroll
            for (int t = 0; t < 5; ++t) oacc[t] = MFMA16(wa[t], ob0, oacc[t]);
            STEP2B(); STEP2C(7);
            if (badm != 0ull) *(unsigned*)(smem + K2_MISC) = 1u;
#pragma unroll
            for (int t = 0; t < 5; ++t) oacc[t] = MFMA16(wl[t], ob1, oacc[t]);
            asm volatile("" : "+v"(oacc[0]), "+v"(oacc[1]), "+v"(oacc[2]), "+v"(oacc[3]), "+v"(oacc[4]));
        }
        BAR_LDS();
        STAMP(1, 13);
        {
            const bf16x8 ob2 = *(const bf16x8*)(otp + 1024), ob3 = *(const bf16x8*)(otp + 1536), ob4 = *(const bf16x8*)(otp + 2048);
#pragma unroll
            for (int t = 0; t < 5; ++t) oacc[t] = MFMA16(wl[5 + t], ob2, oacc[t]);
#pragma unroll
            for (int t = 0; t < 5; ++t) oacc[t] = MFMA16(wt[t], ob3, oacc[t]);
#pragma unroll
            for (int t = 0; t < 5; ++t) oacc[t] = MFMA16(wt[5 + t], ob4, oacc[t]);
        }
        const unsigned bad = *(const unsigned*)(smem + K2_MISC);
        asm volatile("" : "+v"(xa), "+v"(xb1), "+v"(wa4), "+v"(wb1));
        if (__builtin_amdgcn_readfirstlane(bad) == 0u) break;

        {
            FRESH_IDS
            const unsigned l16 = OPQ16();
            const unsigned lane = l16 >> 4, r32 = lane & 31, hi = lane >> 5;
            for (int g = 0; g < 8; ++g) {
                const bf16x8 qa = *(const bf16x8*)(smem + K2_QL + g * 1536 + l16);
                bf16x4 qb = *(const bf16x4*)(smem + K2_QL + g * 1536 + 1024 + (l16 >> 1));
                if (hi == 1) qb = (bf16x4){0, 0, 0, 0};
                float m = -INFINITY;
                for (int t = 0; t < 8; ++t) {
                    const bf16x8 ka = *(const bf16x8*)(ws + OFF_KF1 + (size_t)(w * 8 + t) * 1024 + l16);
                    const bf16x4 kb = *(const bf16x4*)(ws + OFF_KF2 + (size_t)((w * 8 + t) * 32 + r32) * 8);
                    f32x16 s = MFMA32(ka, qa, zero16);
                    s = MFMA32K8(kb, qb, s);
                    m = fmaxf(m, max16(s));
                }
                m = max32(m);
                if (hi == 0) *(float*)(smem + K2_PBUF + ((w * 8 + g) * 32 + r32) * 4) = m;
            }
            BAR_LDS();
            if (hi == 1) {
                float m = -INFINITY;
#pragma unroll
                for (int u = 0; u < 8; ++u) m = fmaxf(m, *(const float*)(smem + K2_PBUF + ((u * 8 + w) * 32 + r32) * 4));
                ushort_t a, b2, c; split3(-m, a, b2, c);
                *(bf16x4*)(smem + K2_QL + w * 1536 + 1024 + (l16 >> 1)) = (bf16x4){(short)a, (short)b2, (short)c, 0};
            }
        }
        asm volatile("" : "=v"(q1w), "=v"(q2w), "=v"(k1w), "=v"(k2w));
    }

    STAMP(1, 10); RSTAMP(24);
    FRESH_IDS
    const int lane = OPQ16() >> 4, l15 = lane & 15, lg = lane >> 4;
    if (l15 < 8) {
#pragma unroll
        for (int t = 0; t < 5; ++t) *(f32x4*)(smem + K2_STG + l15 * 2576 + (w * 80 + t * 16 + lg * 4) * 4) = oacc[t];
    }
    BAR_LDS();
    STAMP(1, 15);
    unsigned hp[5], hnp[5], op[5];
    f32x2 hf[5];
    f32x2 sum2 = {0.f, 0.f};
    {
        const f32x4 a0 = *(const f32x4*)(smem + K2_STG + w * 2576 + lane * 32), a1 = *(const f32x4*)(smem + K2_STG + w * 2576 + lane * 32 + 16);
        const f32x2 a2 = *(const f32x2*)(smem + K2_STG + w * 2576 + 2048 + lane * 8);
        const f32x2 av[5] = {{a0[0], a0[1]}, {a0[2], a0[3]}, {a1[0], a1[1]}, {a1[2], a1[3]}, a2};
#pragma unroll
        for (int i = 0; i < 5; ++i) {
            hp[i] = cvtpk2(unpk(i < 4 ? xa[i] : xb1) + unpk(cvtpk2(av[i])));
            hf[i] = unpk(hp[i]);
        }
    }
    const float c0 = __builtin_bit_cast(float, __builtin_amdgcn_readfirstlane(__builtin_bit_cast(int, hf[0][0])));
    f32x2 sq2 = {0.f, 0.f};
#pragma unroll
    for (int i = 0; i < 5; ++i) { const f32x2 d = hf[i] - c0; sum2 += d; sq2 += d * d; }
    float sd = sum2[0] + sum2[1], sq = sq2[0] + sq2[1];
    wave_sum2(sd, sq);
    sd *= (1.0f / DM);
    const float mean = c0 + sd;
    const float var = fmaxf(sq * (1.0f / DM) - sd * sd, 0.f);
    const float mb = rbf(mean);
    const float sig = rbf(__builtin_amdgcn_sqrtf(rbf(rbf(var) + 1e-5f)));
    const float rsig = __builtin_amdgcn_rcpf(sig);
    f32x2 rp2 = {0.f, 0.f};
    const int token = wg * 8 + w;
#pragma unroll
    for (int i = 0; i < 5; ++i) {
        hnp[i] = cvtpk2(unpk(cvtpk2(hf[i] - mb)) * rsig);
        const f32x2 hn2 = unpk(hnp[i]);
        rp2 += hn2 * unpk(i < 4 ? wa4[i] : wb1);
        op[i] = cvtpk2(hf[i] + hn2);
    }
    const float rsc = wave_sum(rp2[0] + rp2[1]);
    const unsigned rb = f2bf(rsc);
    const unsigned key = (((rb & 0x8000u) ? (~rb & 0xffffu) : (rb | 0x8000u)) << 16) | (unsigned)(2047 - token);
    if (lane == 0) *(unsigned*)(smem + K2_MISC + 16 + w * 4) = key;
    STAMP(1, 19);
    BAR_LDS();
    STAMP(1, 20);
    bool cand;
    {
        const u32x4 ka = *(const u32x4*)(smem + K2_MISC + 16), kb = *(const u32x4*)(smem + K2_MISC + 32);
        const unsigned best = max(max(max(ka[0], ka[1]), max(ka[2], ka[3])), max(max(kb[0], kb[1]), max(kb[2], kb[3])));
        cand = __builtin_amdgcn_readfirstlane(best) == __builtin_amdgcn_readfirstlane(key);
    }
    char* orow = (char*)(out + (size_t)token * DM);
#define OUT_ROW() do { *(u32x4*)(orow + lane * 16) = (u32x4){op[0], op[1], op[2], op[3]}; *(unsigned*)(orow + 1024 + lane * 4) = op[4]; } while (0)
    if (cand) {
        if (lane == 0) __hip_atomic_store((GAS unsigned long long*)(ws + OFF_KEYG) + wg, ((unsigned long long)GTAG << 32) | key, __ATOMIC_RELAXED, __HIP_MEMORY_SCOPE_AGENT);
        const int ro = (int)OFF_CROW + wg * 5120;
        __builtin_amdgcn_raw_buffer_store_b128((u32x4){hnp[0], GTAG, hnp[1], GTAG}, wsr, lane * 32, ro, 16);
        __builtin_amdgcn_raw_buffer_store_b128((u32x4){hnp[2], GTAG, hnp[3], GTAG}, wsr, lane * 32 + 16, ro, 16);
        __builtin_amdgcn_raw_buffer_store_b64((u32x2){hnp[4], GTAG}, wsr, lane * 8, ro + 2048, 16);
        __builtin_amdgcn_raw_buffer_store_b128((u32x4){hp[0], GTAG, hp[1], GTAG}, wsr, lane * 32, ro + 2560, 16);
        __builtin_amdgcn_raw_buffer_store_b128((u32x4){hp[2], GTAG, hp[3], GTAG}, wsr, lane * 32 + 16, ro + 2560, 16);
        __builtin_amdgcn_raw_buffer_store_b64((u32x2){hp[4], GTAG}, wsr, lane * 8, ro + 4608, 16);
    }
    if (!cand) OUT_ROW();
    if (w < 7) {
        if (bi < 216) {
            const char* gp = (const char*)((lane & 1) ? w_up : w_gate) + (size_t)(lane >> 1) * 3456 + gcb * 16;
#pragma unroll
            for (int i = 0; i < 3; ++i) { const int in = min(w + 7 * i, 19); glds16(gp + (size_t)in * 110592, lds0 + K2_GU + in * 1024); }
        } else {
            const unsigned vo2 = (lane >> 1) * 1280 + (lane & 1) * 16;
#pragma unroll
            for (int i = 0; i < 8; ++i) { const int in = min(w + 7 * i, 53); glds16s((const char*)w_down + dcb * 32 + (size_t)in * 40960, vo2, lds0 + K2_TAIL + in * 1024); }
        }
    }
    STAMP(1, 11); RSTAMP(27);
    const int rep = (int)OFF_REPL + (bi & 7) * 256;
    if (w == 7) {
        if (bi < 8) {
            unsigned best = 0;
            for (unsigned spins = 0; spins < (1u << 16); ++spins) {
                bool ok = true; best = 0;
#pragma unroll
                for (int i = 0; i < 2; ++i) {
                    const u32x4 g = __builtin_amdgcn_raw_buffer_load_b128(wsr, lane * 16, (int)OFF_KEYG + 1024 * i, 16);
                    ok = ok && g[1] == GTAG && g[3] == GTAG;
                    best = max(best, max(g[0], g[2]));
                }
                if (__all(ok)) break;
                __builtin_amdgcn_s_sleep(1);
            }
            best = wave_umax(best);
            if (lane == 0) { __builtin_amdgcn_raw_buffer_store_b64((u32x2){best, GTAG}, wsr, 0, rep, 16); *(unsigned*)(smem + K2_MISC + 64) = best; }
        } else {
            u32x2 gk;
            for (unsigned spins = 0; spins < (1u << 16); ++spins) {
                gk = __builtin_amdgcn_raw_buffer_load_b64(wsr, 0, rep, 16);
                if (__builtin_amdgcn_readfirstlane(gk[1]) == GTAG) break;
                __builtin_amdgcn_s_sleep(1);
            }
            if (lane == 0) *(unsigned*)(smem + K2_MISC + 64) = gk[0];
        }
    }
    asm volatile("s_waitcnt vmcnt(0) lgkmcnt(0)\n\ts_barrier" ::: "memory");
    const unsigned wkey = __builtin_amdgcn_readfirstlane(*(const unsigned*)(smem + K2_MISC + 64));
    const int wtok = 2047 - (int)(wkey & 0xffffu);
    const int wro = (int)OFF_CROW + (wtok >> 3) * 5120;
    u32x4 xb[3];
    if (bi < 216) {
        for (unsigned spins = 0; spins < (1u << 16); ++spins) {
            bool ok = true;
#pragma unroll
            for (int i = 0; i < 3; ++i) {
                const int so = wro + min(w + 8 * i, 19) * 128;
                const u32x4 g0 = __builtin_amdgcn_raw_buffer_load_b128(wsr, lg * 32, so, 16);
                const u32x4 g1 = __builtin_amdgcn_raw_buffer_load_b128(wsr, lg * 32 + 16, so, 16);
                ok = ok && g0[1] == GTAG && g0[3] == GTAG && g1[1] == GTAG && g1[3] == GTAG;
                xb[i] = (u32x4){g0[0], g0[2], g1[0], g1[2]};
            }
            if (__all(ok)) break;
            __builtin_amdgcn_s_sleep(1);
        }
    }
    STAMP(1, 16); RSTAMP(26);
    if (bi < 216) {
        f32x4 acc = {0, 0, 0, 0};
        const unsigned ga = lds0 + K2_GU + (8 * lg + (l15 >> 2)) * 32 + (l15 & 3) * 8;
#pragma unroll
        for (int i = 0; i < 3; ++i) {
            if (w + 8 * i < 20) {
                const bf16x4 lo = trread(ga + (w + 8 * i) * 1024), hi = trread(ga + (w + 8 * i) * 1024 + 128);
                acc = MFMA16(__builtin_shufflevector(lo, hi, 0, 1, 2, 3, 4, 5, 6, 7), __builtin_bit_cast(bf16x8, xb[i]), acc);
            }
        }
        if (l15 == 0) *(f32x4*)(smem + K2_MISC + 128 + w * 64 + lg * 16) = acc;
    }
    if (cand && token != wtok) OUT_ROW();
    if (bi < 216) {
        BAR_LDS();
        if (w == 0) {
            float g = 0.f, u = 0.f;
#pragma unroll
            for (int v = 0; v < 8; ++v) { g += *(const float*)(smem + K2_MISC + 128 + v * 64 + (lane & 7) * 4); u += *(const float*)(smem + K2_MISC + 128 + v * 64 + 32 + (lane & 7) * 4); }
            g = rbf(g); u = rbf(u);
            const float sg = rbf(g * __builtin_amdgcn_rcpf(1.0f + __expf(-g)));
            const unsigned a = f2bf(sg * u);
            const unsigned an = (unsigned)__builtin_amdgcn_update_dpp(0, (int)a, 0xB1, 0xf, 0xf, true);
            if (lane < 8 && (lane & 1) == 0)
                __hip_atomic_store((GAS unsigned long long*)(ws + OFF_ACTG) + gcb * 4 + (lane >> 1), ((unsigned long long)GTAG << 32) | a | (an << 16), __ATOMIC_RELAXED, __HIP_MEMORY_SCOPE_AGENT);
        }
    }
    STAMP(1, 17); RSTAMP(25);
    if (bi >= 216) {
        bf16x8 af[7];
        const unsigned da = lds0 + K2_TAIL + (8 * lg + (l15 >> 2)) * 32 + (l15 & 3) * 8;
#pragma unroll
        for (int i = 0; i < 7; ++i) {
            const int ks = min(w + 8 * i, 53);
            const bf16x4 lo = trread(da + ks * 1024), hi = trread(da + ks * 1024 + 128);
            af[i] = __builtin_shufflevector(lo, hi, 0, 1, 2, 3, 4, 5, 6, 7);
        }
        unsigned hh = 0, hnn = 0;
        if (w == 1) {
            u32x4 g[7];
            for (unsigned spins = 0; spins < (1u << 16); ++spins) {
                bool ok = true;
#pragma unroll
                for (int i = 0; i < 7; ++i) {
                    g[i] = __builtin_amdgcn_raw_buffer_load_b128(wsr, min(lane * 16 + i * 1024, 6896), (int)OFF_ACTG, 16);
                    ok = ok && g[i][1] == GTAG && g[i][3] == GTAG;
                }
                if (__all(ok)) break;
                __builtin_amdgcn_s_sleep(1);
            }
#pragma unroll
            for (int i = 0; i < 7; ++i) *(u32x2*)(smem + K2_ACT + min(lane * 8 + i * 512, 3448)) = (u32x2){g[i][0], g[i][2]};
        }
        if (w == 0) {
            for (unsigned spins = 0; spins < (1u << 16); ++spins) {
                const u32x2 g0 = __builtin_amdgcn_raw_buffer_load_b64(wsr, (lane & 7) * 8, wro + dcb * 64, 16);
                const u32x2 g1 = __builtin_amdgcn_raw_buffer_load_b64(wsr, (lane & 7) * 8, wro + 2560 + dcb * 64, 16);
                hnn = g0[0]; hh = g1[0];
                if (__all(g0[1] == GTAG && g1[1] == GTAG)) break;
                __builtin_amdgcn_s_sleep(1);
            }
        }
        asm volatile("s_waitcnt vmcnt(0) lgkmcnt(0)\n\ts_barrier" ::: "memory");
        RSTAMP(23);
        f32x4 acc = {0, 0, 0, 0};
#pragma unroll
        for (int i = 0; i < 7; ++i) {
            if (w + 8 * i < 54) {
                const bf16x8 ab = *(const bf16x8*)(smem + K2_ACT + (w + 8 * i) * 64 + lg * 16);
                acc = MFMA16(af[i], ab, acc);
            }
        }
        if (l15 == 0) *(f32x4*)(smem + K2_MISC + 128 + w * 64 + lg * 16) = acc;
        BAR_LDS();
        if (w == 0 && lane < 8) {
            f32x2 y = {0.f, 0.f};
#pragma unroll
            for (int v = 0; v < 8; ++v) y += *(const f32x2*)(smem + K2_MISC + 128 + v * 64 + lane * 8);
            const unsigned ord = wkey >> 16;
            const float r = bf2f((ushort_t)((ord & 0x8000u) ? (ord & 0x7fffu) : (~ord & 0xffffu)));
            const float sg = rbf(__builtin_amdgcn_rcpf(1.0f + __expf(-r)));
            const f32x2 yy = unpk(cvtpk2(unpk(cvtpk2(y)) * sg));
            *(unsigned*)(out + (size_t)wtok * DM + dcb * 16 + lane * 2) = cvtpk2(unpk(hh) + unpk(cvtpk2(unpk(hnn) + yy)));
        }
    }
    asm volatile(".globl k2_code_end_mark\n\t.protected k2_code_end_mark\nk2_code_end_mark:");
    STAMP(1, 18); RSTAMP(31);
    STAMP_FLUSH(1);
#undef DIAG_LDS
}

extern "C" void kernel_launch(void* const* d_in, const int* in_sizes, int n_in,
                              void* d_out, int out_size, void* d_ws, size_t ws_size,
                              hipStream_t stream) {
    const ushort_t* x = (const ushort_t*)d_in[0];
    const ushort_t* wq = (const ushort_t*)d_in[1];
    const ushort_t* wk = (const ushort_t*)d_in[2];
    const ushort_t* wv = (const ushort_t*)d_in[3];
    const ushort_t* wo = (const ushort_t*)d_in[4];
    const ushort_t* w_r = (const ushort_t*)d_in[6];
    const ushort_t* w_gate = (const ushort_t*)d_in[7];
    const ushort_t* w_up = (const ushort_t*)d_in[8];
    const ushort_t* w_down = (const ushort_t*)d_in[9];
    char* ws = (char*)d_ws;
    ushort_t* out = (ushort_t*)d_out;
    (void)hipFuncSetAttribute((const void*)k1_qkv, hipFuncAttributeMaxDynamicSharedMemorySize, K1_LDS_BYTES);
    (void)hipFuncSetAttribute((const void*)k2_attn, hipFuncAttributeMaxDynamicSharedMemorySize, K2_LDS_BYTES);
    hipLaunchKernelGGL(k1_qkv, dim3(256), dim3(768), K1_LDS_BYTES, stream, x, wq, wk, wv, wo, w_gate, w_up, w_down, ws);
    hipLaunchKernelGGL(k2_attn, dim3(256), dim3(512), K2_LDS_BYTES, stream, x, w_r, w_gate, w_up, w_down, ws, out);
}
```
